```python
import torch

def _softmax(x, axis=-1):
    return torch.softmax(x, dim=axis)
def _gelu(x):
    return torch.nn.functional.gelu(x, approximate="tanh")
def _silu(x):
    return torch.nn.functional.silu(x)
def _sigmoid(x):
    return torch.sigmoid(x)
def _layernorm(x, axis=-1, eps=1e-5):
    m = x.mean(dim=axis, keepdim=True)
    v = x.var(dim=axis, keepdim=True, unbiased=False)
    return (x - m) / torch.sqrt(v + eps)
def _scatter_add(tgt, idx, src, axis=0):
    return tgt.clone().index_add_(axis, idx, src)
def _segment_sum(src, seg, n):
    out = torch.zeros((n,) + src.shape[1:], dtype=src.dtype)
    return out.index_add_(0, seg, src)
def _pad(x, pw):
    flat = tuple(v for pair in reversed(pw) for v in pair)
    return torch.nn.functional.pad(x, flat)

kernel_name = 'gated_mix__linear_attn_block__sliding_local_dilated__prompt'

def setup_inputs():

    _w = (lambda a: (lambda t: t.to(torch.bfloat16) if t.is_floating_point() else t)(torch.as_tensor(a)))
    def _A_setup():
        B, S, D = 1, 2048, 640
        return {
            "x": _w((lambda *s: torch.randn(*s, dtype=torch.bfloat16) * 0.05)(B, S, D)),
            "wqkv": _w((lambda *s: torch.randn(*s, dtype=torch.bfloat16) * 1.6)(D, 3 * D)),
            "wo": _w((lambda *s: torch.randn(*s, dtype=torch.bfloat16) * 0.8)(D, D)),
        }
    _A_in = _A_setup()

    def _B_setup():
        B, S, D = 1, 2048, 640
        return {
            "x": _w((lambda *s: torch.randn(*s, dtype=torch.bfloat16) * 0.05)(B, S, D)),
            "wqkv": _w((lambda *s: torch.randn(*s, dtype=torch.bfloat16) * 0.8)(D, 3 * D)),
            "wo": _w((lambda *s: torch.randn(*s, dtype=torch.bfloat16) * 0.05)(D, D)),
        }
    _B_in = _B_setup()

    return {
        "A_x": _A_in["x"],
        "A_wqkv": _A_in["wqkv"],
        "A_wo": _A_in["wo"],
        "B_x": _B_in["x"],
        "B_wqkv": _B_in["wqkv"],
        "B_wo": _B_in["wo"],
        "gate_w": _w((lambda *s: torch.randn(*s, dtype=torch.bfloat16) * 0.05)(640) * 16.0),
    }

def reference(A_x, A_wqkv, A_wo, B_x, B_wqkv, B_wo, gate_w):
    def _A_ref(x, wqkv, wo):
        B, S, D = x.shape
        H = 32; Dh = D // H
        qkv = (lambda x, *d: x.permute(*d))((x @ wqkv).reshape(B, S, 3, H, Dh), 2, 0, 3, 1, 4)
        q, k, v = qkv[0], qkv[1], qkv[2]
        phi = lambda t: torch.where(t > 0, t + 1, torch.exp(t)) + 1e-6
        pq, pk = phi(q), phi(k)
        kv = torch.einsum("bhsd,bhse->bhde", pk, v)
        z = torch.einsum("bhsd->bhd", pk)
        num = torch.einsum("bhqd,bhde->bhqe", pq, kv)
        den = torch.einsum("bhqd,bhd->bhq", pq, z)[..., None] + 1e-6
        o = num / den
        return (lambda x, *d: x.permute(*d))(o, 0, 2, 1, 3).reshape(B, S, D) @ wo

    def _B_ref(x, wqkv, wo):
        B, S, D = x.shape
        H = 32; Dh = D // H
        W, dil = 8, 2
        qkv = (lambda x, *d: x.permute(*d))((x @ wqkv).reshape(B, S, 3, H, Dh), 2, 0, 3, 1, 4)
        q, k, v = qkv[0], qkv[1], qkv[2]
        scale = Dh ** -0.5
        pos = torch.arange(S)
        off = torch.arange(W) * dil
        idx = pos[:, None] - off[None, :]
        mask = idx >= 0
        idx_c = torch.where(mask, idx, 0).reshape(-1)
        kg = k[:, :, idx_c, :].reshape(B, H, S, W, Dh)
        vg = v[:, :, idx_c, :].reshape(B, H, S, W, Dh)
        logits = torch.einsum("bhsd,bhswd->bhsw", q, kg) * scale
        logits = torch.where(mask[None, None, :, :], logits, -1e9)
        attn = _softmax(logits, axis=-1)
        o = torch.einsum("bhsw,bhswd->bhsd", attn, vg)
        return (lambda x, *d: x.permute(*d))(o, 0, 2, 1, 3).reshape(B, S, D) @ wo

    x = A_x
    g = _sigmoid((x * gate_w).sum(-1, keepdims=True))
    a = _A_ref(x, A_wqkv, A_wo)
    b = _B_ref(x, B_wqkv, B_wo)
    return g * a + (1 - g) * b
```

```cpp
#include <hip/hip_runtime.h>
#include <hip/hip_bf16.h>
#include <stdint.h>

typedef short bf16x8 __attribute__((ext_vector_type(8)));
typedef float f32x4 __attribute__((ext_vector_type(4)));
typedef unsigned short u16;

__device__ __forceinline__ float bf2f(u16 b) { return __builtin_bit_cast(float, (unsigned)b << 16); }
__device__ __forceinline__ u16 f2bf(float f) { __hip_bfloat16 h = __float2bfloat16(f); return __builtin_bit_cast(u16, h); }

constexpr int S = 2048, D = 640, H = 32, DH = 20;

__global__ __launch_bounds__(256) void gemm_simple(const u16* __restrict__ A, int lda, const u16* __restrict__ B, int ldb,
                                                   u16* __restrict__ C, int ldc, int K) {
  __shared__ u16 As[64][40];
  __shared__ u16 Bs[64][40];
  int bm = blockIdx.y * 64, bn = blockIdx.x * 64;
  int tid = threadIdx.x, lane = tid & 63, w = tid >> 6;
  int wr = w >> 1, wc = w & 1;
  f32x4 acc[2][2] = {};
  for (int k0 = 0; k0 < K; k0 += 32) {
    __syncthreads();
    {
      int r = tid >> 2, c = (tid & 3) * 8;
      *(bf16x8*)&As[r][c] = *(const bf16x8*)&A[(size_t)(bm + r) * lda + k0 + c];
      int kk = tid >> 3, n = (tid & 7) * 8;
      bf16x8 v = *(const bf16x8*)&B[(size_t)(k0 + kk) * ldb + bn + n];
      for (int j = 0; j < 8; ++j) Bs[n + j][kk] = (u16)v[j];
    }
    __syncthreads();
    bf16x8 a[2], b[2];
    for (int i = 0; i < 2; ++i) {
      a[i] = *(const bf16x8*)&As[wr * 32 + i * 16 + (lane & 15)][(lane >> 4) * 8];
      b[i] = *(const bf16x8*)&Bs[wc * 32 + i * 16 + (lane & 15)][(lane >> 4) * 8];
    }
    for (int i = 0; i < 2; ++i)
      for (int j = 0; j < 2; ++j) acc[i][j] = __builtin_amdgcn_mfma_f32_16x16x32_bf16(a[i], b[j], acc[i][j], 0, 0, 0);
  }
  for (int i = 0; i < 2; ++i)
    for (int j = 0; j < 2; ++j)
      for (int r = 0; r < 4; ++r)
        C[(size_t)(bm + wr * 32 + i * 16 + (lane >> 4) * 4 + r) * ldc + bn + wc * 32 + j * 16 + (lane & 15)] = f2bf(acc[i][j][r]);
}

__device__ __forceinline__ float phi(float t) { return (t > 0.f ? t + 1.f : __expf(t)) + 1e-6f; }

__global__ __launch_bounds__(448) void kv_kernel(const u16* __restrict__ qkvA, float* __restrict__ kv) {
  int h = blockIdx.x, t = threadIdx.x;
  if (t >= 420) return;
  int d = t / 21, e = t % 21;
  float acc = 0.f;
  for (int s = 0; s < S; ++s) {
    float pk = bf2f(f2bf(phi(bf2f(qkvA[(size_t)s * 1920 + 640 + h * 20 + d]))));
    float v = e < 20 ? bf2f(qkvA[(size_t)s * 1920 + 1280 + h * 20 + e]) : 1.f;
    acc += pk * v;
  }
  kv[h * 420 + t] = acc;
}

__global__ __launch_bounds__(64) void o_kernel(const u16* __restrict__ x, const u16* __restrict__ gw, const u16* __restrict__ qkvA,
                                               const u16* __restrict__ qkvB, const float* __restrict__ kv, u16* __restrict__ o) {
  int s = blockIdx.x, t = threadIdx.x;
  float gs = 0.f;
  for (int i = t; i < D; i += 64) gs += bf2f(x[(size_t)s * D + i]) * bf2f(gw[i]);
  for (int off = 32; off > 0; off >>= 1) gs += __shfl_xor(gs, off);
  float g = 1.f / (1.f + __expf(-gs));
  if (t < 32) {
    int h = t;
    float pq[20];
    float den = 0.f;
    for (int d = 0; d < 20; ++d) { pq[d] = phi(bf2f(qkvA[(size_t)s * 1920 + h * 20 + d])); den += pq[d] * kv[h * 420 + d * 21 + 20]; }
    den += 1e-6f;
    for (int e = 0; e < 20; ++e) {
      float n = 0.f;
      for (int d = 0; d < 20; ++d) n += pq[d] * kv[h * 420 + d * 21 + e];
      o[(size_t)s * 1280 + h * 20 + e] = f2bf(g * n / den);
    }
  } else {
    int h = t - 32;
    float lg[8];
    float m = -1e30f;
    for (int w = 0; w < 8; ++w) {
      int p = s - 2 * w;
      float a = 0.f;
      if (p >= 0) {
        for (int d = 0; d < 20; ++d) a += bf2f(qkvB[(size_t)s * 1920 + h * 20 + d]) * bf2f(qkvB[(size_t)p * 1920 + 640 + h * 20 + d]);
        a *= 0.22360679774997896f;
      } else a = -1e9f;
      lg[w] = a; m = fmaxf(m, a);
    }
    float sum = 0.f;
    for (int w = 0; w < 8; ++w) { lg[w] = __expf(lg[w] - m); sum += lg[w]; }
    float inv = (1.f - g) / sum;
    for (int e = 0; e < 20; ++e) {
      float a = 0.f;
      for (int w = 0; w < 8; ++w) { int p = s - 2 * w; if (p < 0) p = 0; a += lg[w] * bf2f(qkvB[(size_t)p * 1920 + 1280 + h * 20 + e]); }
      o[(size_t)s * 1280 + 640 + h * 20 + e] = f2bf(a * inv);
    }
  }
}

extern "C" void kernel_launch(void* const* d_in, const int* in_sizes, int n_in, void* d_out, int out_size, void* d_ws,
                              size_t ws_size, hipStream_t stream) {
  const u16* x = (const u16*)d_in[0];
  const u16* wA = (const u16*)d_in[1];
  const u16* woA = (const u16*)d_in[2];
  const u16* wB = (const u16*)d_in[4];
  const u16* woB = (const u16*)d_in[5];
  const u16* gw = (const u16*)d_in[6];
  char* ws = (char*)d_ws;
  u16* qkvA = (u16*)ws;
  u16* qkvB = (u16*)(ws + (size_t)S * 1920 * 2);
  u16* o = (u16*)(ws + (size_t)S * 1920 * 4);
  u16* wo = (u16*)(ws + (size_t)S * 1920 * 4 + (size_t)S * 1280 * 2);
  float* kv = (float*)(ws + (size_t)S * 1920 * 4 + (size_t)S * 1280 * 2 + 1280 * 640 * 2);
  hipMemcpyAsync(wo, woA, 640 * 640 * 2, hipMemcpyDeviceToDevice, stream);
  hipMemcpyAsync(wo + 640 * 640, woB, 640 * 640 * 2, hipMemcpyDeviceToDevice, stream);
  gemm_simple<<<dim3(30, 32), 256, 0, stream>>>(x, D, wA, 1920, qkvA, 1920, D);
  gemm_simple<<<dim3(30, 32), 256, 0, stream>>>(x, D, wB, 1920, qkvB, 1920, D);
  kv_kernel<<<32, 448, 0, stream>>>(qkvA, kv);
  o_kernel<<<S, 64, 0, stream>>>(x, gw, qkvA, qkvB, kv, o);
  gemm_simple<<<dim3(10, 32), 256, 0, stream>>>(o, 1280, wo, 640, (u16*)d_out, 640, 1280);
}
```

```cpp
#include <hip/hip_runtime.h>
#include <stdint.h>
#include <type_traits>

#define LAS __attribute__((address_space(3)))
#define GAS __attribute__((address_space(1)))
typedef short bf16x8 __attribute__((ext_vector_type(8)));
typedef short s16x4 __attribute__((ext_vector_type(4)));
typedef unsigned v4u __attribute__((ext_vector_type(4)));
typedef unsigned v2u __attribute__((ext_vector_type(2)));
typedef float f32x4 __attribute__((ext_vector_type(4)));
typedef float f32x2 __attribute__((ext_vector_type(2)));
typedef __bf16 bf2 __attribute__((ext_vector_type(2)));
typedef unsigned long long u64;

#define KROT 1

constexpr int UNIT = 5120, SLOT = 4 * UNIT;
constexpr int L_RING = 0;
constexpr int L_XST = 4 * SLOT;
constexpr int L_PACK = L_XST + 65536;
constexpr int L_AUX = L_PACK + 2 * UNIT;
constexpr int LDS_BYTES = 163840;
constexpr int GSTR = 5888;
constexpr int L_KL = L_XST, L_VL = L_XST + 4 * GSTR;
constexpr int L_SLOT4 = L_XST + 8 * GSTR;
constexpr int L_RED = L_XST;
constexpr int L_KVS = L_XST + 32768;
static_assert(L_SLOT4 + SLOT <= L_AUX && L_AUX + 4096 <= LDS_BYTES, "lds map");
constexpr int slot_base(int s) { return s == 14 ? L_SLOT4 : L_RING + (s & 3) * SLOT; }
constexpr int W_EPOCH = 0;
constexpr int W_FE = 131072, W_FM = 2 * 131072, W_FO = 3 * 131072;
constexpr int W_PART = 1 << 20, PART_STR = 6784;
constexpr int W_HALO = 2 << 20, HALO_STR = 5120;
constexpr int W_WOP = 3 << 20;
constexpr int W_OB = 5 << 20;
constexpr int W_BYTES = W_OB + 128 * 40 * 1024;
constexpr int SC1 = 16;
constexpr unsigned OOB = 0x40000000u;
constexpr unsigned SPIN_CAP = 1u << 16;

struct Params {
  const char *x, *wA, *woA, *wB, *woB, *gw;
  char *out, *ws;
};

template <int I, int N, class F> __device__ __forceinline__ void sfor(F&& f) {
  if constexpr (I < N) { f(std::integral_constant<int, I>{}); sfor<I + 1, N>(f); }
}
template <int I, int N, class F> __device__ __forceinline__ void sfor2(F&& f) {
  if constexpr (N - I <= 20) sfor<I, N>(f);
  else { sfor<I, I + 20>(f); sfor2<I + 20, N>(f); }
}
#define IC(n) std::integral_constant<int, (n)>{}
__device__ __forceinline__ unsigned pk2(float a, float b) {
  f32x2 t = {a, b};
  return __builtin_bit_cast(unsigned, __builtin_convertvector(t, bf2));
}
__device__ __forceinline__ float dot2(unsigned a, unsigned b, float c) {
  return __builtin_amdgcn_fdot2_f32_bf16(__builtin_bit_cast(bf2, a), __builtin_bit_cast(bf2, b), c, false);
}
#define MF "v_mfma_f32_16x16x32_bf16 "
__device__ __forceinline__ void mm_va(f32x4& d, v4u v, v4u a) { asm volatile(MF "%0, %1, %2, %0" : "+v"(d) : "v"(v), "a"(a)); }
__device__ __forceinline__ void mm_av(f32x4& d, v4u a, v4u v) { asm volatile(MF "%0, %1, %2, %0" : "+v"(d) : "a"(a), "v"(v)); }
__device__ __forceinline__ void mm_vv(f32x4& d, v4u a, v4u b) { asm volatile(MF "%0, %1, %2, %0" : "+v"(d) : "v"(a), "v"(b)); }
__device__ __forceinline__ void mm_va0(f32x4& d, v4u v, v4u a) { asm volatile(MF "%0, %1, %2, 0" : "=&v"(d) : "v"(v), "a"(a)); }
__device__ __forceinline__ void mm_av0(f32x4& d, v4u a, v4u v) { asm volatile(MF "%0, %1, %2, 0" : "=&v"(d) : "a"(a), "v"(v)); }
__device__ __forceinline__ void mm_vv0(f32x4& d, v4u a, v4u b) { asm volatile(MF "%0, %1, %2, 0" : "=&v"(d) : "v"(a), "v"(b)); }
__device__ __forceinline__ void mm_vac(f32x4& d, v4u v, v4u a, f32x4 c) { asm volatile(MF "%0, %1, %2, %3" : "=&v"(d) : "v"(v), "a"(a), "v"(c)); }
__device__ __forceinline__ void mm_avc(f32x4& d, v4u a, v4u v, f32x4 c) { asm volatile(MF "%0, %1, %2, %3" : "=&v"(d) : "a"(a), "v"(v), "v"(c)); }
__device__ __forceinline__ void mm_vvc(f32x4& d, v4u a, v4u b, f32x4 c) { asm volatile(MF "%0, %1, %2, %3" : "=&v"(d) : "v"(a), "v"(b), "v"(c)); }
#define NOPS(n) asm volatile("s_nop " #n)
#define PIN(x) asm volatile("" : "+v"(x))

template <int IMM> __device__ __forceinline__ void dma16(const char* sbase, unsigned voff, unsigned ldsbase) {
  asm volatile("s_add_u32 m0, %2, %3\n\ts_nop 0\n\tglobal_load_lds_dwordx4 %0, %1" ::"v"(voff), "s"(sbase), "s"(ldsbase), "i"(IMM) : "memory", "scc");
}
template <int N> __device__ __forceinline__ void wait_vm() { asm volatile("s_waitcnt vmcnt(%0)" ::"i"(N) : "memory"); }
__device__ __forceinline__ void wait_lgkm() { asm volatile("s_waitcnt lgkmcnt(0)" ::: "memory"); }
__device__ __forceinline__ v2u trread(LAS char* p) {
  return __builtin_bit_cast(v2u, __builtin_amdgcn_ds_read_tr16_b64_v4i16((LAS s16x4*)p));
}
#define SB0() __builtin_amdgcn_sched_barrier(0)
#define BAR() __builtin_amdgcn_s_barrier()
#define LD128(off) __builtin_amdgcn_raw_buffer_load_b128(rs, (off), 0, SC1)
#define LD128S(voff, soff) __builtin_amdgcn_raw_buffer_load_b128(rs, (voff), (soff), SC1)
#define ST128S(v, voff, soff) __builtin_amdgcn_raw_buffer_store_b128((v), rs, (voff) + (soff), 0, SC1)
#define ST128(v, off) __builtin_amdgcn_raw_buffer_store_b128((v), rs, (off), 0, SC1)
#define STAMP(i) do {} while (0)

__device__ __forceinline__ u64 ld_flag(char* ws, unsigned off) {
  return __hip_atomic_load((GAS u64*)(ws + off), __ATOMIC_RELAXED, __HIP_MEMORY_SCOPE_AGENT);
}
__device__ __forceinline__ void settle_flags(char* ws, unsigned off, u64 tag, u64 v) {
  if (__builtin_expect(!__all(v == tag), 0)) {
    for (unsigned spins = 0; spins < SPIN_CAP; ++spins) {
      if (__all(ld_flag(ws, off) == tag)) break;
      __builtin_amdgcn_s_sleep(1);
    }
  }
  __builtin_amdgcn_fence(__ATOMIC_ACQUIRE, "wavefront");
}
__device__ __forceinline__ void st_flag(__amdgpu_buffer_rsrc_t rs, unsigned off, int lane, u64 tag) {
  __builtin_amdgcn_raw_buffer_store_b64(v2u{(unsigned)tag, (unsigned)(tag >> 32)}, rs, lane == 0 ? off : OOB, 0, SC1);
}
template <class T> __device__ __forceinline__ T sel4(int i, T a, T b, T c, T d) {
  const bool lo = (i & 1) != 0, hi = (i & 2) != 0;
  const T x = lo ? b : a, y = lo ? d : c;
  return hi ? y : x;
}
constexpr float EPS = 1e-6f, LOG2E = 1.4426950408889634f;
__device__ __forceinline__ float phi_a(float t) { return __builtin_fmaf(t, LOG2E, -EPS * LOG2E); }
__device__ __forceinline__ float phi_b(float u) { return __builtin_amdgcn_fmed3f(__builtin_amdgcn_exp2f(u), 0.f, 1.f); }
__device__ __forceinline__ float phi_c(float t) { float r; asm("v_max_f32 %0, %1, %2" : "=v"(r) : "s"(EPS), "v"(t)); return r; }
__device__ __forceinline__ float phi(float t) { return phi_c(t) + phi_b(phi_a(t)); }

template <bool ISB> constexpr int fr_n(int f) { return f < 240 ? f / 5 : 48 + (f - 240) % 12; }
template <bool ISB> constexpr int fr_j(int f) { return f < 240 ? f % 5 : (ISB ? (f - 240) / 12 : ((f - 240) / 12 + 4) % 5); }

template <bool ISB>
__device__ __forceinline__ void phase1(const Params& p, LAS char* L, __amdgpu_buffer_rsrc_t rs, int b, int rt, int hg, int w, int lane, int rot, int c2,
                                       u64& tag, v4u (&wo)[50]) {
  const int r = lane & 15, g = lane >> 4;
  const char* W = ISB ? p.wB : p.wA;
  unsigned woff[5], xo[8];
#pragma unroll
  for (int i = 0; i < 8; ++i) {
    const int e = i & 1, tt = (i >> 1) & 1, hh = i >> 2;
    xo[i] = (unsigned)((16 * e + 4 * g + ((lane >> 3) & 1) + 2 * tt) * 1280 + hh * 128 + (((lane & 7) ^ (4 * e + g)) * 16));
  }
  const char* Wb = W + hg * 160 + (size_t)w * 32 * 3840;
  const char* xb = p.x + (size_t)(rt * 128 + w * 32) * 1280;
  const unsigned lw = (unsigned)(w * UNIT), lx = (unsigned)(w * 16384);
  auto kblk = [&](int pos) { int k = pos + rot; return k >= 5 ? k - 5 : k; };
  auto loadW1 = [&](auto S, auto I) {
    constexpr int s = S, i = I, ch = s / 5, seg = ch == 0 ? 1 : ch == 1 ? 2 : 0;
    dma16<slot_base(s) + i * 1024>(Wb + seg * 1280 + (size_t)kblk(s % 5) * (128 * 3840), woff[i], lw);
  };
  auto loadX1 = [&](auto S, auto I) {
    constexpr int s = S, i = I;
    dma16<L_XST + (s & 1) * 8192 + i * 1024>(xb + kblk(s) * 256, xo[i], lx);
  };
  {
    const char* ab = sel4(w, p.gw, p.gw + 1024, (const char*)p.out, (const char*)p.ws);
    const unsigned ao = (unsigned)((w == 1 ? r : lane) * sel4(w, 16, 16, 40960, 163840));
    asm volatile("s_mov_b32 m0, %2\n\ts_nop 0\n\tglobal_load_lds_dwordx4 %0, %1 sc1" ::"v"(ao), "s"(ab), "s"((unsigned)(L_AUX + w * 1024)) : "memory");
  }
  sfor<0, 8>([&](auto I) { loadX1(IC(0), I); });
#pragma unroll
  for (int i = 0; i < 5; ++i) {
    const int c = i * 64 + lane, rho = c / 10, cc = c % 10;
    woff[i] = (unsigned)((8 * ((rho >> 2) & 3) + 4 * (rho >> 4) + (rho & 3)) * 3840 + cc * 16);
  }
  sfor<0, 5>([&](auto I) { loadW1(IC(0), I); });
  sfor<0, 5>([&](auto I) { loadW1(IC(1), I); });
  sfor<0, 8>([&](auto I) { loadX1(IC(1), I); });

  LAS char* a1 = L + (lane >> 2) * 160 + (lane & 3) * 40;
  LAS char* a2 = L + (lane >> 2) * 160 + (lane & 3) * 8;
  LAS char* a1h = a1 + 61440;
  LAS char* a2h = a2 + 61440;
  LAS char* a1s = a1 + L_SLOT4;
  LAS char* a2s = a2 + L_SLOT4;
  LAS char* ax0 = L + L_XST + w * 16384 + r * 128 + ((g ^ (r >> 1)) * 16);
  LAS char* ax1 = L + L_XST + w * 16384 + r * 128 + (((4 + g) ^ (r >> 1)) * 16);
  LAS char* ag = L + L_AUX + g * 16;
  const int unit = b + 256 * w;

  v4u xf[20][2], wf[8], gf[4];
  f32x4 acc[3][2][5], gacc[2];
  f32x4 eps4 = {EPS, EPS, EPS, EPS};
  PIN(eps4);

  auto readW = [&](auto F, auto T) {
    constexpr int f = F, t = T, n = fr_n<ISB>(f), jt = fr_j<ISB>(f), s = n / 4, j = n % 4;
    constexpr bool use1 = ISB || jt == 4;
    constexpr int col = ISB ? 8 * jt : (jt == 4 ? 32 : 40 * jt);
    constexpr int off = j * UNIT + t * 2560 + col;
    v2u v;
    if constexpr (s == 14) v = trread((use1 ? a1s : a2s) + off);
    else if constexpr ((s & 3) == 3) v = trread((use1 ? a1h : a2h) + off);
    else v = trread((use1 ? a1 : a2) + (s & 3) * SLOT + off);
    if constexpr (t == 0) { wf[f & 7].x = v.x; wf[f & 7].y = v.y; }
    else { wf[f & 7].z = v.x; wf[f & 7].w = v.y; }
  };
  auto readX = [&](auto S, auto U) {
    constexpr int s = S, u = U, j = u / 3, k = u % 3;
    if constexpr (k < 2) xf[4 * s + j][k] = *(LAS v4u*)(((j & 1) ? ax1 : ax0) + (s & 1) * 8192 + (j >> 1) * 4096 + k * 2048);
    else gf[j] = *(LAS v4u*)(ag + kblk(s) * 256 + j * 64);
  };

  v4u Kf[5], Vf[5];
  v2u Pq[2][5];
  f32x4 kvT[4], kvU[4], kvW[4], kvX, kvZ, rd[8];
  float ta[2], tb[2], tc[2];
  unsigned kp[2][10], vp[20], ke[4][20], qp[2][2];
  float s0[8], s1[8];
  v4u hcp, hv[3];
  v2u ept;
  u64 poll1 = 0, poll2 = 0;
  const int e0 = 16 * w + 8 + r;
  LAS char* aK = L + L_KL + g * GSTR + e0 * 80;
  LAS char* aV = L + L_VL + g * GSTR + e0 * 80;
  const int cg = (ISB ? 8 : 0) + hg;
  unsigned poll1_off, poll2_off;
  {
    const int li = lane < 10 ? lane : 0;
    const int pu = c2 * 40 + (li / 5) * 20 + 5 * w + li % 5;
    poll1_off = W_FE + ((pu & 255) * 4 + (pu >> 8)) * 128;
    const int rp = rt > 0 ? rt - 1 : 0;
    const int bp = ((cg & 3) * 8 + (rp & 7)) * 8 + (cg >> 2) * 2 + (rp >> 3);
    if (ISB && g == 1) poll1_off = W_FE + (bp * 4 + (lane & 3)) * 128;
    poll2_off = ISB ? W_FM + ((cg * 16 + rp) * 4 + (lane & 3)) * 128 : W_FM + (cg * 64 + lane) * 128;
  }
  auto wo_off = [&](int i) { return W_WOP + ((c2 * 40 + (i < 25 ? 20 : 0) + 5 * w) * 5 + (i < 25 ? i : i - 25)) * 1024; };
  hv[0] = hv[1] = hv[2] = v4u{0u, 0u, 0u, 0u};
  const unsigned halo_rd = (unsigned)(W_HALO + (hg * 16 + rt - 1) * HALO_STR + lane * 16);
  unsigned hl[3];
#pragma unroll
  for (int i = 0; i < 3; ++i) { int q = i * 64 + lane; q = q < 160 ? q : 159; hl[i] = (unsigned)((q / 40) * GSTR + (q % 40) * 16); }

  wait_vm<13>();
  BAR();
  STAMP(1);
  sfor<0, 3>([&](auto U) { readX(IC(0), U); });
  sfor<0, 5>([&](auto F) { readW(F, IC(0)); readW(F, IC(1)); });
  sfor<3, 12>([&](auto U) { readX(IC(0), U); });
  SB0();

  constexpr int NWO1 = ISB ? 39 : 24;
  sfor2<0, 600>([&](auto M) {
    constexpr int m = M, f = m / 2, tt = m & 1, n = fr_n<ISB>(f), jt = fr_j<ISB>(f), ch = n / 20, ks = n % 20;
    if constexpr (m % 40 == 30 && m <= 470) {
      constexpr int s = m / 40;
      constexpr int cnt = s <= 2 ? 13 : s <= 9 ? 5 : s == 10 ? 10 : (ISB ? 8 : 9);
      if constexpr (s == 10) asm volatile("s_waitcnt vmcnt(10)" : "+v"(poll1)::"memory");
      else wait_vm<cnt>();
      BAR();
      if constexpr (s == 8) st_flag(rs, W_FE + (b * 4 + w) * 128, lane, tag);
      if constexpr (s == 9) asm volatile("global_load_dwordx2 %0, %1, %2 sc1" : "+v"(poll1) : "v"(poll1_off), "s"(p.ws) : "memory");
      SB0();
    }
    if constexpr (!ISB && m == 440) { wait_lgkm(); BAR(); SB0(); }
    if constexpr (f + 5 < 300) readW(IC(f + 5 < 300 ? f + 5 : 0), IC(tt));
    if constexpr (m >= 30 && m < 170 && (m - 30) % 40 < 12) readX(IC((m - 30) / 40 + 1), IC((m - 30) % 40));
    {
      f32x4& d = acc[ch][tt][jt];
      if constexpr (ISB) { if constexpr (ks == 0) mm_va0(d, wf[f & 7], xf[ks][tt]); else mm_va(d, wf[f & 7], xf[ks][tt]); }
      else if constexpr (ch == 2) { if constexpr (ks == 0) mm_vac(d, wf[f & 7], xf[ks][tt], eps4); else mm_va(d, wf[f & 7], xf[ks][tt]); }
      else if constexpr (ks != 0) mm_av(d, xf[ks][tt], wf[f & 7]);
      else if constexpr (ch == 0) mm_avc(d, xf[ks][tt], wf[f & 7], eps4);
      else mm_av0(d, xf[ks][tt], wf[f & 7]);
    }
    if constexpr (m < 200 && m % 10 == 9) {
      if constexpr (n == 0) { mm_va0(gacc[0], gf[0], xf[0][0]); mm_va0(gacc[1], gf[0], xf[0][1]); }
      else { mm_va(gacc[0], gf[n & 3], xf[n][0]); mm_va(gacc[1], gf[n & 3], xf[n][1]); }
    }
    if constexpr (m >= 2 && m <= 10 && m % 2 == 0) loadW1(IC(2), IC((m - 2) / 2));
    if constexpr (m == 12) {
      if (unit < 320) {
        const int pf = unit % 40, pc = unit / 40, pbr = pf / 20, pP = (pf % 20) / 5, pi = pf % 5;
        const int ii = pi >= 3 ? pi - 3 : pi, hb = 160 * pP + (pi >= 3 ? 80 : 0);
        const int cgx = pbr ? 20 : 4, cg_ = pi == 2 ? 40 : cgx;
        const int ct = (pi != 2 && pbr) ? 4 : 20;
        const int c0x = hb + (pbr ? 8 : 40) * ii, c0 = pi == 2 ? 160 * pP + 16 : c0x;
        const char* pb = (pbr ? p.woB : p.woA) + pc * 160 + c0 * 1280;
        sfor<0, 5>([&](auto I) {
          const int c = I * 64 + lane, rho = c / 10, cc = c % 10;
          dma16<L_PACK + I * 1024>(pb, (unsigned)((cg_ * ((rho >> 2) & 3) + ct * (rho >> 4) + (rho & 3)) * 1280 + cc * 16), lw);
        });
      }
    }
    if constexpr (m < 120 && m % 40 >= 14 && m % 40 <= 28 && m % 2 == 0) loadX1(IC(m / 40 + 2), IC((m % 40 - 14) / 2));
    if constexpr (m >= 32 && m <= 80 && (m - 32) % 40 <= 8 && m % 2 == 0) loadW1(IC((m - 32) / 40 + 3), IC(((m - 32) % 40) / 2));
    if constexpr (m >= 112 && m < 430 && (m - 32) % 8 == 0) loadW1(IC((m - 32) / 40 + 3), IC(((m - 32) % 40) / 8));
    if constexpr (m >= 396 && m < 430 && (m - 396) % 8 == 0) loadW1(IC(14), IC((m - 396) / 8));
    if constexpr (m >= 431 && m <= 439 && m % 2 == 1) loadW1(IC(13), IC((m - 431) / 2));
    if constexpr (m == 206) { PIN(gacc[0]); PIN(gacc[1]); }
    if constexpr (m == 44) ept = *(LAS v2u*)(L + L_AUX + 3072);
    if constexpr (m == 56) tag = (((u64)(unsigned)__builtin_amdgcn_readfirstlane(ept.y) << 32) | (unsigned)__builtin_amdgcn_readfirstlane(ept.x)) + 1ull;
    if constexpr (m == 84) {
      if (unit < 320) {
        v4u pv[5];
        sfor<0, 5>([&](auto J) {
          const v2u lo = trread(a1 + L_PACK + w * UNIT + 8 * J), hi = trread(a1 + L_PACK + w * UNIT + 2560 + 8 * J);
          pv[J] = v4u{lo.x, lo.y, hi.x, hi.y};
        });
        sfor<0, 5>([&](auto J) { ST128S(pv[J], lane * 16, W_WOP + (unit * 5 + J) * 1024); });
      }
    }
    if constexpr (m == 441) settle_flags(p.ws, poll1_off, tag, poll1);
    if constexpr (m >= 444 && m % 4 == 0 && (m - 444) / 4 < NWO1) wo[(m - 444) / 4] = LD128S(lane * 16, wo_off((m - 444) / 4));
    if constexpr (!ISB) {
      auto phi_op = [&](auto Z, const f32x4& t, auto H, unsigned& out) {
        constexpr int z = Z, h = H;
        if constexpr (z < 2) { ta[z] = phi_a(t[2 * h + z]); PIN(ta[z]); }
        else if constexpr (z < 4) { tb[z - 2] = phi_b(ta[z - 2]); PIN(tb[z - 2]); }
        else if constexpr (z < 6) { tc[z - 4] = phi_c(t[2 * h + z - 4]); PIN(tc[z - 4]); }
        else if constexpr (z < 8) { tc[z - 6] += tb[z - 6]; PIN(tc[z - 6]); }
        else { out = pk2(tc[0], tc[1]); PIN(out); }
      };
      if constexpr (m >= 200 && m < 400 && m % 10 < 9) {
        constexpr int pr = (m - 200) / 10, kt = pr >> 2, t2 = (pr >> 1) & 1, h = pr & 1;
        unsigned o = 0;
        phi_op(IC(m % 10), acc[0][t2][kt], IC(h), o);
        if constexpr (m % 10 == 8) Kf[kt][2 * t2 + h] = o;
      }
      if constexpr (m >= 400 && m < 410) {
        constexpr int vt = (m - 400) >> 1;
        unsigned lo = pk2(acc[1][tt][vt][0], acc[1][tt][vt][1]), hi = pk2(acc[1][tt][vt][2], acc[1][tt][vt][3]);
        PIN(lo); PIN(hi);
        Vf[vt][2 * tt] = lo; Vf[vt][2 * tt + 1] = hi;
      }
      if constexpr (m >= 411 && m < 415) mm_vv0(kvU[m - 411], Kf[4], Vf[m - 411]);
      if constexpr (m >= 415 && m < 419) mm_vv0(kvW[m - 415], Kf[m - 415], Vf[4]);
      if constexpr (m >= 419 && m < 423) mm_vv0(kvT[m - 419], Kf[m - 419], Vf[m - 419]);
      if constexpr (m == 423) mm_vv0(kvX, Kf[4], Vf[4]);
      if constexpr (m >= 424 && m < 429) {
        const unsigned one = (r == m - 424) ? 0x3F803F80u : 0u;
        v4u ones = {one, one, one, one};
        PIN(ones); NOPS(1);
        if constexpr (m == 424) mm_vv0(kvZ, Kf[0], ones); else mm_vv(kvZ, Kf[m - 424], ones);
      }
      if constexpr (m >= 419 && m < 423) {
        constexpr int q = m - 419;
        float u = sel4(g, kvU[0][q], kvU[1][q], kvU[2][q], kvU[3][q]);
        PIN(u); kvU[0][q] = u;
      }
      if constexpr (m >= 429 && m < 433) {
        constexpr int q = m - 429;
        float u = sel4(r >> 2, kvW[0][q], kvW[1][q], kvW[2][q], kvW[3][q]);
        PIN(u); kvW[0][q] = u;
      }
      {
        LAS f32x4* red = (LAS f32x4*)(L + L_RED + w * 8192 + lane * 16);
        if constexpr (m >= 424 && m < 428) red[(m - 424) * 64] = kvT[m - 424];
        if constexpr (m == 428) red[4 * 64] = kvU[0];
        if constexpr (m == 429) red[6 * 64] = kvX;
        if constexpr (m == 433) red[5 * 64] = kvW[0];
        if constexpr (m == 434) red[7 * 64] = kvZ;
      }
      if constexpr (m >= 440 && m < 448) rd[m - 440] = *(LAS f32x4*)(L + L_RED + w * 2048 + lane * 16 + ((m - 440) >> 1) * 8192 + ((m - 440) & 1) * 1024);
      if constexpr (m >= 450 && m < 456) { constexpr int q = m - 450; rd[q & 1] += rd[2 + q]; PIN(rd[q & 1]); }
      if constexpr (m == 457 || m == 458) {
        const int pb = W_PART + (hg * 16 + rt) * PART_STR;
        unsigned o = pb + (2 * w + (m - 457)) * 1024 + lane * 16;
        const unsigned o3 = m == 457 ? ((g == (r >> 2)) ? pb + (384 + r) * 16 : OOB) : ((r <= 4) ? pb + (400 + 4 * r + g) * 16 : OOB);
        o = w == 3 ? o3 : o;
        ST128(__builtin_bit_cast(v4u, rd[m - 457]), o);
      }
      if constexpr (m == 514) { wait_vm<14>(); st_flag(rs, W_FM + ((cg * 16 + rt) * 4 + w) * 128, lane, tag); }
      if constexpr (m == 560) poll2 = ld_flag(p.ws, poll2_off);
      if constexpr (m >= 508) {
        constexpr int t = (m - 480) / 24 - 1, u = (m - 480) % 24 - 4, qt = (t + 4) % 5;
        if constexpr (u >= 0 && u < 18) {
          sfor<0, 2>([&](auto E) {
            constexpr int z = 2 * u + E, pr = z / 9, t2 = pr >> 1, h = pr & 1;
            unsigned o = 0;
            phi_op(IC(z % 9), acc[2][t2][qt], IC(h), o);
            if constexpr (z % 9 == 8) Pq[t2][qt][h] = o;
          });
        }
      }
    } else {
      if constexpr (m >= 200 && m < 220) {
        constexpr int q = m - 200, kt = q >> 2, t2 = (q >> 1) & 1, h = q & 1;
        kp[t2][2 * kt + h] = pk2(acc[0][t2][kt][2 * h], acc[0][t2][kt][2 * h + 1]);
        PIN(kp[t2][2 * kt + h]);
      }
      if constexpr (m >= 220 && m < 230 && m % 2 == 0) {
        constexpr int c = (m - 220) / 2;
        auto kk = [&](int i) { return i < 10 ? kp[0][i] : kp[1][i - 10]; };
        *(LAS v4u*)(aK + c * 16) = v4u{kk(4 * c), kk(4 * c + 1), kk(4 * c + 2), kk(4 * c + 3)};
      }
      if constexpr (m == 273) hcp = *(LAS v4u*)(L + L_KL + w * GSTR + 64 * 80 + (lane < 40 ? lane : 0) * 16);
      if constexpr (m == 285) ST128(hcp, lane < 40 ? W_HALO + (hg * 16 + rt) * HALO_STR + w * 640 + lane * 16 : OOB);
      if constexpr (m >= 400 && m < 410) {
        constexpr int vt = (m - 400) >> 1;
        vp[4 * vt + 2 * tt] = pk2(acc[1][0][vt][2 * tt], acc[1][1][vt][2 * tt]);
        vp[4 * vt + 2 * tt + 1] = pk2(acc[1][0][vt][2 * tt + 1], acc[1][1][vt][2 * tt + 1]);
        PIN(vp[4 * vt + 2 * tt]); PIN(vp[4 * vt + 2 * tt + 1]);
      }
      if constexpr (m >= 403 && m <= 411 && m % 2 == 1) {
        constexpr int c = (m - 403) / 2;
        *(LAS v4u*)(aV + c * 16) = v4u{vp[4 * c], vp[4 * c + 1], vp[4 * c + 2], vp[4 * c + 3]};
      }
      if constexpr (m == 430) hcp = *(LAS v4u*)(L + L_VL + w * GSTR + 64 * 80 + (lane < 40 ? lane : 0) * 16);
      if constexpr (m == 441) {
        if (w == 0 && rt > 0) {
#pragma unroll
          for (int i = 0; i < 3; ++i) hv[i] = LD128(i < 2 || lane < 32 ? halo_rd + i * 1024 : halo_rd + 159 * 16 - lane * 16);
        }
      }
      if constexpr (m == 442) ST128(hcp, lane < 40 ? W_HALO + (hg * 16 + rt) * HALO_STR + 2560 + w * 640 + lane * 16 : OOB);
      if constexpr (m == 481) {
        if (w == 0) {
#pragma unroll
          for (int i = 0; i < 3; ++i) *(LAS v4u*)(L + L_KL + hl[i]) = hv[i];
        }
      }
      if constexpr (m >= 484 && m < 502) {
        constexpr int q = m - 484, i = q / 5, c = i == 3 ? q - 15 + 2 : q % 5;
        const v4u v = *(LAS v4u*)(aK - (i + 1) * 160 + c * 16);
        ke[i][4 * c] = v.x; ke[i][4 * c + 1] = v.y; ke[i][4 * c + 2] = v.z; ke[i][4 * c + 3] = v.w;
      }
      if constexpr (m == 498) { wait_vm<14>(); st_flag(rs, W_FM + ((cg * 16 + rt) * 4 + w) * 128, lane, tag); }
      if constexpr (m == 545) poll2 = ld_flag(p.ws, poll2_off);
      if constexpr (m == 577) {
        if (w == 0 && rt > 0) {
          settle_flags(p.ws, poll2_off, tag, poll2);
#pragma unroll
          for (int i = 0; i < 3; ++i) hv[i] = LD128(i < 2 || lane < 32 ? halo_rd + 2560 + i * 1024 : halo_rd + 2560 + 159 * 16 - lane * 16);
        }
      }
    }
    if constexpr (ISB && m >= 508) {
      constexpr int t = (m - 480) / 24 - 1, u = (m - 480) % 24 - 4;
      if constexpr (u >= 0 && u < 2) {
        qp[u][0] = pk2(acc[2][u][t][0], acc[2][u][t][1]); qp[u][1] = pk2(acc[2][u][t][2], acc[2][u][t][3]);
        PIN(qp[u][0]); PIN(qp[u][1]);
      }
      if constexpr (u >= 2 && u < 18) {
        constexpr int z = u - 2, qi = z & 1, wi = z >> 1;
        constexpr int kr = wi + 1 - qi;
        float& sv = qi ? s1[wi] : s0[wi];
        auto key = [&](int d) { return kr == 0 ? kp[1][d] : kr == 1 ? kp[0][d] : ke[(kr >= 2 ? kr - 2 : 0) / 2][(kr & 1) ? d : 10 + d]; };
        sv = dot2(qp[qi][0], key(2 * t), t == 0 ? 0.f : sv);
        sv = dot2(qp[qi][1], key(2 * t + 1), sv);
        PIN(sv);
      }
    }
    SB0();
    if constexpr (m == 199) STAMP(2);
    if constexpr (m == 399) STAMP(3);
  });
  STAMP(4);

  float gate[2];
#pragma unroll
  for (int tt = 0; tt < 2; ++tt) {
    const float e = __builtin_amdgcn_exp2f(gacc[tt][0] * (ISB ? LOG2E : -LOG2E));
    gate[tt] = __builtin_amdgcn_rcpf(1.f + e);
  }
  v4u F0[2], F1[2];
  v2u F2[2];
  if constexpr (!ISB) {
    settle_flags(p.ws, poll2_off, tag, poll2);
    const int t = threadIdx.x;
    const int o0 = t * 16, o1 = (t + 256 < 420 ? t + 256 : 419) * 16;
    v4u l0[16], l1[16];
#pragma unroll
    for (int i = 0; i < 16; ++i) l0[i] = LD128S(o0, W_PART + (hg * 16 + i) * PART_STR);
#pragma unroll
    for (int i = 0; i < 16; ++i) l1[i] = LD128S(o1, W_PART + (hg * 16 + i) * PART_STR);
    SB0();
#pragma unroll
    for (int tt = 0; tt < 2; ++tt) {
      const f32x4 q = acc[2][tt][3];
      Pq[tt][3] = v2u{pk2(phi(q[0]), phi(q[1])), pk2(phi(q[2]), phi(q[3]))};
    }
    SB0();
    f32x4 a0 = __builtin_bit_cast(f32x4, l0[0]), a1_ = __builtin_bit_cast(f32x4, l1[0]);
#pragma unroll
    for (int i = 1; i < 16; ++i) a0 += __builtin_bit_cast(f32x4, l0[i]);
#pragma unroll
    for (int i = 1; i < 16; ++i) a1_ += __builtin_bit_cast(f32x4, l1[i]);
    LAS v2u* kvs = (LAS v2u*)(L + L_KVS);
    kvs[t] = v2u{pk2(a0[0], a0[1]), pk2(a0[2], a0[3])};
    kvs[t + 256 < 420 ? t + 256 : 419] = v2u{pk2(a1_[0], a1_[1]), pk2(a1_[2], a1_[3])};
    SB0();
#pragma unroll
    for (int i = NWO1; i < 50; ++i) wo[i] = LD128S(lane * 16, wo_off(i));
    wait_lgkm();
    BAR();
    STAMP(5);
    const v2u uu = kvs[256 + lane], ww = kvs[320 + lane], xx = kvs[384 + r], z4 = kvs[416 + g];
    v4u Nh[4], N4[4], Zh[4], Pf[2][4];
    v2u tl4[4], zl4[4];
#pragma unroll
    for (int h = 0; h < 4; ++h) { tl4[h] = kvs[h * 64 + lane]; zl4[h] = kvs[400 + 4 * h + g]; }
    SB0();
#pragma unroll
    for (int h = 0; h < 4; ++h) {
      const v2u tl = tl4[h], zl = zl4[h];
      const bool gh = g == h, rh = (r >> 2) == h;
      Nh[h] = v4u{tl.x, tl.y, gh ? uu.x : 0u, gh ? uu.y : 0u};
      N4[h] = v4u{rh ? ww.x : 0u, rh ? ww.y : 0u, (rh && gh) ? xx.x : 0u, (rh && gh) ? xx.y : 0u};
      Zh[h] = v4u{zl.x, zl.y, gh ? z4.x : 0u, gh ? z4.y : 0u};
      PIN(Nh[h]); PIN(N4[h]); PIN(Zh[h]);
#pragma unroll
      for (int tt = 0; tt < 2; ++tt) { Pf[tt][h] = v4u{Pq[tt][h].x, Pq[tt][h].y, Pq[tt][4].x, Pq[tt][4].y}; PIN(Pf[tt][h]); }
    }
    NOPS(1);
    SB0();
    f32x4 num[2][5], den[2][4];
#pragma unroll
    for (int h = 0; h < 4; ++h)
#pragma unroll
      for (int tt = 0; tt < 2; ++tt) mm_vvc(den[tt][h], Zh[h], Pf[tt][h], eps4);
#pragma unroll
    for (int h = 0; h < 4; ++h)
#pragma unroll
      for (int tt = 0; tt < 2; ++tt) mm_vv0(num[tt][h], Nh[h], Pf[tt][h]);
    SB0();
    float inv[2][4];
#pragma unroll
    for (int h = 0; h < 4; ++h)
#pragma unroll
      for (int tt = 0; tt < 2; ++tt) {
        if (h == 0) mm_vv0(num[tt][4], N4[h], Pf[tt][h]); else mm_vv(num[tt][4], N4[h], Pf[tt][h]);
        inv[tt][h] = gate[tt] * __builtin_amdgcn_rcpf(den[tt][h][0]);
        PIN(inv[tt][h]);
        SB0();
      }
#pragma unroll
    for (int tt = 0; tt < 2; ++tt) {
      const f32x4 o0_ = num[tt][0] * inv[tt][0], o1_ = num[tt][1] * inv[tt][1], o2_ = num[tt][2] * inv[tt][2], o3_ = num[tt][3] * inv[tt][3];
      F0[tt] = v4u{pk2(o0_[0], o0_[1]), pk2(o0_[2], o0_[3]), pk2(o1_[0], o1_[1]), pk2(o1_[2], o1_[3])};
      F1[tt] = v4u{pk2(o2_[0], o2_[1]), pk2(o2_[2], o2_[3]), pk2(o3_[0], o3_[1]), pk2(o3_[2], o3_[3])};
      PIN(F0[tt]); PIN(F1[tt]);
    }
    SB0();
#pragma unroll
    for (int tt = 0; tt < 2; ++tt) {
      const float i4 = sel4(g, inv[tt][0], inv[tt][1], inv[tt][2], inv[tt][3]);
      const f32x4 o4_ = num[tt][4] * i4;
      F2[tt] = v2u{pk2(o4_[0], o4_[1]), pk2(o4_[2], o4_[3])};
    }
  } else {
#pragma unroll
    for (int i = NWO1; i < 50; ++i) wo[i] = LD128S(lane * 16, wo_off(i));
    NOPS(7);
    SB0();
    {
      constexpr int t = 4;
#pragma unroll
      for (int u = 0; u < 2; ++u) { qp[u][0] = pk2(acc[2][u][t][0], acc[2][u][t][1]); qp[u][1] = pk2(acc[2][u][t][2], acc[2][u][t][3]); }
      sfor<0, 16>([&](auto Z) {
        constexpr int z = Z, qi = z & 1, wi = z >> 1, kr = wi + 1 - qi;
        float& sv = qi ? s1[wi] : s0[wi];
        auto key = [&](int d) { return kr == 0 ? kp[1][d] : kr == 1 ? kp[0][d] : ke[(kr >= 2 ? kr - 2 : 0) / 2][(kr & 1) ? d : 10 + d]; };
        sv = dot2(qp[qi][0], key(2 * t), sv);
        sv = dot2(qp[qi][1], key(2 * t + 1), sv);
      });
    }
    constexpr float SCALE = 0.22360679774997896f;
    if (w == 0) {
#pragma unroll
      for (int i = 0; i < 3; ++i) *(LAS v4u*)(L + L_VL + hl[i]) = hv[i];
      if (rt == 0) {
        const int u0 = 4 * (r >> 1) + (r & 1);
#pragma unroll
        for (int i = 1; i < 8; ++i) {
          if (u0 - 2 * i < 0) s0[i] = -1e9f / SCALE;
          if (u0 + 2 - 2 * i < 0) s1[i] = -1e9f / SCALE;
        }
      }
    }
    STAMP(5);
    v4u ve[4][5];
#pragma unroll
    for (int i = 0; i < 4; ++i)
#pragma unroll
      for (int c = 0; c < 5; ++c) ve[i][c] = *(LAS v4u*)(aV - (i + 1) * 160 + c * 16);
    SB0();
    float m0 = s0[0], m1 = s1[0];
#pragma unroll
    for (int i = 1; i < 8; ++i) { m0 = __builtin_fmaxf(m0, s0[i]); m1 = __builtin_fmaxf(m1, s1[i]); }
    constexpr float C = SCALE * LOG2E;
    float sum0 = 0.f, sum1 = 0.f;
#pragma unroll
    for (int i = 0; i < 8; ++i) {
      s0[i] = __builtin_amdgcn_exp2f((s0[i] - m0) * C); sum0 += s0[i];
      s1[i] = __builtin_amdgcn_exp2f((s1[i] - m1) * C); sum1 += s1[i];
    }
    const float i0 = gate[0] * __builtin_amdgcn_rcpf(sum0), i1 = gate[1] * __builtin_amdgcn_rcpf(sum1);
#pragma unroll
    for (int i = 0; i < 8; ++i) { s0[i] *= i0; s1[i] *= i1; }
    unsigned pw0[5], pw1[4];
    pw0[0] = pk2(s0[0], 0.f);
#pragma unroll
    for (int i = 1; i < 4; ++i) pw0[i] = pk2(s0[2 * i], s0[2 * i - 1]);
    pw0[4] = pk2(0.f, s0[7]);
#pragma unroll
    for (int i = 0; i < 4; ++i) pw1[i] = pk2(s1[2 * i + 1], s1[2 * i]);
    float o0[20], o1[20];
#pragma unroll
    for (int d = 0; d < 20; ++d) { o0[d] = dot2(vp[d], pw0[0], 0.f); o1[d] = dot2(vp[d], pw1[0], 0.f); }
#pragma unroll
    for (int i = 1; i <= 4; ++i)
#pragma unroll
      for (int d = 0; d < 20; ++d) {
        o0[d] = dot2(ve[i - 1][d >> 2][d & 3], pw0[i], o0[d]);
        if (i < 4) o1[d] = dot2(ve[i - 1][d >> 2][d & 3], pw1[i], o1[d]);
      }
    F0[0] = v4u{pk2(o0[0], o0[1]), pk2(o0[2], o0[3]), pk2(o0[4], o0[5]), pk2(o0[6], o0[7])};
    F1[0] = v4u{pk2(o0[8], o0[9]), pk2(o0[10], o0[11]), pk2(o0[12], o0[13]), pk2(o0[14], o0[15])};
    F2[0] = v2u{pk2(o0[16], o0[17]), pk2(o0[18], o0[19])};
    F0[1] = v4u{pk2(o1[0], o1[1]), pk2(o1[2], o1[3]), pk2(o1[4], o1[5]), pk2(o1[6], o1[7])};
    F1[1] = v4u{pk2(o1[8], o1[9]), pk2(o1[10], o1[11]), pk2(o1[12], o1[13]), pk2(o1[14], o1[15])};
    F2[1] = v2u{pk2(o1[16], o1[17]), pk2(o1[18], o1[19])};
  }
  {
    const int fb = (ISB ? 20 : 0) + (hg >> 1) * 5, f0 = fb + ((hg & 1) ? 3 : 0);
    const int TT = rt * 8 + 2 * w;
#pragma unroll
    for (int tt = 0; tt < 2; ++tt) {
      ST128S(F0[tt], lane * 16, W_OB + ((TT + tt) * 40 + f0) * 1024);
      ST128S(F1[tt], lane * 16, W_OB + ((TT + tt) * 40 + f0 + 1) * 1024);
    }
    const auto sx = __builtin_amdgcn_permlane16_swap(F2[0].x, F2[1].x, false, false);
    const auto sy = __builtin_amdgcn_permlane16_swap(F2[0].y, F2[1].y, false, false);
    ST128((v4u{sx[0], sy[0], sx[1], sy[1]}), W_OB + ((TT + (g & 1)) * 40 + fb + 2) * 1024 + ((hg & 1) * 2 + (g >> 1)) * 256 + r * 16);
  }
  STAMP(6);
  __builtin_amdgcn_s_waitcnt(0x0F70);
  wait_vm<0>();
  st_flag(rs, W_FO + ((rt * 16 + cg) * 4 + w) * 128, lane, tag);
  STAMP(7);
}

__device__ __forceinline__ void phase2(const Params& p, LAS char* L, __amdgpu_buffer_rsrc_t rs, int b, int w, int lane, int jt, int c, u64 tag,
                                       v4u (&wo)[50]) {
  const int r = lane & 15, g = lane >> 4;
  v4u of[2][5][4];
  f32x4 acc[4][5];
#pragma unroll
  for (int hb = 0; hb < 2; ++hb) {
    const int br = 1 - hb;
    const unsigned fo = W_FO + (((jt >> 1) * 16 + br * 8 + 2 * w + (lane & 1)) * 4 + 2 * (jt & 1) + ((lane >> 1) & 1)) * 128;
    settle_flags(p.ws, fo, tag, ld_flag(p.ws, fo));
    if (hb == 0) STAMP(8); else STAMP(9);
#pragma unroll
    for (int i = 0; i < 5; ++i)
#pragma unroll
      for (int a = 0; a < 4; ++a) of[hb][i][a] = LD128S(lane * 16, W_OB + ((4 * jt + a) * 40 + br * 20 + 5 * w + i) * 1024);
    SB0();
  }
#pragma unroll
  for (int hb = 0; hb < 2; ++hb)
#pragma unroll
    for (int i = 0; i < 5; ++i)
#pragma unroll
      for (int a = 0; a < 4; ++a)
#pragma unroll
        for (int n = 0; n < 5; ++n) {
          if (hb == 0 && i == 0) mm_av0(acc[a][n], wo[n], of[0][0][a]);
          else mm_av(acc[a][n], wo[hb * 25 + i * 5 + n], of[hb][i][a]);
        }
  STAMP(10);
  BAR();
  NOPS(7);
  SB0();
  LAS f32x4* red = (LAS f32x4*)(L + lane * 16);
#pragma unroll
  for (int a = 0; a < 4; ++a)
#pragma unroll
    for (int n = 0; n < 5; ++n) red[((w * 4 + a) * 5 + n) * 64] = acc[a][n];
  wait_lgkm();
  BAR();
  unsigned o[10];
#pragma unroll
  for (int n = 0; n < 5; ++n) {
    const f32x4 s = (red[((0 * 4 + w) * 5 + n) * 64] + red[((1 * 4 + w) * 5 + n) * 64]) + (red[((2 * 4 + w) * 5 + n) * 64] + red[((3 * 4 + w) * 5 + n) * 64]);
    o[2 * n] = pk2(s[0], s[1]);
    o[2 * n + 1] = pk2(s[2], s[3]);
  }
  const int tok = 64 * jt + 32 * (w >> 1) + 4 * (r >> 1) + 2 * (w & 1) + (r & 1);
  GAS char* op = (GAS char*)p.out + (size_t)tok * 1280 + c * 160 + g * 40;
#pragma unroll
  for (int i = 0; i < 5; ++i) *(GAS v2u*)(op + 8 * i) = v2u{o[2 * i], o[2 * i + 1]};
  STAMP(11);
  if (b == 0) {
    const unsigned fo = W_FO + (threadIdx.x * 4) * 128;
    settle_flags(p.ws, fo, tag, ld_flag(p.ws, fo));
    BAR();
    if (w == 0) st_flag(rs, W_EPOCH, lane, tag);
  }
}

__global__ __launch_bounds__(256, 1) void fused_kernel(Params p) {
  extern __shared__ __attribute__((aligned(16))) char smem[];
  LAS char* L = (LAS char*)smem;
  const int b = blockIdx.x, lane = threadIdx.x & 63;
  const int w = __builtin_amdgcn_readfirstlane(threadIdx.x >> 6);
  STAMP(0);
  const __amdgpu_buffer_rsrc_t rs = __builtin_amdgcn_make_buffer_rsrc((void*)p.ws, 0, W_BYTES, 0x00020000);
  const int xcd = b & 7, idx = b >> 3;
  const int rt = (xcd & 1) * 8 + (idx & 7), cg = (xcd >> 1) * 4 + (idx >> 3);
  const int jt = xcd * 4 + (idx & 3), c2 = idx >> 2;
  const int rot = KROT ? idx % 5 : 0;
  u64 tag = 0;
  v4u wo[50];
  if (cg < 8) phase1<false>(p, L, rs, b, rt, cg, w, lane, rot, c2, tag, wo);
  else phase1<true>(p, L, rs, b, rt, cg - 8, w, lane, rot, c2, tag, wo);
  phase2(p, L, rs, b, w, lane, jt, c2, tag, wo);
}

extern "C" void kernel_launch(void* const* d_in, const int* in_sizes, int n_in, void* d_out, int out_size, void* d_ws, size_t ws_size,
                              hipStream_t stream) {
  Params p;
  p.x = (const char*)d_in[0];
  p.wA = (const char*)d_in[1];
  p.woA = (const char*)d_in[2];
  p.wB = (const char*)d_in[4];
  p.woB = (const char*)d_in[5];
  p.gw = (const char*)d_in[6];
  p.out = (char*)d_out;
  p.ws = (char*)d_ws;
  (void)hipFuncSetAttribute((const void*)fused_kernel, hipFuncAttributeMaxDynamicSharedMemorySize, LDS_BYTES);
  fused_kernel<<<256, 256, LDS_BYTES, stream>>>(p);
}
```

```cpp
#include <hip/hip_runtime.h>
#include <stdint.h>
#include <type_traits>

#define LAS __attribute__((address_space(3)))
#define GAS __attribute__((address_space(1)))
typedef short bf16x8 __attribute__((ext_vector_type(8)));
typedef short s16x4 __attribute__((ext_vector_type(4)));
typedef unsigned v4u __attribute__((ext_vector_type(4)));
typedef unsigned v2u __attribute__((ext_vector_type(2)));
typedef float f32x4 __attribute__((ext_vector_type(4)));
typedef float f32x2 __attribute__((ext_vector_type(2)));
typedef __bf16 bf2 __attribute__((ext_vector_type(2)));
typedef unsigned long long u64;

#define KROT 1
#define CODEPF 1

constexpr int UNIT = 5120, SLOT = 4 * UNIT;
constexpr int L_RING = 0;
constexpr int L_XST = 4 * SLOT;
constexpr int L_PACK = L_XST + 65536;
constexpr int L_AUX = L_PACK + 2 * UNIT;
constexpr int LDS_BYTES = 163840;
constexpr int GSTR = 5888;
constexpr int L_KL = L_XST, L_VL = L_XST + 4 * GSTR;
constexpr int L_SLOT4 = L_XST + 8 * GSTR;
constexpr int L_RED = L_XST;
constexpr int L_KVS = L_XST + 32768;
static_assert(L_SLOT4 + SLOT <= L_AUX && L_AUX + 4096 <= LDS_BYTES, "lds map");
constexpr int slot_base(int s) { return s == 14 ? L_SLOT4 : L_RING + (s & 3) * SLOT; }
constexpr int W_EPOCH = 0;
constexpr int W_FE = 131072, W_FM = 2 * 131072, W_FO = 3 * 131072, W_FK = 4 * 131072;
constexpr int W_PART = 1 << 20, PART_STR = 4096;
constexpr int W_HALO = 2 << 20, HALO_STR = 5120;
constexpr int W_WOP = 3 << 20;
constexpr int W_OB = 5 << 20;
constexpr int W_BYTES = W_OB + 128 * 40 * 1024;
constexpr int SC1 = 16;
constexpr unsigned OOB = 0x40000000u;
constexpr unsigned SPIN_CAP = 1u << 16;

struct Params {
  const char *x, *wA, *woA, *wB, *woB, *gw;
  char *out, *ws;
};

template <int I, int N, class F> __device__ __forceinline__ void sfor(F&& f) {
  if constexpr (I < N) { f(std::integral_constant<int, I>{}); sfor<I + 1, N>(f); }
}
template <int I, int N, class F> __device__ __forceinline__ void sfor2(F&& f) {
  if constexpr (N - I <= 20) sfor<I, N>(f);
  else { sfor<I, I + 20>(f); sfor2<I + 20, N>(f); }
}
#define IC(n) std::integral_constant<int, (n)>{}
__device__ __forceinline__ unsigned pk2(float a, float b) {
  f32x2 t = {a, b};
  return __builtin_bit_cast(unsigned, __builtin_convertvector(t, bf2));
}
__device__ __forceinline__ float dot2(unsigned a, unsigned b, float c) {
  return __builtin_amdgcn_fdot2_f32_bf16(__builtin_bit_cast(bf2, a), __builtin_bit_cast(bf2, b), c, false);
}
#define MF "v_mfma_f32_16x16x32_bf16 "
__device__ __forceinline__ void mm_va(f32x4& d, v4u v, v4u a) { asm volatile(MF "%0, %1, %2, %0" : "+v"(d) : "v"(v), "a"(a)); }
__device__ __forceinline__ void mm_av(f32x4& d, v4u a, v4u v) { asm volatile(MF "%0, %1, %2, %0" : "+v"(d) : "a"(a), "v"(v)); }
__device__ __forceinline__ void mm_vv(f32x4& d, v4u a, v4u b) { asm volatile(MF "%0, %1, %2, %0" : "+v"(d) : "v"(a), "v"(b)); }
__device__ __forceinline__ void mm_va0(f32x4& d, v4u v, v4u a) { asm volatile(MF "%0, %1, %2, 0" : "=&v"(d) : "v"(v), "a"(a)); }
__device__ __forceinline__ void mm_av0(f32x4& d, v4u a, v4u v) { asm volatile(MF "%0, %1, %2, 0" : "=&v"(d) : "a"(a), "v"(v)); }
__device__ __forceinline__ void mm_vv0(f32x4& d, v4u a, v4u b) { asm volatile(MF "%0, %1, %2, 0" : "=&v"(d) : "v"(a), "v"(b)); }
__device__ __forceinline__ void mm_vac(f32x4& d, v4u v, v4u a, f32x4 c) { asm volatile(MF "%0, %1, %2, %3" : "=&v"(d) : "v"(v), "a"(a), "v"(c)); }
__device__ __forceinline__ void mm_avc(f32x4& d, v4u a, v4u v, f32x4 c) { asm volatile(MF "%0, %1, %2, %3" : "=&v"(d) : "a"(a), "v"(v), "v"(c)); }
__device__ __forceinline__ void mm_vvc(f32x4& d, v4u a, v4u b, f32x4 c) { asm volatile(MF "%0, %1, %2, %3" : "=&v"(d) : "v"(a), "v"(b), "v"(c)); }
#define NOPS(n) asm volatile("s_nop " #n)
#define PIN(x) asm volatile("" : "+v"(x))

template <int IMM> __device__ __forceinline__ void dma16(const char* sbase, unsigned voff, unsigned ldsbase) {
  asm volatile("s_add_u32 m0, %2, %3\n\ts_nop 0\n\tglobal_load_lds_dwordx4 %0, %1" ::"v"(voff), "s"(sbase), "s"(ldsbase), "i"(IMM) : "memory", "scc");
}
template <int IMM> __device__ __forceinline__ void dma_m0(unsigned ldsbase) { asm volatile("s_add_u32 m0, %0, %1" ::"s"(ldsbase), "i"(IMM) : "memory", "scc"); }
__device__ __forceinline__ void dma_go(const char* sbase, unsigned voff) { asm volatile("global_load_lds_dwordx4 %0, %1" ::"v"(voff), "s"(sbase) : "memory"); }
template <int N> __device__ __forceinline__ void wait_vm() { asm volatile("s_waitcnt vmcnt(%0)" ::"i"(N) : "memory"); }
__device__ __forceinline__ void wait_lgkm() { asm volatile("s_waitcnt lgkmcnt(0)" ::: "memory"); }
__device__ __forceinline__ v2u trread(LAS char* p) {
  return __builtin_bit_cast(v2u, __builtin_amdgcn_ds_read_tr16_b64_v4i16((LAS s16x4*)p));
}
#define SB0() __builtin_amdgcn_sched_barrier(0)
#define BAR() __builtin_amdgcn_s_barrier()
#define LD128(off) __builtin_amdgcn_raw_buffer_load_b128(rs, (off), 0, SC1)
#define LD128S(voff, soff) __builtin_amdgcn_raw_buffer_load_b128(rs, (voff), (soff), SC1)
#define ST128S(v, voff, soff) __builtin_amdgcn_raw_buffer_store_b128((v), rs, (voff) + (soff), 0, SC1)
#define ST128(v, off) __builtin_amdgcn_raw_buffer_store_b128((v), rs, (off), 0, SC1)
#define STAMP(i) do {} while (0)

__device__ __forceinline__ u64 ld_flag(char* ws, unsigned off) {
  return __hip_atomic_load((GAS u64*)(ws + off), __ATOMIC_RELAXED, __HIP_MEMORY_SCOPE_AGENT);
}
__device__ __forceinline__ void settle_flags(char* ws, unsigned off, u64 tag, u64 v) {
  if (__builtin_expect(!__all(v == tag), 0)) {
    for (unsigned spins = 0; spins < SPIN_CAP; ++spins) {
      if (__all(ld_flag(ws, off) == tag)) break;
      __builtin_amdgcn_s_sleep(1);
    }
  }
  __builtin_amdgcn_fence(__ATOMIC_ACQUIRE, "wavefront");
}
__device__ __forceinline__ void st_flag(__amdgpu_buffer_rsrc_t rs, unsigned off, int lane, u64 tag) {
  __builtin_amdgcn_raw_buffer_store_b64(v2u{(unsigned)tag, (unsigned)(tag >> 32)}, rs, lane == 0 ? off : OOB, 0, SC1);
}
template <class T> __device__ __forceinline__ T sel4(int i, T a, T b, T c, T d) {
  const bool lo = (i & 1) != 0, hi = (i & 2) != 0;
  const T x = lo ? b : a, y = lo ? d : c;
  return hi ? y : x;
}
constexpr float EPS = 1e-6f, LOG2E = 1.4426950408889634f;
__device__ __forceinline__ float phi_a(float t) { return __builtin_fmaf(t, LOG2E, -EPS * LOG2E); }
__device__ __forceinline__ float phi_b(float u) { return __builtin_amdgcn_fmed3f(__builtin_amdgcn_exp2f(u), 0.f, 1.f); }
__device__ __forceinline__ float phi_c(float t) { float r; asm("v_max_f32 %0, %1, %2" : "=v"(r) : "s"(EPS), "v"(t)); return r; }
__device__ __forceinline__ float phi(float t) { return phi_c(t) + phi_b(phi_a(t)); }

template <bool ISB> constexpr int fr_n(int f) { return (ISB || f < 240) ? f / 5 : 48 + (f - 240) % 12; }
template <bool ISB> constexpr int fr_j(int f) { return (ISB || f < 240) ? f % 5 : ((f - 240) / 12 + 4) % 5; }
constexpr int AHEAD = 8, RING = 12;

struct Lane {
  unsigned woff[5], xo[8];
  const char *Wb, *xb;
  unsigned lw, lx;
  int rot;
};
__device__ __forceinline__ int kblk_of(int rot, int pos) { const int k = pos + rot; return k >= 5 ? k - 5 : k; }
template <int S> __device__ __forceinline__ const char* w_src(const Lane& q) {
  constexpr int ch = S / 5, seg = ch == 0 ? 1 : ch == 1 ? 2 : 0;
  return q.Wb + seg * 1280 + (size_t)kblk_of(q.rot, S % 5) * (128 * 3840);
}
template <int S, int I> __device__ __forceinline__ void load_w(const Lane& q) { dma16<slot_base(S) + I * 1024>(w_src<S>(q), q.woff[I], q.lw); }
template <int S, int I> __device__ __forceinline__ void load_x(const Lane& q) {
  dma16<L_XST + (S & 1) * 8192 + I * 1024>(q.xb + kblk_of(q.rot, S) * 256, q.xo[I], q.lx);
}

template <bool ISB>
__device__ __forceinline__ void phase1(const Params& p, const Lane& q, LAS char* L, __amdgpu_buffer_rsrc_t rs, int b, int rt, int hg, int w, int lane, int c2,
                                       u64& tag, v4u (&wo)[50]) {
  const int r = lane & 15, g = lane >> 4;
  const unsigned lw = q.lw;
  auto kblk = [&](int pos) { return kblk_of(q.rot, pos); };
  auto loadW1 = [&](auto S, auto I) { load_w<S, I>(q); };
  auto loadWa = [&](auto S, auto I) { dma_m0<slot_base(S) + I * 1024>(lw); };
  auto loadWb = [&](auto S, auto I) { dma_go(w_src<S>(q), q.woff[I]); };
  auto loadX1 = [&](auto S, auto I) { load_x<S, I>(q); };

  LAS char* a1 = L + (lane >> 2) * 160 + (lane & 3) * 40;
  LAS char* a2 = L + (lane >> 2) * 160 + (lane & 3) * 8;
  LAS char* a1h = a1 + 61440;
  LAS char* a2h = a2 + 61440;
  LAS char* a1s = a1 + L_SLOT4;
  LAS char* a2s = a2 + L_SLOT4;
  LAS char* ax0 = L + L_XST + w * 16384 + r * 128 + ((g ^ (r >> 1)) * 16);
  LAS char* ax1 = L + L_XST + w * 16384 + r * 128 + (((4 + g) ^ (r >> 1)) * 16);
  LAS char* ag = L + L_AUX + g * 16;
  const int unit = b + 256 * w;

  v4u xf[20][2], wf[RING], gf2[2][4];
  f32x4 acc[3][2][5], gacc[2];
  f32x4 eps4 = {EPS, EPS, EPS, EPS};
  PIN(eps4);

  auto readW = [&](auto F, auto T) {
    constexpr int f = F, t = T, n = fr_n<ISB>(f), jt = fr_j<ISB>(f), s = n / 4, j = n % 4;
    constexpr bool use1 = ISB || jt == 4;
    constexpr int col = ISB ? 8 * jt : (jt == 4 ? 32 : 40 * jt);
    constexpr int off = j * UNIT + t * 2560 + col;
    v2u v;
    if constexpr (s == 14) v = trread((use1 ? a1s : a2s) + off);
    else if constexpr ((s & 3) == 3) v = trread((use1 ? a1h : a2h) + off);
    else v = trread((use1 ? a1 : a2) + (s & 3) * SLOT + off);
    if constexpr (t == 0) { wf[f % RING].x = v.x; wf[f % RING].y = v.y; }
    else { wf[f % RING].z = v.x; wf[f % RING].w = v.y; }
  };
  auto readX = [&](auto S, auto U) {
    constexpr int s = S, u = U, j = u / 3, k = u % 3;
    if constexpr (k < 2) xf[4 * s + j][k] = *(LAS v4u*)(((j & 1) ? ax1 : ax0) + (s & 1) * 8192 + (j >> 1) * 4096 + k * 2048);
    else gf2[s & 1][j] = *(LAS v4u*)(ag + kblk(s) * 256 + j * 64);
  };

  v4u Kf[5], Vf[5];
  v2u Pq[2][5];
  f32x4 kvT[4], kvU[4], kvW[4], kvX, kvZ, rd[8];
  v4u pst, gl[16];
  float ta[2], tb[2], tc[2];
  unsigned kp[2][10], vp[20], ke[4][20];
  v4u hcp, hv[3];
  v2u ept;
  u64 poll0 = 0, poll1 = 0, poll2 = 0;
  const int e0 = 16 * w + 8 + r;
  LAS char* aK = L + L_KL + g * GSTR + e0 * 80 - 640;
  LAS char* aV = L + L_VL + g * GSTR + e0 * 80 - 640;
  const int cg = (ISB ? 8 : 0) + hg;
  unsigned poll0_off, poll1_off, poll2_off;
  {
    const int li = lane < 10 ? lane : 0;
    const int pu = c2 * 40 + (li / 5) * 20 + 5 * w + li % 5;
    poll0_off = W_FE + ((pu & 255) * 4 + (pu >> 8)) * 128;
    const int rp = rt > 0 ? rt - 1 : 0;
    poll1_off = W_FK + ((cg * 16 + rp) * 4 + (lane & 3)) * 128;
    poll2_off = ISB ? W_FM + ((cg * 16 + rp) * 4 + (lane & 3)) * 128 : W_FM + ((cg * 16 + r) * 4 + w) * 128;
  }
  hv[0] = hv[1] = hv[2] = v4u{0u, 0u, 0u, 0u};
  const unsigned halo_rd = (unsigned)(W_HALO + (hg * 16 + rt - 1) * HALO_STR + lane * 16);
  unsigned hl[3];
#pragma unroll
  for (int i = 0; i < 3; ++i) { int q = i * 64 + lane; q = q < 160 ? q : 159; hl[i] = (unsigned)((q / 40) * GSTR + (q % 40) * 16); }
  unsigned lane16 = lane * 16, fl0 = lane == 0 ? 0u : OOB;
  PIN(lane16); PIN(fl0); PIN(poll0_off); PIN(poll1_off); PIN(poll2_off);
  const int wo_b = W_WOP + (c2 * 40 + 5 * w) * 5 * 1024;
  auto ld_wo = [&](auto I) {
    constexpr int i = I, fi = i < 25 ? 100 + i : i - 25;
    wo[i] = LD128S(lane16 + (fi & 3) * 1024, wo_b + (fi >> 2) * 4096);
  };
  auto flag = [&](int off) { __builtin_amdgcn_raw_buffer_store_b64(v2u{(unsigned)tag, (unsigned)(tag >> 32)}, rs, fl0, off, SC1); };

  wait_vm<13>();
  BAR();
  STAMP(1);
  sfor<0, 3>([&](auto U) { readX(IC(0), U); });
  sfor<0, AHEAD>([&](auto F) { readW(F, IC(0)); readW(F, IC(1)); });
  sfor<3, 12>([&](auto U) { readX(IC(0), U); });
  SB0();

  constexpr int KE = ISB ? 2 : 4;
  constexpr int NE = 4 * KE;
  constexpr int WO0 = 441;
  constexpr int NWO1 = ISB ? 47 : 38;
  sfor2<0, 600>([&](auto M) {
    constexpr int m = M, f = m / 2, tt = m & 1, n = fr_n<ISB>(f), jt = fr_j<ISB>(f), ch = n / 20, ks = n % 20;
    constexpr int sb = (m - 24) / 40, mb = (m - 24) % 40;
    if constexpr (m >= 24 && mb == 0 && sb <= 11) {
      constexpr int cnt = sb <= 2 ? 13 : sb <= 5 ? 5 : sb <= 9 ? 5 + KE : sb == 10 ? 10 : 7;
      if constexpr (sb == 5) asm volatile("s_waitcnt vmcnt(5)" : "+v"(poll0)::"memory");
      else if constexpr (sb == 10) asm volatile("s_waitcnt vmcnt(10)" : "+v"(poll1)::"memory");
      else wait_vm<cnt>();
      BAR();
      SB0();
    }
    if constexpr (!ISB && m == 438) { wait_lgkm(); BAR(); SB0(); }
    if constexpr (m % 8 == 0 && m > 0 && f + AHEAD <= 300) { __builtin_amdgcn_s_waitcnt(0xC07F | (8 << 8)); SB0(); }
    {
      f32x4& d = acc[ch][tt][jt];
      const v4u& wv = wf[f % RING];
      if constexpr (ISB) { if constexpr (ks == 0) mm_va0(d, wv, xf[ks][tt]); else mm_va(d, wv, xf[ks][tt]); }
      else if constexpr (ch == 2) { if constexpr (ks == 0) mm_vac(d, wv, xf[ks][tt], eps4); else mm_va(d, wv, xf[ks][tt]); }
      else if constexpr (ks != 0) mm_av(d, xf[ks][tt], wv);
      else if constexpr (ch == 0) mm_avc(d, xf[ks][tt], wv, eps4);
      else mm_av0(d, xf[ks][tt], wv);
    }
    SB0();
    if constexpr (f + AHEAD < 300) readW(IC(f + AHEAD < 300 ? f + AHEAD : 0), IC(tt));
    if constexpr (m >= 24 && sb <= 3 && mb < 12) readX(IC(sb + 1), IC(mb));
    SB0();
    if constexpr (m < 200 && m % 10 == 9) {
      if constexpr (n == 0) { mm_va0(gacc[0], gf2[0][0], xf[0][0]); mm_va0(gacc[1], gf2[0][0], xf[0][1]); }
      else { mm_va(gacc[0], gf2[(n >> 2) & 1][n & 3], xf[n][0]); mm_va(gacc[1], gf2[(n >> 2) & 1][n & 3], xf[n][1]); }
    }
    if constexpr (m < 5) loadW1(IC(2), IC(m));
    if constexpr (m == 6) {
      if (unit < 320) {
        const int pf = unit % 40, pc = unit / 40, pbr = pf / 20, pP = (pf % 20) / 5, pi = pf % 5;
        const int ii = pi >= 3 ? pi - 3 : pi, hb = 160 * pP + (pi >= 3 ? 80 : 0);
        const int cgx = pbr ? 20 : 4, cg_ = pi == 2 ? 40 : cgx;
        const int ct = (pi != 2 && pbr) ? 4 : 20;
        const int c0x = hb + (pbr ? 8 : 40) * ii, c0 = pi == 2 ? 160 * pP + 16 : c0x;
        const char* pb = (pbr ? p.woB : p.woA) + pc * 160 + c0 * 1280;
        sfor<0, 5>([&](auto I) {
          const int c = I * 64 + lane, rho = c / 10, cc = c % 10;
          dma16<L_PACK + I * 1024>(pb, (unsigned)((cg_ * ((rho >> 2) & 3) + ct * (rho >> 4) + (rho & 3)) * 1280 + cc * 16), lw);
        });
      }
    }
    if constexpr (m < 104 && m % 40 >= 8 && m % 40 <= 22 && m % 2 == 0) loadX1(IC(m / 40 + 2), IC((m % 40 - 8) / 2));
    if constexpr (m >= 24 && sb <= 1 && mb >= 12 && mb <= 20 && mb % 2 == 0) loadW1(IC(sb + 3), IC((mb - 12) / 2));
    if constexpr (m >= 24 && (sb == 2 || sb == 3) && mb >= 12 && mb <= 28 && mb % 4 == 0) loadW1(IC(sb + 3), IC((mb - 12) / 4));
    if constexpr (m >= 24 && sb >= 4 && sb <= 9 && mb % 8 == 2) loadWa(IC(sb + 3), IC(mb / 8));
    if constexpr (m >= 24 && sb >= 4 && sb <= 9 && mb % 8 == 3) loadWb(IC(sb + 3), IC(mb / 8));
    if constexpr (m >= 24 && sb == 9 && mb % 8 == 6) loadWa(IC(14), IC(mb / 8));
    if constexpr (m >= 24 && sb == 9 && mb % 8 == 7) loadWb(IC(14), IC(mb / 8));
    if constexpr (m >= 24 && sb == 10 && mb >= 1 && mb <= 10 && mb % 2 == 1) loadWa(IC(13), IC((mb - 1) / 2));
    if constexpr (m >= 24 && sb == 10 && mb >= 1 && mb <= 10 && mb % 2 == 0) loadWb(IC(13), IC((mb - 1) / 2));
    if constexpr (m == 206) { PIN(gacc[0]); PIN(gacc[1]); }
    if constexpr (m == 46) ept = *(LAS v2u*)(L + L_AUX + 3072);
    if constexpr (m == 63) tag = (((u64)(unsigned)__builtin_amdgcn_readfirstlane(ept.y) << 32) | (unsigned)__builtin_amdgcn_readfirstlane(ept.x)) + 1ull;
    if constexpr (m == 86) {
      if (unit < 320) {
        v4u pv[5];
        sfor<0, 5>([&](auto J) {
          const v2u lo = trread(a1 + L_PACK + w * UNIT + 8 * J), hi = trread(a1 + L_PACK + w * UNIT + 2560 + 8 * J);
          pv[J] = v4u{lo.x, lo.y, hi.x, hi.y};
        });
        sfor<0, 5>([&](auto J) { ST128S(pv[J], lane16, W_WOP + (unit * 5 + J) * 1024); });
      }
    }
    if constexpr (m == 145) flag(W_FE + (b * 4 + w) * 128);
    if constexpr (m == 185) asm volatile("global_load_dwordx2 %0, %1, %2 sc1" : "+v"(poll0) : "v"(poll0_off), "s"(p.ws) : "memory");
    if constexpr (m == 225) settle_flags(p.ws, poll0_off, tag, poll0);
    if constexpr (m >= 24 && sb >= 5 && sb <= 8 && mb % (32 / KE) == 6 && mb < 32) ld_wo(IC((sb - 5) * KE + mb / (32 / KE)));
    if constexpr (m >= WO0 && (m - WO0) % 4 == 0 && NE + (m - WO0) / 4 < NWO1) ld_wo(IC(NE + (m - WO0) / 4));
    if constexpr (ISB && m == 345) flag(W_FK + ((cg * 16 + rt) * 4 + w) * 128);
    if constexpr (ISB && m == 385) asm volatile("global_load_dwordx2 %0, %1, %2 sc1" : "+v"(poll1) : "v"(poll1_off), "s"(p.ws) : "memory");
    if constexpr (!ISB) {
      auto phi_op = [&](auto Z, const f32x4& t, auto H, unsigned& out) {
        constexpr int z = Z, h = H;
        if constexpr (z < 2) { ta[z] = phi_a(t[2 * h + z]); PIN(ta[z]); }
        else if constexpr (z < 4) { tb[z - 2] = phi_b(ta[z - 2]); PIN(tb[z - 2]); }
        else if constexpr (z < 6) { tc[z - 4] = phi_c(t[2 * h + z - 4]); PIN(tc[z - 4]); }
        else if constexpr (z < 8) { tc[z - 6] += tb[z - 6]; PIN(tc[z - 6]); }
        else { out = pk2(tc[0], tc[1]); PIN(out); }
      };
      if constexpr (m >= 200 && m < 400 && m % 10 < 9) {
        constexpr int pr = (m - 200) / 10, kt = pr >> 2, t2 = (pr >> 1) & 1, h = pr & 1;
        unsigned o = 0;
        phi_op(IC(m % 10), acc[0][t2][kt], IC(h), o);
        if constexpr (m % 10 == 8) Kf[kt][2 * t2 + h] = o;
      }
      if constexpr (m >= 400 && m < 410) {
        constexpr int vt = (m - 400) >> 1;
        unsigned lo = pk2(acc[1][tt][vt][0], acc[1][tt][vt][1]), hi = pk2(acc[1][tt][vt][2], acc[1][tt][vt][3]);
        PIN(lo); PIN(hi);
        Vf[vt][2 * tt] = lo; Vf[vt][2 * tt + 1] = hi;
      }
      if constexpr (m >= 402 && m < 407) {
        const unsigned one = (r == m - 402) ? 0x3F803F80u : 0u;
        v4u ones = {one, one, one, one};
        PIN(ones); NOPS(1);
        if constexpr (m == 402) mm_vv0(kvZ, Kf[0], ones); else mm_vv(kvZ, Kf[m - 402], ones);
      }
      if constexpr (m >= 409 && m < 413) mm_vv0(kvU[m - 409], Kf[4], Vf[m - 409]);
      if constexpr (m >= 413 && m < 417) mm_vv0(kvW[m - 413], Kf[m - 413], Vf[4]);
      if constexpr (m >= 417 && m < 421) mm_vv0(kvT[m - 417], Kf[m - 417], Vf[m - 417]);
      if constexpr (m == 421) mm_vv0(kvX, Kf[4], Vf[4]);
      if constexpr (m >= 417 && m < 421) {
        constexpr int q = m - 417;
        float u = sel4(g, kvU[0][q], kvU[1][q], kvU[2][q], kvU[3][q]);
        PIN(u); kvU[0][q] = u;
      }
      if constexpr (m >= 421 && m < 425) {
        constexpr int q = m - 421;
        float u = sel4(r >> 2, kvW[0][q], kvW[1][q], kvW[2][q], kvW[3][q]);
        PIN(u); kvW[0][q] = u;
      }
      {
        LAS f32x4* red = (LAS f32x4*)(L + L_RED + w * 8192 + lane * 16);
        if constexpr (m == 414) red[7 * 64] = kvZ;
        if constexpr (m >= 425 && m < 429) red[(m - 425) * 64] = kvT[m - 425];
        if constexpr (m == 429) red[4 * 64] = kvU[0];
        if constexpr (m == 430) red[6 * 64] = kvX;
        if constexpr (m == 431) red[5 * 64] = kvW[0];
      }
      if constexpr (m >= 438 && m < 446) rd[m - 438] = *(LAS f32x4*)(L + L_RED + w * 2048 + lane * 16 + ((m - 438) >> 1) * 8192 + ((m - 438) & 1) * 1024);
      if constexpr (m >= 448 && m < 460) {
        constexpr int q = (m - 448) >> 1, e = 2 * ((m - 448) & 1);
        float u0 = rd[q & 1][e] + rd[2 + q][e]; PIN(u0);
        float u1 = rd[q & 1][e + 1] + rd[2 + q][e + 1]; PIN(u1);
        rd[q & 1][e] = u0; rd[q & 1][e + 1] = u1;
      }
      if constexpr (m == 460 || m == 461) {
        unsigned lo = pk2(rd[m - 460][0], rd[m - 460][1]), hi = pk2(rd[m - 460][2], rd[m - 460][3]);
        PIN(lo); PIN(hi);
        pst[2 * (m - 460)] = lo; pst[2 * (m - 460) + 1] = hi;
      }
      if constexpr (m == 462) ST128S(pst, lane16, W_PART + (hg * 16 + rt) * PART_STR + w * 1024);
      if constexpr (m == 514) { wait_vm<13>(); flag(W_FM + ((cg * 16 + rt) * 4 + w) * 128); }
      if constexpr (m == 543) poll2 = ld_flag(p.ws, poll2_off);
      if constexpr (m == 582) settle_flags(p.ws, poll2_off, tag, poll2);
      if constexpr (m >= 583 && m < 599) gl[m - 583] = LD128S(lane16, W_PART + (hg * 16 + m - 583) * PART_STR + w * 1024);
      if constexpr (m >= 508) {
        constexpr int t = (m - 480) / 24 - 1, u = (m - 480) % 24 - 4, qt = (t + 4) % 5;
        if constexpr (u >= 0 && u < 18) {
          sfor<0, 2>([&](auto E) {
            constexpr int z = 2 * u + E, pr = z / 9, t2 = pr >> 1, h = pr & 1;
            unsigned o = 0;
            phi_op(IC(z % 9), acc[2][t2][qt], IC(h), o);
            if constexpr (z % 9 == 8) Pq[t2][qt][h] = o;
          });
        }
      }
    } else {
      if constexpr (m >= 200 && m < 220) {
        constexpr int q = m - 200, kt = q >> 2, t2 = (q >> 1) & 1, h = q & 1;
        kp[t2][2 * kt + h] = pk2(acc[0][t2][kt][2 * h], acc[0][t2][kt][2 * h + 1]);
        PIN(kp[t2][2 * kt + h]);
      }
      if constexpr (m >= 221 && m < 241 && m % 4 == 1) {
        constexpr int c = (m - 221) / 4;
        auto kk = [&](int i) { return i < 10 ? kp[0][i] : kp[1][i - 10]; };
        *(LAS v4u*)(aK + 640 + c * 16) = v4u{kk(4 * c), kk(4 * c + 1), kk(4 * c + 2), kk(4 * c + 3)};
      }
      if constexpr (m == 265) hcp = *(LAS v4u*)(L + L_KL + w * GSTR + 64 * 80 + (lane < 40 ? lane : 0) * 16);
      if constexpr (m == 284) ST128(hcp, lane < 40 ? W_HALO + (hg * 16 + rt) * HALO_STR + w * 640 + lane * 16 : OOB);
      if constexpr (m >= 400 && m < 410) {
        constexpr int vt = (m - 400) >> 1;
        vp[4 * vt + 2 * tt] = pk2(acc[1][0][vt][2 * tt], acc[1][1][vt][2 * tt]);
        vp[4 * vt + 2 * tt + 1] = pk2(acc[1][0][vt][2 * tt + 1], acc[1][1][vt][2 * tt + 1]);
        PIN(vp[4 * vt + 2 * tt]); PIN(vp[4 * vt + 2 * tt + 1]);
      }
      if constexpr (m >= 410 && m <= 418 && m % 2 == 0) {
        constexpr int c = (m - 410) / 2;
        *(LAS v4u*)(aV + 640 + c * 16) = v4u{vp[4 * c], vp[4 * c + 1], vp[4 * c + 2], vp[4 * c + 3]};
      }
      if constexpr (m == 423) wait_lgkm();
      if constexpr (m == 424) hcp = *(LAS v4u*)(L + L_VL + w * GSTR + 64 * 80 + (lane < 40 ? lane : 0) * 16);
      if constexpr (m == 437) {
        if (w == 0 && rt > 0) {
          settle_flags(p.ws, poll1_off, tag, poll1);
#pragma unroll
          for (int i = 0; i < 3; ++i) hv[i] = LD128(i < 2 || lane < 32 ? halo_rd + i * 1024 : halo_rd + 159 * 16 - lane * 16);
        }
      }
      if constexpr (m == 439) ST128(hcp, lane < 40 ? W_HALO + (hg * 16 + rt) * HALO_STR + 2560 + w * 640 + lane * 16 : OOB);
      if constexpr (m == 494) { wait_vm<14>(); flag(W_FM + ((cg * 16 + rt) * 4 + w) * 128); }
      if constexpr (m == 498) {
        if (w == 0) {
#pragma unroll
          for (int i = 0; i < 3; ++i) *(LAS v4u*)(L + L_KL + hl[i]) = hv[i];
        }
      }
      if constexpr (m == 526) poll2 = ld_flag(p.ws, poll2_off);
      if constexpr (m == 566) {
        if (w == 0 && rt > 0) {
          settle_flags(p.ws, poll2_off, tag, poll2);
#pragma unroll
          for (int i = 0; i < 3; ++i) hv[i] = LD128(i < 2 || lane < 32 ? halo_rd + 2560 + i * 1024 : halo_rd + 2560 + 159 * 16 - lane * 16);
        }
      }
      if constexpr (m >= 580 && m < 598) {
        constexpr int q = m - 580, i = q / 5, c = i == 3 ? q - 15 + 2 : q % 5;
        const v4u v = *(LAS v4u*)(aK + 640 - (i + 1) * 160 + c * 16);
        ke[i][4 * c] = v.x; ke[i][4 * c + 1] = v.y; ke[i][4 * c + 2] = v.z; ke[i][4 * c + 3] = v.w;
      }
    }
    SB0();
    if constexpr (m == 199) STAMP(2);
    if constexpr (m == 399) STAMP(3);
  });
  STAMP(4);
  sfor<NWO1, 50>([&](auto I) { ld_wo(I); });
  NOPS(7);
  SB0();

  float gate[2];
#pragma unroll
  for (int tt = 0; tt < 2; ++tt) {
    const float e = __builtin_amdgcn_exp2f(gacc[tt][0] * (ISB ? LOG2E : -LOG2E));
    gate[tt] = __builtin_amdgcn_rcpf(1.f + e);
  }
  v4u F0[2], F1[2];
  v2u F2[2];
  if constexpr (!ISB) {
#pragma unroll
    for (int tt = 0; tt < 2; ++tt) {
      const f32x4 q = acc[2][tt][3];
      Pq[tt][3] = v2u{pk2(phi(q[0]), phi(q[1])), pk2(phi(q[2]), phi(q[3]))};
    }
    v4u Pf[2][4];
#pragma unroll
    for (int h = 0; h < 4; ++h)
#pragma unroll
      for (int tt = 0; tt < 2; ++tt) { Pf[tt][h] = v4u{Pq[tt][h].x, Pq[tt][h].y, Pq[tt][4].x, Pq[tt][4].y}; PIN(Pf[tt][h]); }
    const unsigned sv = (g == (r >> 2)) ? ((r & 1) ? 0x3F800000u : 0x00003F80u) : 0u;
    const unsigned s0_ = (r & 2) ? 0u : sv, s1_ = (r & 2) ? sv : 0u;
    v4u A1 = {s0_, s1_, 0u, 0u}, A2 = {0u, 0u, s0_, s1_};
    PIN(A1); PIN(A2); NOPS(1);
    SB0();
    f32x4 d1, d2;
#pragma unroll
    for (int i = 0; i < 16; ++i) {
      if (i == 0) { mm_vv0(d1, A1, gl[0]); mm_vv0(d2, A2, gl[0]); }
      else { mm_vv(d1, A1, gl[i]); mm_vv(d2, A2, gl[i]); }
    }
    NOPS(7);
    SB0();
    LAS v2u* kvs = (LAS v2u*)(L + L_KVS);
    kvs[(2 * w) * 64 + lane] = v2u{pk2(d1[0], d1[1]), pk2(d1[2], d1[3])};
    kvs[(2 * w + 1) * 64 + lane] = v2u{pk2(d2[0], d2[1]), pk2(d2[2], d2[3])};
    wait_lgkm();
    BAR();
    STAMP(5);
    const v2u uu = kvs[256 + lane], ww = kvs[320 + lane], xx = kvs[384 + lane], z4 = kvs[448 + g * 16 + 4];
    v4u Nh[4], N4[4], Zh[4];
    v2u tl4[4], zl4[4];
#pragma unroll
    for (int h = 0; h < 4; ++h) { zl4[h] = kvs[448 + g * 16 + h]; tl4[h] = kvs[h * 64 + lane]; }
    SB0();
#pragma unroll
    for (int h = 0; h < 4; ++h) {
      const v2u tl = tl4[h], zl = zl4[h];
      const bool gh = g == h, rh = (r >> 2) == h;
      Nh[h] = v4u{tl.x, tl.y, gh ? uu.x : 0u, gh ? uu.y : 0u};
      N4[h] = v4u{rh ? ww.x : 0u, rh ? ww.y : 0u, (rh && gh) ? xx.x : 0u, (rh && gh) ? xx.y : 0u};
      Zh[h] = v4u{zl.x, zl.y, gh ? z4.x : 0u, gh ? z4.y : 0u};
      PIN(Nh[h]); PIN(N4[h]); PIN(Zh[h]);
    }
    NOPS(1);
    SB0();
    f32x4 num[2][5], den[2][4];
#pragma unroll
    for (int h = 0; h < 4; ++h)
#pragma unroll
      for (int tt = 0; tt < 2; ++tt) mm_vvc(den[tt][h], Zh[h], Pf[tt][h], eps4);
#pragma unroll
    for (int h = 0; h < 4; ++h)
#pragma unroll
      for (int tt = 0; tt < 2; ++tt) mm_vv0(num[tt][h], Nh[h], Pf[tt][h]);
    SB0();
    float inv[2][4];
#pragma unroll
    for (int h = 0; h < 4; ++h)
#pragma unroll
      for (int tt = 0; tt < 2; ++tt) {
        if (h == 0) mm_vv0(num[tt][4], N4[h], Pf[tt][h]); else mm_vv(num[tt][4], N4[h], Pf[tt][h]);
        inv[tt][h] = gate[tt] * __builtin_amdgcn_rcpf(den[tt][h][0]);
        PIN(inv[tt][h]);
        SB0();
      }
#pragma unroll
    for (int tt = 0; tt < 2; ++tt) {
      const f32x4 o0_ = num[tt][0] * inv[tt][0], o1_ = num[tt][1] * inv[tt][1], o2_ = num[tt][2] * inv[tt][2], o3_ = num[tt][3] * inv[tt][3];
      F0[tt] = v4u{pk2(o0_[0], o0_[1]), pk2(o0_[2], o0_[3]), pk2(o1_[0], o1_[1]), pk2(o1_[2], o1_[3])};
      F1[tt] = v4u{pk2(o2_[0], o2_[1]), pk2(o2_[2], o2_[3]), pk2(o3_[0], o3_[1]), pk2(o3_[2], o3_[3])};
      PIN(F0[tt]); PIN(F1[tt]);
    }
    SB0();
#pragma unroll
    for (int tt = 0; tt < 2; ++tt) {
      const float i4 = sel4(g, inv[tt][0], inv[tt][1], inv[tt][2], inv[tt][3]);
      const f32x4 o4_ = num[tt][4] * i4;
      F2[tt] = v2u{pk2(o4_[0], o4_[1]), pk2(o4_[2], o4_[3])};
    }
  } else {
    unsigned qp[2][10];
#pragma unroll
    for (int tt = 0; tt < 2; ++tt)
#pragma unroll
      for (int jt = 0; jt < 5; ++jt) {
        qp[tt][2 * jt] = pk2(acc[2][tt][jt][0], acc[2][tt][jt][1]);
        qp[tt][2 * jt + 1] = pk2(acc[2][tt][jt][2], acc[2][tt][jt][3]);
      }
    float s0[8], s1[8];
    sfor<0, 16>([&](auto Z) {
      constexpr int z = Z, qi = z & 1, wi = z >> 1, kr = wi + 1 - qi;
      float sv = 0.f;
#pragma unroll
      for (int d = 0; d < 10; ++d) sv = dot2(qp[qi][d], kr == 0 ? kp[1][d] : kr == 1 ? kp[0][d] : ke[(kr >= 2 ? kr - 2 : 0) / 2][(kr & 1) ? d : 10 + d], sv);
      (qi ? s1[wi] : s0[wi]) = sv;
    });
    SB0();
    constexpr float SCALE = 0.22360679774997896f;
    if (w == 0) {
#pragma unroll
      for (int i = 0; i < 3; ++i) *(LAS v4u*)(L + L_VL + hl[i]) = hv[i];
      if (rt == 0) {
        const int u0 = 4 * (r >> 1) + (r & 1);
#pragma unroll
        for (int i = 1; i < 8; ++i) {
          if (u0 - 2 * i < 0) s0[i] = -1e9f / SCALE;
          if (u0 + 2 - 2 * i < 0) s1[i] = -1e9f / SCALE;
        }
      }
    }
    STAMP(5);
    v4u ve[4][5];
#pragma unroll
    for (int i = 0; i < 4; ++i)
#pragma unroll
      for (int c = 0; c < 5; ++c) ve[i][c] = *(LAS v4u*)(aV + 640 - (i + 1) * 160 + c * 16);
    SB0();
    float m0 = s0[0], m1 = s1[0];
#pragma unroll
    for (int i = 1; i < 8; ++i) { m0 = __builtin_fmaxf(m0, s0[i]); m1 = __builtin_fmaxf(m1, s1[i]); }
    constexpr float C = SCALE * LOG2E;
    float sum0 = 0.f, sum1 = 0.f;
#pragma unroll
    for (int i = 0; i < 8; ++i) {
      s0[i] = __builtin_amdgcn_exp2f((s0[i] - m0) * C); sum0 += s0[i];
      s1[i] = __builtin_amdgcn_exp2f((s1[i] - m1) * C); sum1 += s1[i];
    }
    const float i0 = gate[0] * __builtin_amdgcn_rcpf(sum0), i1 = gate[1] * __builtin_amdgcn_rcpf(sum1);
#pragma unroll
    for (int i = 0; i < 8; ++i) { s0[i] *= i0; s1[i] *= i1; }
    unsigned pw0[5], pw1[4];
    pw0[0] = pk2(s0[0], 0.f);
#pragma unroll
    for (int i = 1; i < 4; ++i) pw0[i] = pk2(s0[2 * i], s0[2 * i - 1]);
    pw0[4] = pk2(0.f, s0[7]);
#pragma unroll
    for (int i = 0; i < 4; ++i) pw1[i] = pk2(s1[2 * i + 1], s1[2 * i]);
    float o0[20], o1[20];
#pragma unroll
    for (int d = 0; d < 20; ++d) { o0[d] = dot2(vp[d], pw0[0], 0.f); o1[d] = dot2(vp[d], pw1[0], 0.f); }
#pragma unroll
    for (int i = 1; i <= 4; ++i)
#pragma unroll
      for (int d = 0; d < 20; ++d) {
        o0[d] = dot2(ve[i - 1][d >> 2][d & 3], pw0[i], o0[d]);
        if (i < 4) o1[d] = dot2(ve[i - 1][d >> 2][d & 3], pw1[i], o1[d]);
      }
    F0[0] = v4u{pk2(o0[0], o0[1]), pk2(o0[2], o0[3]), pk2(o0[4], o0[5]), pk2(o0[6], o0[7])};
    F1[0] = v4u{pk2(o0[8], o0[9]), pk2(o0[10], o0[11]), pk2(o0[12], o0[13]), pk2(o0[14], o0[15])};
    F2[0] = v2u{pk2(o0[16], o0[17]), pk2(o0[18], o0[19])};
    F0[1] = v4u{pk2(o1[0], o1[1]), pk2(o1[2], o1[3]), pk2(o1[4], o1[5]), pk2(o1[6], o1[7])};
    F1[1] = v4u{pk2(o1[8], o1[9]), pk2(o1[10], o1[11]), pk2(o1[12], o1[13]), pk2(o1[14], o1[15])};
    F2[1] = v2u{pk2(o1[16], o1[17]), pk2(o1[18], o1[19])};
  }
  {
    const int fb = (ISB ? 20 : 0) + (hg >> 1) * 5, f0 = fb + ((hg & 1) ? 3 : 0);
    const int TT = rt * 8 + 2 * w;
#pragma unroll
    for (int tt = 0; tt < 2; ++tt) {
      ST128S(F0[tt], lane16, W_OB + ((TT + tt) * 40 + f0) * 1024);
      ST128S(F1[tt], lane16, W_OB + ((TT + tt) * 40 + f0 + 1) * 1024);
    }
    const auto sx = __builtin_amdgcn_permlane16_swap(F2[0].x, F2[1].x, false, false);
    const auto sy = __builtin_amdgcn_permlane16_swap(F2[0].y, F2[1].y, false, false);
    ST128((v4u{sx[0], sy[0], sx[1], sy[1]}), W_OB + ((TT + (g & 1)) * 40 + fb + 2) * 1024 + ((hg & 1) * 2 + (g >> 1)) * 256 + r * 16);
  }
  STAMP(6);
  __builtin_amdgcn_s_waitcnt(0x0F70);
  wait_vm<0>();
  flag(W_FO + ((rt * 16 + cg) * 4 + w) * 128);
  STAMP(7);
}

__device__ __forceinline__ void phase2(const Params& p, LAS char* L, __amdgpu_buffer_rsrc_t rs, int b, int w, int lane, int jt, int c, u64 tag,
                                       v4u (&wo)[50]) {
  const int r = lane & 15, g = lane >> 4;
  v4u of[2][4][5];
  f32x4 acc[4][5];
  const unsigned lane16 = lane * 16;
  const unsigned fo = W_FO + (((jt >> 1) * 16 + ((lane & 4) ? 0 : 8) + 2 * w + (lane & 1)) * 4 + 2 * (jt & 1) + ((lane >> 1) & 1)) * 128;
  const unsigned foB = (lane & 4) ? fo + 8 * 4 * 128 : fo;
  auto o_off = [&](int hb, int a, int i) { return W_OB + ((4 * jt + ((a + w + 1) & 3)) * 40 + (hb ? 0 : 20) + 5 * w + i) * 1024; };
  settle_flags(p.ws, foB, tag, ld_flag(p.ws, foB));
  STAMP(8);
#pragma unroll
  for (int a = 0; a < 4; ++a)
#pragma unroll
    for (int i = 0; i < 5; ++i) of[0][a][i] = LD128S(lane16, o_off(0, a, i));
  u64 pa = ld_flag(p.ws, fo);
  SB0();
  LAS f32x4* red = (LAS f32x4*)(L + lane * 16);
  sfor2<0, 200>([&](auto M) {
    constexpr int m = M, hb = m / 100, a = (m % 100) / 25, i = (m % 25) / 5, n = m % 5;
    if constexpr (m == 75) { settle_flags(p.ws, fo, tag, pa); STAMP(9); }
    if constexpr (hb == 0 && i == 0) mm_av0(acc[a][n], wo[n], of[0][a][0]);
    else mm_av(acc[a][n], wo[hb * 25 + i * 5 + n], of[hb][a][i]);
    SB0();
    if constexpr (m >= 75 && m < 95) of[1][(m - 75) / 5][(m - 75) % 5] = LD128S(lane16, o_off(1, (m - 75) / 5, (m - 75) % 5));
    if constexpr (m == 120) BAR();
    if constexpr (m >= 130 && (m - 130) % 25 < 20 && (m - 130) % 25 % 4 == 0) {
      constexpr int a2 = (m - 130) / 25, n2 = (m - 130) % 25 / 4;
      red[((((a2 + w + 1) & 3) * 3 + a2) * 5 + n2) * 64] = acc[a2][n2];
    }
    SB0();
  });
  STAMP(10);
  wait_lgkm();
  BAR();
  unsigned o[10];
#pragma unroll
  for (int n = 0; n < 5; ++n) {
    const f32x4 s = (red[((w * 3 + 0) * 5 + n) * 64] + red[((w * 3 + 1) * 5 + n) * 64]) + (red[((w * 3 + 2) * 5 + n) * 64] + acc[3][n]);
    o[2 * n] = pk2(s[0], s[1]);
    o[2 * n + 1] = pk2(s[2], s[3]);
  }
  const int tok = 64 * jt + 32 * (w >> 1) + 4 * (r >> 1) + 2 * (w & 1) + (r & 1);
  GAS char* op = (GAS char*)p.out + (size_t)tok * 1280 + c * 160 + g * 40;
#pragma unroll
  for (int i = 0; i < 5; ++i) *(GAS v2u*)(op + 8 * i) = v2u{o[2 * i], o[2 * i + 1]};
  STAMP(11);
  if (b == 0) {
    const unsigned fa = W_FO + (threadIdx.x * 4) * 128;
    settle_flags(p.ws, fa, tag, ld_flag(p.ws, fa));
    BAR();
    if (w == 0) st_flag(rs, W_EPOCH, lane, tag);
  }
}

__global__ __launch_bounds__(256, 1) void fused_kernel(Params p) {
  extern __shared__ __attribute__((aligned(16))) char smem[];
  LAS char* L = (LAS char*)smem;
  const int b = blockIdx.x, lane = threadIdx.x & 63;
  const int w = __builtin_amdgcn_readfirstlane(threadIdx.x >> 6);
  STAMP(0);
  const int xcd = b & 7, idx = b >> 3;
  const int rt = (xcd & 1) * 8 + (idx & 7), cg = (xcd >> 1) * 4 + (idx >> 3);
  const int jt = xcd * 4 + (idx & 3), c2 = idx >> 2;
  const int r = lane & 15, g = lane >> 4;
  {
    const char* pc; unsigned len;
    asm volatile("s_getpc_b64 %0\nfk_here:\n\ts_mov_b32 %1, fk_end-fk_here" : "=s"(pc), "=s"(len));
    pc = (const char*)((uintptr_t)pc & ~(uintptr_t)63);
    unsigned co = (unsigned)((idx * 4 + w) * 1024 + lane * 16);
    co = co + 16 <= len ? co : 0u;
    asm volatile("s_mov_b32 m0, %2\n\ts_nop 0\n\tglobal_load_lds_dwordx4 %0, %1" ::"v"(co), "s"(pc), "s"((unsigned)(L_AUX + 2048)) : "memory");
  }
  Lane q;
  q.rot = KROT == 1 ? idx % 5 : KROT == 2 ? (idx & 1) * 2 : 0;
#pragma unroll
  for (int i = 0; i < 8; ++i) {
    const int e = i & 1, tt = (i >> 1) & 1, hh = i >> 2;
    q.xo[i] = (unsigned)((16 * e + 4 * g + ((lane >> 3) & 1) + 2 * tt) * 1280 + hh * 128 + (((lane & 7) ^ (4 * e + g)) * 16));
  }
  q.Wb = (cg < 8 ? p.wA : p.wB) + (cg & 7) * 160 + (size_t)w * 32 * 3840;
  q.xb = p.x + (size_t)(rt * 128 + w * 32) * 1280;
  q.lw = (unsigned)(w * UNIT);
  q.lx = (unsigned)(w * 16384);
  sfor<0, 8>([&](auto I) { load_x<0, I>(q); });
  STAMP(12);
#pragma unroll
  for (int i = 0; i < 5; ++i) {
    const int c = i * 64 + lane, rho = c / 10, cc = c % 10;
    q.woff[i] = (unsigned)((8 * ((rho >> 2) & 3) + 4 * (rho >> 4) + (rho & 3)) * 3840 + cc * 16);
  }
  sfor<0, 5>([&](auto I) { load_w<0, I>(q); });
  {
    const char* ab = sel4(w, p.gw, p.gw + 1024, (const char*)p.out, (const char*)p.ws);
    const unsigned ao = (unsigned)((w == 1 ? r : lane) * sel4(w, 16, 16, 40960, 163840));
    asm volatile("s_mov_b32 m0, %2\n\ts_nop 0\n\tglobal_load_lds_dwordx4 %0, %1 sc1" ::"v"(ao), "s"(ab), "s"((unsigned)(L_AUX + w * 1024)) : "memory");
  }
  sfor<0, 5>([&](auto I) { load_w<1, I>(q); });
  sfor<0, 8>([&](auto I) { load_x<1, I>(q); });
  STAMP(13);
  const __amdgpu_buffer_rsrc_t rs = __builtin_amdgcn_make_buffer_rsrc((void*)p.ws, 0, W_BYTES, 0x00020000);
  u64 tag = 0;
  v4u wo[50];
  if (cg < 8) phase1<false>(p, q, L, rs, b, rt, cg, w, lane, c2, tag, wo);
  else phase1<true>(p, q, L, rs, b, rt, cg - 8, w, lane, c2, tag, wo);
  phase2(p, L, rs, b, w, lane, jt, c2, tag, wo);
  asm volatile("fk_end:");
}

extern "C" void kernel_launch(void* const* d_in, const int* in_sizes, int n_in, void* d_out, int out_size, void* d_ws, size_t ws_size,
                              hipStream_t stream) {
  Params p;
  p.x = (const char*)d_in[0];
  p.wA = (const char*)d_in[1];
  p.woA = (const char*)d_in[2];
  p.wB = (const char*)d_in[4];
  p.woB = (const char*)d_in[5];
  p.gw = (const char*)d_in[6];
  p.out = (char*)d_out;
  p.ws = (char*)d_ws;
  (void)hipFuncSetAttribute((const void*)fused_kernel, hipFuncAttributeMaxDynamicSharedMemorySize, LDS_BYTES);
  fused_kernel<<<256, 256, LDS_BYTES, stream>>>(p);
}
```

```cpp
#include <hip/hip_runtime.h>
#include <stdint.h>
#include <type_traits>

#define LAS __attribute__((address_space(3)))
#define GAS __attribute__((address_space(1)))
typedef short s16x4 __attribute__((ext_vector_type(4)));
typedef unsigned v4u __attribute__((ext_vector_type(4)));
typedef unsigned v2u __attribute__((ext_vector_type(2)));
typedef float f32x4 __attribute__((ext_vector_type(4)));
typedef float f32x2 __attribute__((ext_vector_type(2)));
typedef __bf16 bf2 __attribute__((ext_vector_type(2)));
typedef unsigned long long u64;

#define P2CHK 50

constexpr int UNIT = 5120, SLOT = 4 * UNIT;
constexpr int L_XST = 4 * SLOT;
constexpr int L_SCR = L_XST;
constexpr int L_SLOT4 = L_SCR + 40960;
constexpr int L_PACK = L_XST + 65536;
constexpr int L_AUX = L_PACK + 2 * UNIT;
constexpr int LDS_BYTES = 163840;
static_assert(L_SLOT4 + SLOT <= L_PACK && L_AUX + 4096 <= LDS_BYTES, "lds map");
constexpr int slot_of(int s) { return s < 8 ? (s & 3) : (s - 4) % 5; }
constexpr int slot_base(int s) { return slot_of(s) == 4 ? L_SLOT4 : slot_of(s) * SLOT; }
constexpr int W_EPOCH = 0;
constexpr int W_FE = 131072, W_FO = 2 * 131072, W_F1 = 3 * 131072;
constexpr int W_PART = 1 << 20, PART_STR = 4096;
constexpr int W_HALO = 2 << 20, HALO_STR = 10240;
constexpr int W_WOP = 4 << 20;
constexpr int W_OB = 6 << 20;
constexpr int W_BYTES = W_OB + 128 * 40 * 1024;
constexpr int SC1 = 16;
constexpr unsigned OOB = 0x40000000u;
constexpr unsigned SPIN_CAP = 1u << 16;

struct Params {
  const char *x, *wA, *woA, *wB, *woB, *gw;
  char *out, *ws;
};

template <int I, int N, class F> __device__ __forceinline__ void sfor(F&& f) {
  if constexpr (I < N) { f(std::integral_constant<int, I>{}); sfor<I + 1, N>(f); }
}
template <int I, int N, class F> __device__ __forceinline__ void sfor2(F&& f) {
  if constexpr (N - I <= 20) sfor<I, N>(f);
  else { sfor<I, I + 20>(f); sfor2<I + 20, N>(f); }
}
#define IC(n) std::integral_constant<int, (n)>{}
__device__ __forceinline__ unsigned pk2(float a, float b) {
  f32x2 t = {a, b};
  return __builtin_bit_cast(unsigned, __builtin_convertvector(t, bf2));
}
#define MF "v_mfma_f32_16x16x32_bf16 "
__device__ __forceinline__ void mm_va(f32x4& d, v4u v, v4u a) { asm volatile(MF "%0, %1, %2, %0" : "+v"(d) : "v"(v), "a"(a)); }
__device__ __forceinline__ void mm_av(f32x4& d, v4u a, v4u v) { asm volatile(MF "%0, %1, %2, %0" : "+v"(d) : "a"(a), "v"(v)); }
__device__ __forceinline__ void mm_vv(f32x4& d, v4u a, v4u b) { asm volatile(MF "%0, %1, %2, %0" : "+v"(d) : "v"(a), "v"(b)); }
__device__ __forceinline__ void mm_va0(f32x4& d, v4u v, v4u a) { asm volatile(MF "%0, %1, %2, 0" : "=&v"(d) : "v"(v), "a"(a)); }
__device__ __forceinline__ void mm_av0(f32x4& d, v4u a, v4u v) { asm volatile(MF "%0, %1, %2, 0" : "=&v"(d) : "a"(a), "v"(v)); }
__device__ __forceinline__ void mm_vv0(f32x4& d, v4u a, v4u b) { asm volatile(MF "%0, %1, %2, 0" : "=&v"(d) : "v"(a), "v"(b)); }
__device__ __forceinline__ void mm_vac(f32x4& d, v4u v, v4u a, f32x4 c) { asm volatile(MF "%0, %1, %2, %3" : "=&v"(d) : "v"(v), "a"(a), "v"(c)); }
__device__ __forceinline__ void mm_avc(f32x4& d, v4u a, v4u v, f32x4 c) { asm volatile(MF "%0, %1, %2, %3" : "=&v"(d) : "a"(a), "v"(v), "v"(c)); }
__device__ __forceinline__ void mm_vvc(f32x4& d, v4u a, v4u b, f32x4 c) { asm volatile(MF "%0, %1, %2, %3" : "=&v"(d) : "v"(a), "v"(b), "v"(c)); }
#define NOPS(n) asm volatile("s_nop " #n)
#define PIN(x) asm volatile("" : "+v"(x))

template <int IMM> __device__ __forceinline__ void dma16(const char* sbase, unsigned voff, unsigned ldsbase) {
  asm volatile("s_add_u32 m0, %2, %3\n\ts_nop 0\n\tglobal_load_lds_dwordx4 %0, %1" ::"v"(voff), "s"(sbase), "s"(ldsbase), "i"(IMM) : "memory", "scc");
}
template <int IMM> __device__ __forceinline__ void dma_m0(unsigned ldsbase) { asm volatile("s_add_u32 m0, %0, %1" ::"s"(ldsbase), "i"(IMM) : "memory", "scc"); }
__device__ __forceinline__ void dma_go(const char* sbase, unsigned voff) { asm volatile("global_load_lds_dwordx4 %0, %1" ::"v"(voff), "s"(sbase) : "memory"); }
template <int OFF> __device__ __forceinline__ void dma_sc1(const char* sbase, unsigned voff) {
  asm volatile("global_load_lds_dwordx4 %0, %1 offset:%2 sc1" ::"v"(voff), "s"(sbase), "i"(OFF) : "memory");
}
template <int N> __device__ __forceinline__ void wait_vm() { asm volatile("s_waitcnt vmcnt(%0)" ::"i"(N) : "memory"); }
template <int N> __device__ __forceinline__ void wait_vm_p(u64& v) { asm volatile("s_waitcnt vmcnt(%1)" : "+v"(v) : "i"(N) : "memory"); }
__device__ __forceinline__ void wait_lgkm() { asm volatile("s_waitcnt lgkmcnt(0)" ::: "memory"); }
__device__ __forceinline__ v2u trread(LAS char* p) {
  return __builtin_bit_cast(v2u, __builtin_amdgcn_ds_read_tr16_b64_v4i16((LAS s16x4*)p));
}
#define SB0() __builtin_amdgcn_sched_barrier(0)
#define BAR() __builtin_amdgcn_s_barrier()
#define LD128S(voff, soff) __builtin_amdgcn_raw_buffer_load_b128(rs, (voff), (soff), SC1)
#define ST128(v, off) __builtin_amdgcn_raw_buffer_store_b128((v), rs, (off), 0, SC1)
#define STAMP(i) do {} while (0)

__device__ __forceinline__ u64 ld_flag(char* ws, unsigned off) {
  return __hip_atomic_load((GAS u64*)(ws + off), __ATOMIC_RELAXED, __HIP_MEMORY_SCOPE_AGENT);
}
__device__ __forceinline__ void poll_async(u64& v, char* ws, unsigned off) {
  asm volatile("global_load_dwordx2 %0, %1, %2 sc1" : "+v"(v) : "v"(off), "s"(ws) : "memory");
}
__device__ __forceinline__ void settle_flags(char* ws, unsigned off, u64 tag, u64 v) {
  if (!__all(v == tag)) {
    unsigned spins = 0;
    bool ok;
#pragma unroll 1
    do { ok = __all(ld_flag(ws, off) == tag); } while (!ok && ++spins < SPIN_CAP);
  }
  __builtin_amdgcn_fence(__ATOMIC_ACQUIRE, "wavefront");
}
template <class T> __device__ __forceinline__ T sel4(int i, T a, T b, T c, T d) {
  const bool lo = (i & 1) != 0, hi = (i & 2) != 0;
  const T x = lo ? b : a, y = lo ? d : c;
  return hi ? y : x;
}
__device__ __forceinline__ float asf(unsigned u) { return __builtin_bit_cast(float, u); }
__device__ __forceinline__ unsigned asu(float f) { return __builtin_bit_cast(unsigned, f); }
#define vmax(a, b) __builtin_amdgcn_fmed3f((a), (b), vinf)
__device__ __forceinline__ float vmax3(float a, float b, float c) { float r; asm("v_max3_f32 %0, %1, %2, %3" : "=v"(r) : "v"(a), "v"(b), "v"(c)); return r; }
constexpr float EPS = 1e-6f, LOG2E = 1.4426950408889634f;
__device__ __forceinline__ float phi_a(float t) { return __builtin_fmaf(t, LOG2E, -EPS * LOG2E); }
__device__ __forceinline__ float phi_b(float u) { return __builtin_amdgcn_fmed3f(__builtin_amdgcn_exp2f(u), 0.f, 1.f); }
#define phi_c(t) vmax((t), EPS)

constexpr int fr_k(int f) { return f >= 240 ? 8 + (f - 240) % 12 : (f >= 180 && f < 200) ? 16 + (f - 180) % 4 : (f % 100) / 5; }
constexpr int fr_j(int f) { return f >= 240 ? ((f - 240) / 12 + 4) % 5 : (f >= 180 && f < 200) ? ((f - 180) / 4 + 4) % 5 : f % 5; }
constexpr int AHEAD = 8, RING = 12;

struct Lane {
  unsigned woff[5], xo[8];
  const char *Wb, *xb;
  unsigned lw, lx;
  int rot;
};
__device__ __forceinline__ int rot5(int rot, int pos) { const int k = pos + rot; return k >= 5 ? k - 5 : k; }
template <int S> __device__ __forceinline__ const char* w_src(const Lane& q) {
  return q.Wb + (S / 5 == 0 ? 1280 : S / 5 == 1 ? 2560 : 0) + (size_t)rot5(q.rot, S % 5) * (128 * 3840);
}
template <int S, int I> __device__ __forceinline__ void load_w(const Lane& q) { dma16<slot_base(S) + I * 1024>(w_src<S>(q), q.woff[I], q.lw); }
template <int S, int I> __device__ __forceinline__ void load_x(const Lane& q) {
  dma16<L_XST + (S & 1) * 8192 + I * 1024>(q.xb + rot5(q.rot, S) * 256, q.xo[I], q.lx);
}
__device__ __forceinline__ unsigned fo_of(int jt, int w, int lane, int br) {
  return W_FO + (((jt >> 1) * 16 + br * 8 + 2 * w + (lane & 1)) * 4 + 2 * (jt & 1) + ((lane >> 1) & 1)) * 128;
}
constexpr int wo_hb(int z) { return z / 5 < 6 ? ((z / 5) & 1) : (z / 5 < 8 ? 0 : 1); }
constexpr int wo_i(int z) { return z / 5 < 6 ? (z / 10) : (z / 5 < 8 ? z / 5 - 3 : z / 5 - 5); }
constexpr int wo_idx(int z) { return wo_hb(z) * 25 + wo_i(z) * 5 + z % 5; }

template <bool ISB>
__device__ __forceinline__ void phase1(const Params& p, const Lane& q, LAS char* L, __amdgpu_buffer_rsrc_t rs, int b, int rt, int hg, int w, int lane, int c2,
                                       int jt2, int rj, u64& tag, u64& pollB, u64& pollA, v4u (&wo)[50]) {
  const int r = lane & 15, g = lane >> 4;
  const int cg = (ISB ? 8 : 0) + hg;
  const unsigned lw = q.lw;
  auto lbase = [&](int off) { unsigned a = (unsigned)(uintptr_t)L + (unsigned)off; PIN(a); return (LAS char*)(uintptr_t)a; };
  const int o1 = (lane >> 2) * 160 + (lane & 3) * 40, o2 = (lane >> 2) * 160 + (lane & 3) * 8;
  LAS char* a1 = lbase(o1);
  LAS char* a2 = lbase(o2);
  LAS char* a1h = lbase(o1 + 3 * SLOT);
  LAS char* a2h = lbase(o2 + 3 * SLOT);
  LAS char* ax0 = lbase(L_XST + w * 16384 + r * 128 + ((g ^ (r >> 1)) * 16));
  LAS char* ax1 = lbase(L_XST + w * 16384 + r * 128 + (((4 + g) ^ (r >> 1)) * 16));
  LAS char* ag = L + L_AUX + g * 16;
  LAS char *a1s, *a2s;
  const int unit = b + 256 * w;
  unsigned lane16 = lane * 16, fl0 = lane < 8 ? lane * 16u : OOB;
  PIN(lane16); PIN(fl0);

  v4u xf[20][2], wf[RING], gf2[2][4];
  f32x4 acc[3][2][5], gacc[2];
  f32x4 eps4 = {EPS, EPS, EPS, EPS};
  PIN(eps4);
  float vinf = __builtin_inff();
  PIN(vinf);

  auto readW = [&](auto F, auto T) {
    constexpr int f = F, t = T, ks = fr_k(f), jt = fr_j(f), sl = slot_of(f / 100 * 5 + ks / 4);
    constexpr int off = (ks % 4) * UNIT + t * 2560 + (jt == 4 ? 32 : 40 * jt);
    v2u v;
    if constexpr (sl == 4) v = trread((jt == 4 ? a1s : a2s) + off);
    else if constexpr (sl == 3) v = trread((jt == 4 ? a1h : a2h) + off);
    else v = trread((jt == 4 ? a1 : a2) + sl * SLOT + off);
    if constexpr (t == 0) { wf[f % RING].x = v.x; wf[f % RING].y = v.y; }
    else { wf[f % RING].z = v.x; wf[f % RING].w = v.y; }
  };
  auto readX = [&](auto S, auto U) {
    constexpr int s = S, u = U, j = u / 3, k = u % 3;
    if constexpr (k < 2) xf[4 * s + j][k] = *(LAS v4u*)(((j & 1) ? ax1 : ax0) + (s & 1) * 8192 + (j >> 1) * 4096 + k * 2048);
    else gf2[s & 1][j] = *(LAS v4u*)(ag + rot5(q.rot, s) * 256 + j * 64);
  };

  v2u ept;
  v4u tag4;
  u64 poll = 0;
  unsigned poll0_off, poll1_off, st_off;
  LAS char *aW, *aR;
  const int rp = rt > 0 ? rt - 1 : 0;
  {
    const int li = lane < 10 ? lane : 0;
    const int pu = c2 * 40 + (li / 5) * 20 + 5 * w + li % 5;
    poll0_off = W_FE + ((pu & 255) * 4 + (pu >> 8)) * 128;
    poll1_off = ISB ? W_F1 + ((cg * 16 + rp) * 4 + 3) * 128 : W_F1 + ((cg * 16 + r) * 4 + w) * 128;
    PIN(poll0_off); PIN(poll1_off);
  }
  const char* halo_src = p.ws + W_HALO + (hg * 16 + rp) * HALO_STR;
  const int wo_b = W_WOP + (c2 * 40 + 5 * w) * 5 * 1024;
  auto wo_soff = [&](int z) { return wo_b + (wo_hb(z) ? 0 : 102400) + rot5(rj, wo_i(z)) * 5120 + ((z % 5) >> 2) * 4096; };
  auto ld_wo = [&](auto Z) { wo[wo_idx(Z)] = LD128S(lane16 + ((Z % 5) & 3) * 1024, wo_soff(Z)); };
  auto flag = [&](int off) { __builtin_amdgcn_raw_buffer_store_b128(tag4, rs, fl0, off, SC1); };
  constexpr int NWO1 = ISB ? 40 : 32;
  float gate[2];
  v4u Kf[5], Vf[5], Ej[5], K4m[4], V4m, pst, gl[16];
  v2u Pq[2][5];
  f32x4 kvT, kvU, kvW, kvX, kvZ, rd[8];
  float ta[2], tb[2], tc[2];
  v4u kx[5], kpv[5], vx5[5], Vb[2][5], Kc[2], Kp[2], Qf[2], Pb[2][2], V4b[2], ones4;
  v2u q4[2];
  f32x4 biasC, biasP, Sc[2], Sp[2];
  float sm[2][4], mx[2], mt;
  unsigned mC[2], mP[2], pp[2][2];
  f32x4 num[2], den[2], num4[2];
  float inv[2][4];
  v4u F[2][2];

  auto fin_op = [&](auto H, auto Z) {
    constexpr int h = H, z = Z;
    if constexpr (z < 2) { inv[z][h] = __builtin_amdgcn_rcpf(den[z][0]); PIN(inv[z][h]); PIN(den[z]); }
    else if constexpr (z < 4) { inv[z - 2][h] *= gate[z - 2]; PIN(inv[z - 2][h]); }
    else if constexpr (z < 12) { constexpr int t2 = (z - 4) >> 2, e = (z - 4) & 3; float u = num[t2][e] * inv[t2][h]; PIN(u); num[t2][e] = u; }
    else { constexpr int t2 = (z - 12) >> 1, e = (z - 12) & 1; unsigned o = pk2(num[t2][2 * e], num[t2][2 * e + 1]); PIN(o); F[t2][h >> 1][2 * (h & 1) + e] = o; }
  };
  constexpr float CS = 0.22360679774997896f * LOG2E;
  auto att_op = [&](auto H, auto Z) {
    constexpr int h = H, z = Z;
    if constexpr (z < 4) { constexpr int t2 = z >> 1, e = z & 1; unsigned o = pk2(acc[2][t2][h][2 * e], acc[2][t2][h][2 * e + 1]); PIN(o); Qf[t2][e] = o; }
    else if constexpr (z == 4) { PIN(Qf[0]); PIN(Qf[1]); NOPS(1); }
    else if constexpr (z < 9) { constexpr int t2 = (z - 5) >> 1; if constexpr ((z - 5) & 1) mm_vvc(Sp[t2], Kp[t2], Qf[t2], biasP); else mm_vvc(Sc[t2], Kc[t2], Qf[t2], biasC); }
    else if constexpr (z < 17) { constexpr int t2 = (z - 9) >> 2, e = (z - 9) & 3; unsigned o = (r >> 2) == h ? Vb[t2][4][e] : 0u; PIN(o); V4b[t2][e] = o; }
    else if constexpr (z < 25) { constexpr int t2 = (z - 17) >> 2, e = (z - 17) & 3; sm[t2][e] = vmax(Sc[t2][e], Sp[t2][e]); PIN(sm[t2][e]); }
    else if constexpr (z < 27) { mx[z - 25] = vmax3(sm[z - 25][0], sm[z - 25][1], sm[z - 25][2]); PIN(mx[z - 25]); }
    else if constexpr (z < 29) { mx[z - 27] = vmax(mx[z - 27], sm[z - 27][3]); PIN(mx[z - 27]); }
    else if constexpr (z == 29) {
      const auto s = __builtin_amdgcn_permlane16_swap(asu(mx[0]), asu(mx[1]), false, false);
      mt = vmax(asf(s[0]), asf(s[1])); PIN(mt);
    } else if constexpr (z == 30) {
      const auto s = __builtin_amdgcn_permlane32_swap(asu(mt), asu(mt), false, false);
      mt = vmax(asf(s[0]), asf(s[1])) * -CS; PIN(mt);
    } else if constexpr (z == 31) {
      const auto s = __builtin_amdgcn_permlane16_swap(asu(mt), asu(mt), false, false);
      mx[0] = asf(s[0]); mx[1] = asf(s[1]); PIN(mx[0]); PIN(mx[1]);
    }
    else if constexpr (z < 40) { constexpr int t2 = (z - 32) >> 2, e = (z - 32) & 3; sm[t2][e] = __builtin_fmaf(sm[t2][e], CS, mx[t2]); PIN(sm[t2][e]); }
    else if constexpr (z < 48) { constexpr int t2 = (z - 40) >> 2, e = (z - 40) & 3; sm[t2][e] = __builtin_amdgcn_exp2f(sm[t2][e]); PIN(sm[t2][e]); }
    else if constexpr (z < 52) { constexpr int t2 = (z - 48) >> 1, e = (z - 48) & 1; pp[t2][e] = pk2(sm[t2][2 * e], sm[t2][2 * e + 1]); PIN(pp[t2][e]); }
    else if constexpr (z < 60) { constexpr int t2 = (z - 52) >> 2, e = (z - 52) & 3; unsigned o = pp[t2][e & 1] & (e < 2 ? mP[e & 1] : mC[e & 1]); PIN(o); Pb[h & 1][t2][e] = o; }
    else if constexpr (z == 60) { PIN(Pb[h & 1][0]); PIN(Pb[h & 1][1]); PIN(V4b[0]); PIN(V4b[1]); PIN(Vb[0][h]); PIN(Vb[1][h]); NOPS(1); }
    else if constexpr (z < 63) mm_vv0(den[z - 61], ones4, Pb[h & 1][z - 61]);
    else if constexpr (z < 65) mm_vv0(num[z - 63], Vb[z - 63][h], Pb[h & 1][z - 63]);
    else if constexpr (z < 67) { if constexpr (h == 0) mm_vv0(num4[z - 65], V4b[z - 65], Pb[h & 1][z - 65]); else mm_vv(num4[z - 65], V4b[z - 65], Pb[h & 1][z - 65]); }
  };
  auto pv_op = [&](auto H, auto U) {
    constexpr int u = U;
    if constexpr (u >= 6 && u < 10) { att_op(H, IC(9 + 2 * (u - 6))); att_op(H, IC(10 + 2 * (u - 6))); }
    if constexpr (u == 10) { att_op(H, IC(60)); att_op(H, IC(61)); }
    if constexpr (u >= 11 && u < 16) att_op(H, IC(51 + u));
    if constexpr (u >= 16) { fin_op(H, IC(2 * (u - 16))); fin_op(H, IC(2 * (u - 16) + 1)); }
  };
  auto att_pre = [&](auto H, auto Z) {
    constexpr int h = H, z = Z;
    if constexpr (z < 2) { Kc[z] = v4u{kx[h][2 * z], kx[h][2 * z + 1], kx[4][2 * z], kx[4][2 * z + 1]}; PIN(Kc[z]); }
    else if constexpr (z < 4) { constexpr int t2 = z - 2; Kp[t2] = v4u{kpv[h][2 * t2], kpv[h][2 * t2 + 1], kpv[4][2 * t2], kpv[4][2 * t2 + 1]}; PIN(Kp[t2]); }
    else { constexpr int t2 = z - 4; unsigned lo = g == h ? q4[t2].x : 0u, hi = g == h ? q4[t2].y : 0u; PIN(lo); PIN(hi); Qf[t2].z = lo; Qf[t2].w = hi; }
  };
  auto phi_op = [&](auto Z, const f32x4& t, auto H, unsigned& out) {
    constexpr int z = Z, h = H;
    if constexpr (z < 2) { ta[z] = phi_a(t[2 * h + z]); PIN(ta[z]); }
    else if constexpr (z < 4) { tb[z - 2] = phi_b(ta[z - 2]); PIN(tb[z - 2]); }
    else if constexpr (z < 6) { tc[z - 4] = phi_c(t[2 * h + z - 4]); PIN(tc[z - 4]); }
    else if constexpr (z < 8) { tc[z - 6] += tb[z - 6]; PIN(tc[z - 6]); }
    else { out = pk2(tc[0], tc[1]); PIN(out); }
  };

  wait_vm<13>();
  BAR();
  STAMP(1);
  ept = *(LAS v2u*)(L + L_AUX + 3072);
  sfor<0, 3>([&](auto U) { readX(IC(0), U); });
  sfor<0, AHEAD>([&](auto F_) { readW(F_, IC(0)); readW(F_, IC(1)); });
  sfor<3, 12>([&](auto U) { readX(IC(0), U); });
  tag = (((u64)(unsigned)__builtin_amdgcn_readfirstlane(ept.y) << 32) | (unsigned)__builtin_amdgcn_readfirstlane(ept.x)) + 1ull;
  tag4 = v4u{(unsigned)tag, (unsigned)(tag >> 32), (unsigned)tag, (unsigned)(tag >> 32)};
  PIN(tag4);
  SB0();

  sfor2<0, 600>([&](auto M) {
    constexpr int m = M, f = m / 2, tt = m & 1, ks = fr_k(f), jt = fr_j(f), ch = f / 100;
    constexpr int sb = (m + 16) / 40 - 1, mb = (m + 16) % 40;
    constexpr int tv = (m - 368) / 8, uv = (m - 368) % 8, jv = (tv + 4) % 5;
    constexpr int tq = (m - 504) / 24, uq = (m - 504) % 24, jq = (tq + 4) % 5;
    if constexpr (mb == 0 && sb >= 0 && sb <= 11) {
      constexpr int cntA[12] = {13, 13, 13, 5, 5, 10, 5, 13, 16, 16, 16, 6};
      constexpr int cntB[12] = {13, 13, 13, 5, 5, 10, 5, 8, 16, 8, 16, 5};
      constexpr int cnt = ISB ? cntB[sb] : cntA[sb];
      wait_vm_p<cnt>(poll);
      BAR();
      SB0();
    }
    if constexpr (ISB && m == 552) { wait_vm<8>(); BAR(); SB0(); }
    if constexpr (m % 8 == 0 && m > 0 && f + AHEAD <= 300) { __builtin_amdgcn_s_waitcnt(0xC07F | (8 << 8)); SB0(); }
    {
      f32x4& d = acc[ch][tt][jt];
      const v4u& wv = wf[f % RING];
      if constexpr (ch == 2 || (ch == 0 && ISB)) {
        if constexpr (ks != 0) mm_va(d, wv, xf[ks][tt]);
        else if constexpr (ISB) mm_va0(d, wv, xf[ks][tt]);
        else mm_vac(d, wv, xf[ks][tt], eps4);
      } else {
        if constexpr (ks != 0) mm_av(d, xf[ks][tt], wv);
        else if constexpr (ch == 0) mm_avc(d, xf[ks][tt], wv, eps4);
        else mm_av0(d, xf[ks][tt], wv);
      }
    }
    SB0();
    if constexpr (f + AHEAD < 300) readW(IC(f + AHEAD < 300 ? f + AHEAD : 0), IC(tt));
    if constexpr (sb >= 0 && sb <= 3 && mb < 12) readX(IC(sb + 1), IC(mb));
    SB0();
    if constexpr (m < 200 && m % 10 == 9) {
      if constexpr (ks == 0) { mm_va0(gacc[0], gf2[0][0], xf[0][0]); mm_va0(gacc[1], gf2[0][0], xf[0][1]); }
      else { mm_va(gacc[0], gf2[(ks >> 2) & 1][ks & 3], xf[ks][0]); mm_va(gacc[1], gf2[(ks >> 2) & 1][ks & 3], xf[ks][1]); }
    }
    auto split_w = [&](auto S, auto P0, auto ST) {
      constexpr int p0 = P0, st = ST;
      if constexpr (mb >= p0 && (mb - p0) / st < 5) {
        if constexpr ((mb - p0) % st == 0) dma_m0<slot_base(S) + (mb - p0) / st * 1024>(lw);
        if constexpr ((mb - p0) % st == 1) dma_go(w_src<S>(q), q.woff[(mb - p0) / st]);
      }
    };
    if constexpr (m < 5) load_w<2, m>(q);
    if constexpr (m < 104 && m % 40 >= 8 && m % 40 <= 22 && m % 2 == 0) load_x<m / 40 + 2, (m % 40 - 8) / 2>(q);
    if constexpr (sb >= 0 && sb <= 1 && mb >= 12 && mb <= 20 && mb % 2 == 0) load_w<sb + 3, (mb - 12) / 2>(q);
    if constexpr (sb == 2 || sb == 3) split_w(IC(sb + 3), IC(13), IC(4));
    if constexpr (sb == 4) { split_w(IC(7), IC(2), IC(4)); split_w(IC(8), IC(21), IC(4)); }
    if constexpr (sb >= 5 && sb <= 9) split_w(IC(sb + 4), IC(2), IC(8));
    if constexpr (sb == 10) split_w(IC(14), IC(1), IC(3));
    if constexpr (m == 156) {
      if (unit < 320) {
        v4u pv[5];
        sfor<0, 5>([&](auto J) {
          const v2u lo = trread(a1 + L_PACK + w * UNIT + 8 * J), hi = trread(a1 + L_PACK + w * UNIT + 2560 + 8 * J);
          pv[J] = v4u{lo.x, lo.y, hi.x, hi.y};
        });
        sfor<0, 5>([&](auto J) { ST128(pv[J], lane16 + W_WOP + (unit * 5 + J) * 1024); });
      }
    }
    if constexpr (m == 185) flag(W_FE + (b * 4 + w) * 128);
    if constexpr (m == 225) poll_async(poll, p.ws, poll0_off);
    if constexpr (m == 265) settle_flags(p.ws, poll0_off, tag, poll);
    if constexpr (m == 45) { a1s = lbase(o1 + L_SLOT4); a2s = lbase(o2 + L_SLOT4); }
    if constexpr (m == 47) {
      aW = lbase(ISB ? L_SCR + ((w + 1) & 3) * 10240 + lane * 16 : L_SCR + w * 8192 + lane * 16);
      aR = lbase(ISB ? L_SCR + w * 10240 + lane * 16 : L_SCR + w * 2048 + lane * 16);
      st_off = ISB ? W_HALO + (hg * 16 + rt) * HALO_STR + lane * 16 : W_PART + (hg * 16 + rt) * PART_STR + w * 1024 + lane * 16;
      PIN(st_off);
    }
    if constexpr (m == 206) { PIN(gacc[0]); PIN(gacc[1]); }
    if constexpr (m == 483 || m == 487) {
      constexpr int t2 = (m - 483) / 4;
      gate[t2] = __builtin_amdgcn_rcpf(1.f + __builtin_amdgcn_exp2f(gacc[t2][0] * (ISB ? LOG2E : -LOG2E)));
      PIN(gate[t2]);
    }
    if constexpr (sb >= 6 && sb <= 9 && (mb == 6 || mb == 14 || mb == 30)) ld_wo(IC((sb - 6) * 3 + (mb == 6 ? 0 : mb == 14 ? 1 : 2)));
    if constexpr (m >= 445 && (m - 445) % 4 == 0 && 12 + (m - 445) / 4 < NWO1) ld_wo(IC(12 + (m - 445) / 4));

    if constexpr (!ISB) {
      if constexpr (m >= 49 && m < 59 && m % 2 == 1) { const unsigned one = (r == (m - 49) / 2) ? 0x3F803F80u : 0u; Ej[(m - 49) / 2] = v4u{one, one, one, one}; PIN(Ej[(m - 49) / 2]); }
      if constexpr (m >= 202 && m < 382) {
        constexpr int z = m - 202, kt = (z / 36 + 4) % 5, pr = z % 36 / 9, t2 = pr >> 1, h = pr & 1;
        unsigned o = 0;
        phi_op(IC(z % 9), acc[0][t2][kt], IC(h), o);
        if constexpr (z % 9 == 8) Kf[kt][2 * t2 + h] = o;
      }
      if constexpr (m >= 240 && m < 386 && (m - 240) % 36 == 0) {
        constexpr int kt = ((m - 240) / 36 + 4) % 5;
        PIN(Kf[kt]); NOPS(1);
        if constexpr (m == 240) mm_vv0(kvZ, Kf[kt], Ej[kt]); else mm_vv(kvZ, Kf[kt], Ej[kt]);
      }
      if constexpr (m >= 244 && m < 372 && (m - 244) % 8 == 0) {
        constexpr int z = (m - 244) / 8;
        unsigned o = (r >> 2) == (z >> 2) ? Kf[4][z & 3] : 0u;
        PIN(o);
        K4m[z >> 2][z & 3] = o;
      }
      LAS f32x4* red = (LAS f32x4*)aW;
      if constexpr (m >= 368 && tv < 5) {
        if constexpr (uv == 3 || uv == 4) {
          constexpr int t2 = uv - 3;
          unsigned lo = pk2(acc[1][t2][jv][0], acc[1][t2][jv][1]), hi = pk2(acc[1][t2][jv][2], acc[1][t2][jv][3]);
          PIN(lo); PIN(hi);
          Vf[jv][2 * t2] = lo; Vf[jv][2 * t2 + 1] = hi;
        }
        if constexpr (uv == 5) { PIN(Vf[jv]); if constexpr (tv > 0) PIN(K4m[jv & 3]); }
        if constexpr (tv == 0) { if constexpr (uv == 6) mm_vv0(kvX, Kf[4], Vf[4]); }
        else {
          if constexpr (uv < 2) {
            unsigned lo = (r >> 2) == jv ? Vf[4][2 * uv] : 0u, hi = (r >> 2) == jv ? Vf[4][2 * uv + 1] : 0u;
            PIN(lo); PIN(hi);
            V4m[2 * uv] = lo; V4m[2 * uv + 1] = hi;
          }
          if constexpr (uv == 2) { PIN(V4m); if constexpr (tv == 1) red[6 * 64] = kvX; else red[(jv - 1) * 64] = kvT; }
          if constexpr (uv == 3) { if constexpr (tv == 1) mm_vv0(kvW, Kf[jv], V4m); else mm_vv(kvW, Kf[jv], V4m); }
          if constexpr (uv == 6) mm_vv0(kvT, Kf[jv], Vf[jv]);
          if constexpr (uv == 7) { if constexpr (tv == 1) mm_vv0(kvU, K4m[jv & 3], Vf[jv]); else mm_vv(kvU, K4m[jv & 3], Vf[jv]); }
        }
      }
      if constexpr (m == 397) red[7 * 64] = kvZ;
      if constexpr (m == 409) red[5 * 64] = kvW;
      if constexpr (m == 412) red[3 * 64] = kvT;
      if constexpr (m == 415) red[4 * 64] = kvU;
      if constexpr (m == 423) asm volatile("s_waitcnt lgkmcnt(8)" ::: "memory");
      if constexpr (m >= 424 && m < 432) rd[m - 424] = *(LAS f32x4*)(aR + ((m - 424) >> 1) * 8192 + ((m - 424) & 1) * 1024);
      if constexpr (m >= 433 && m < 441) {
        sfor<0, 3>([&](auto E) {
          constexpr int z = 3 * (m - 433) + E, qq = z >> 2, e = z & 3;
          float s = rd[qq & 1][e] + rd[2 + qq][e]; PIN(s);
          rd[qq & 1][e] = s;
        });
      }
      if constexpr (m == 441 || m == 442) {
        unsigned lo = pk2(rd[m - 441][0], rd[m - 441][1]), hi = pk2(rd[m - 441][2], rd[m - 441][3]);
        PIN(lo); PIN(hi);
        pst[2 * (m - 441)] = lo; pst[2 * (m - 441) + 1] = hi;
      }
      if constexpr (m == 444) ST128(pst, st_off);
      if constexpr (m == 494) { wait_vm<13>(); flag(W_F1 + ((cg * 16 + rt) * 4 + w) * 128); }
      if constexpr (m == 523) poll = ld_flag(p.ws, poll1_off);
      if constexpr (m == 562) settle_flags(p.ws, poll1_off, tag, poll);
      if constexpr (m >= 563 && m < 595 && m % 2 == 1) gl[(m - 563) / 2] = LD128S(lane16, W_PART + (hg * 16 + (m - 563) / 2) * PART_STR + w * 1024);
      if constexpr (m >= 504 && uq >= 4 && uq < 22) {
        sfor<0, 2>([&](auto E) {
          constexpr int z = 2 * (uq - 4) + E, pr = z / 9, t2 = pr >> 1, h = pr & 1;
          unsigned o = 0;
          phi_op(IC(z % 9), acc[2][t2][jq], IC(h), o);
          if constexpr (z % 9 == 8) Pq[t2][jq][h] = o;
        });
      }
    } else {
      if constexpr (m >= 49 && m < 57 && m % 2 == 1) {
        constexpr int e = (m - 49) / 2;
        const int key = 4 * g + e;
        float bc = (key <= r && key + 7 >= r) ? 0.f : -1e30f, bp = (key >= r + 9 && (rt > 0 || w > 0)) ? 0.f : -1e30f;
        PIN(bc); PIN(bp);
        biasC[e] = bc; biasP[e] = bp;
      }
      if constexpr (m == 57 || m == 59) {
        constexpr int e = (m - 57) / 2;
        mC[e] = (4 * g + 2 * e <= r ? 0xFFFFu : 0u) | (4 * g + 2 * e + 1 <= r ? 0xFFFF0000u : 0u);
        mP[e] = ~mC[e];
        PIN(mC[e]); PIN(mP[e]);
      }
      if constexpr (m == 61) { ones4 = v4u{0x3F803F80u, 0x3F803F80u, 0x3F803F80u, 0x3F803F80u}; PIN(ones4); PIN(biasC); PIN(biasP); }
      if constexpr (m >= 228 && m < 248) {
        constexpr int z = m - 228, kt = z >> 2, t2 = (z >> 1) & 1, e = z & 1;
        unsigned o = pk2(acc[0][t2][kt][2 * e], acc[0][t2][kt][2 * e + 1]);
        PIN(o);
        kx[kt][2 * t2 + e] = o;
      }
      if constexpr (m >= 249 && m < 264 && (m - 249) % 3 == 0) *(LAS v4u*)(aW + (m - 249) / 3 * 1024) = kx[(m - 249) / 3];
      if constexpr (m == 265) { if (w == 3) sfor<0, 5>([&](auto J) { ST128(kx[J], st_off + J * 1024); }); }
      if constexpr (m >= 368 && tv < 5) {
        if constexpr (uv == 3 || uv == 4) {
          constexpr int t2 = uv - 3;
          unsigned lo = pk2(acc[1][t2][jv][0], acc[1][t2][jv][1]), hi = pk2(acc[1][t2][jv][2], acc[1][t2][jv][3]);
          PIN(lo); PIN(hi);
          vx5[jv][2 * t2] = lo; vx5[jv][2 * t2 + 1] = hi;
        }
        if constexpr (uv == 6) *(LAS v4u*)(aW + (5 + jv) * 1024) = vx5[jv];
      }
      if constexpr (m == 408) { if (w == 3) sfor<0, 5>([&](auto J) { ST128(vx5[J], st_off + (5 + J) * 1024); }); }
      if constexpr (m >= 410 && m < 420) { constexpr int j = (m - 410) >> 1; Vb[tt][j].z = vx5[j][2 * tt]; Vb[tt][j].w = vx5[j][2 * tt + 1]; }
      if constexpr (m == 305) flag(W_F1 + ((cg * 16 + rt) * 4 + w) * 128);
      if constexpr (m == 345) poll_async(poll, p.ws, poll1_off);
      if constexpr (m == 384) {
        settle_flags(p.ws, poll1_off, tag, poll);
        dma_m0<L_SCR>((unsigned)(w * 1024)); NOPS(0); dma_sc1<0>(halo_src + w * 1024, lane16);
        dma_m0<L_SCR + 4096>(0u); NOPS(0); dma_sc1<0>(halo_src + 4096, lane16);
      }
      if constexpr (m == 466) flag(W_F1 + 131072 + ((cg * 16 + rt) * 4 + w) * 128);
      if constexpr (m == 490) poll = ld_flag(p.ws + 131072, poll1_off);
      if constexpr (m == 520) {
        settle_flags(p.ws + 131072, poll1_off, tag, poll);
        dma_m0<L_SCR + 5120>((unsigned)(w * 1024)); NOPS(0); dma_sc1<0>(halo_src + 5120 + w * 1024, lane16);
        dma_m0<L_SCR + 9216>(0u); NOPS(0); dma_sc1<0>(halo_src + 9216, lane16);
      }
      if constexpr (m >= 491 && m < 496) kpv[m - 491] = *(LAS v4u*)(aR + (m - 491) * 1024);
      if constexpr (m >= 552 && m < 562) {
        constexpr int j = ((m - 552) / 2 + 4) % 5;
        const v2u v = *(LAS v2u*)(aR + (5 + j) * 1024 + tt * 8);
        Vb[tt][j].x = v.x; Vb[tt][j].y = v.y;
      }
      if constexpr (m == 509 || m == 511) {
        constexpr int t2 = (m - 509) / 2;
        q4[t2] = v2u{pk2(acc[2][t2][4][0], acc[2][t2][4][1]), pk2(acc[2][t2][4][2], acc[2][t2][4][3])};
        PIN(q4[t2]);
      }
      if constexpr (m >= 513 && m < 525 && m % 2 == 1) att_pre(IC(0), IC((m - 513) / 2));
      if constexpr (m >= 528) {
        constexpr int h = tq - 1;
        constexpr int z0[25] = {0, 0, 0, 0, 2, 5, 7, 9, 9, 9, 9, 13, 17, 21, 22, 23, 24, 28, 32, 35, 38, 41, 46, 52, 52};
        sfor<z0[uq], z0[uq + 1]>([&](auto Z) { att_op(IC(h), IC(Z < 9 ? Z : Z + 8)); });
        if constexpr (uq >= 7 && uq < 10) { att_pre(IC(h + 1), IC(2 * (uq - 7))); att_pre(IC(h + 1), IC(2 * (uq - 7) + 1)); }
        if constexpr (h > 0) pv_op(IC(h > 0 ? h - 1 : 0), IC(uq));
      }
    }
    SB0();
    if constexpr (m == 199) STAMP(2);
    if constexpr (m == 399) STAMP(3);
  });
  STAMP(4);
  constexpr int WO_PRE = ISB ? 46 : 40;
  sfor<NWO1, WO_PRE>([&](auto Z) { ld_wo(Z); });
  NOPS(3);
  SB0();
  if constexpr (!ISB) {
    sfor<0, 36>([&](auto Z) {
      constexpr int z = Z, pr = z / 9, t2 = pr >> 1, h = pr & 1;
      unsigned o = 0;
      phi_op(IC(z % 9), acc[2][t2][3], IC(h), o);
      if constexpr (z % 9 == 8) Pq[t2][3][h] = o;
    });
    v4u Pf[2][4];
#pragma unroll
    for (int h = 0; h < 4; ++h)
#pragma unroll
      for (int tt = 0; tt < 2; ++tt) { Pf[tt][h] = v4u{Pq[tt][h].x, Pq[tt][h].y, Pq[tt][4].x, Pq[tt][4].y}; PIN(Pf[tt][h]); }
    const unsigned sv = (g == (r >> 2)) ? ((r & 1) ? 0x3F800000u : 0x00003F80u) : 0u;
    const unsigned s0_ = (r & 2) ? 0u : sv, s1_ = (r & 2) ? sv : 0u;
    v4u A1 = {s0_, s1_, 0u, 0u}, A2 = {0u, 0u, s0_, s1_};
    LAS v2u* kvs = (LAS v2u*)lbase(L_SCR + 32768 + lane * 8);
    LAS v2u* kvw = (LAS v2u*)lbase(L_SCR + 32768 + w * 1024 + lane * 8);
    LAS v2u* kvz = (LAS v2u*)lbase(L_SCR + 32768 + (448 + g * 16) * 8);
    PIN(A1); PIN(A2); NOPS(1);
    SB0();
    f32x4 d1, d2;
#pragma unroll
    for (int i = 0; i < 16; ++i) {
      if (i == 0) { mm_vv0(d1, A1, gl[0]); mm_vv0(d2, A2, gl[0]); }
      else { mm_vv(d1, A1, gl[i]); mm_vv(d2, A2, gl[i]); }
    }
    NOPS(7);
    SB0();
    kvw[0] = v2u{pk2(d1[0], d1[1]), pk2(d1[2], d1[3])};
    kvw[64] = v2u{pk2(d2[0], d2[1]), pk2(d2[2], d2[3])};
    wait_lgkm();
    BAR();
    STAMP(5);
    const v2u uu = kvs[256], ww = kvs[320], xx = kvs[384], z4 = kvz[4];
    v4u Nh[4], N4[4], Zh[4];
    v2u tl4[4], zl4[4];
#pragma unroll
    for (int h = 0; h < 4; ++h) { zl4[h] = kvz[h]; tl4[h] = kvs[h * 64]; }
    SB0();
#pragma unroll
    for (int h = 0; h < 4; ++h) {
      const v2u tl = tl4[h], zl = zl4[h];
      const bool gh = g == h, rh = (r >> 2) == h;
      Nh[h] = v4u{tl.x, tl.y, gh ? uu.x : 0u, gh ? uu.y : 0u};
      N4[h] = v4u{rh ? ww.x : 0u, rh ? ww.y : 0u, (rh && gh) ? xx.x : 0u, (rh && gh) ? xx.y : 0u};
      Zh[h] = v4u{zl.x, zl.y, gh ? z4.x : 0u, gh ? z4.y : 0u};
      PIN(Nh[h]); PIN(N4[h]); PIN(Zh[h]);
    }
    NOPS(1);
    SB0();
    f32x4 nm[2][4], dn[2][4];
#pragma unroll
    for (int h = 0; h < 4; ++h)
#pragma unroll
      for (int tt = 0; tt < 2; ++tt) mm_vvc(dn[tt][h], Zh[h], Pf[tt][h], eps4);
#pragma unroll
    for (int h = 0; h < 4; ++h)
#pragma unroll
      for (int tt = 0; tt < 2; ++tt) mm_vv0(nm[tt][h], Nh[h], Pf[tt][h]);
    SB0();
#pragma unroll
    for (int h = 0; h < 4; ++h)
#pragma unroll
      for (int tt = 0; tt < 2; ++tt) {
        if (h == 0) mm_vv0(num4[tt], N4[h], Pf[tt][h]); else mm_vv(num4[tt], N4[h], Pf[tt][h]);
        inv[tt][h] = gate[tt] * __builtin_amdgcn_rcpf(dn[tt][h][0]);
        PIN(inv[tt][h]);
        SB0();
      }
#pragma unroll
    for (int tt = 0; tt < 2; ++tt)
#pragma unroll
      for (int hp = 0; hp < 2; ++hp) {
        const f32x4 oa = nm[tt][2 * hp] * inv[tt][2 * hp], ob = nm[tt][2 * hp + 1] * inv[tt][2 * hp + 1];
        F[tt][hp] = v4u{pk2(oa[0], oa[1]), pk2(oa[2], oa[3]), pk2(ob[0], ob[1]), pk2(ob[2], ob[3])};
        PIN(F[tt][hp]);
      }
    SB0();
  } else {
    sfor<0, 9>([&](auto Z) { att_op(IC(3), Z); });
    SB0();
    sfor<9, 17>([&](auto Z) { att_op(IC(2), Z); });
    PIN(biasC); PIN(biasP);
    sfor<60, 67>([&](auto Z) { att_op(IC(2), Z); });
    SB0();
    STAMP(5);
    sfor<17, 60>([&](auto Z) { att_op(IC(3), Z); });
    sfor<0, 16>([&](auto Z) { fin_op(IC(2), Z); });
    sfor<9, 17>([&](auto Z) { att_op(IC(3), Z); });
    sfor<60, 67>([&](auto Z) { att_op(IC(3), Z); });
    NOPS(3);
    SB0();
    sfor<0, 16>([&](auto Z) { fin_op(IC(3), Z); });
    SB0();
  }
  {
    const int fb = (ISB ? 20 : 0) + (hg >> 1) * 5, f0 = fb + ((hg & 1) ? 3 : 0);
    const int TT = rt * 8 + 2 * w;
#pragma unroll
    for (int tt = 0; tt < 2; ++tt)
#pragma unroll
      for (int hp = 0; hp < 2; ++hp) ST128(F[tt][hp], lane16 + W_OB + ((TT + tt) * 40 + f0 + hp) * 1024);
    v2u F2[2];
#pragma unroll
    for (int tt = 0; tt < 2; ++tt) {
      const f32x4 o4 = num4[tt] * sel4(g, inv[tt][0], inv[tt][1], inv[tt][2], inv[tt][3]);
      F2[tt] = v2u{pk2(o4[0], o4[1]), pk2(o4[2], o4[3])};
    }
    const auto sx = __builtin_amdgcn_permlane16_swap(F2[0].x, F2[1].x, false, false);
    const auto sy = __builtin_amdgcn_permlane16_swap(F2[0].y, F2[1].y, false, false);
    ST128((v4u{sx[0], sy[0], sx[1], sy[1]}), W_OB + ((TT + (g & 1)) * 40 + fb + 2) * 1024 + ((hg & 1) * 2 + (g >> 1)) * 256 + r * 16);
  }
  STAMP(6);
  SB0();
  sfor<WO_PRE, 50>([&](auto Z) { ld_wo(Z); });
  SB0();
  wait_vm<50 - WO_PRE>();
  flag(W_FO + ((rt * 16 + cg) * 4 + w) * 128);
  pollB = ld_flag(p.ws, fo_of(jt2, w, lane, 1));
  pollA = ld_flag(p.ws, fo_of(jt2, w, lane, 0));
  asm volatile("" ::"v"(tag4));
  STAMP(7);
  __builtin_amdgcn_s_waitcnt(0x0F70);
  if constexpr (ISB) asm volatile("; end of phase 1, local branch"); else asm volatile("; end of phase 1, linear branch");
}

template <int CHK>
__device__ __forceinline__ void phase2(const Params& p, LAS char* L, __amdgpu_buffer_rsrc_t rs, int b, int w, int lane, int jt, int c, int rj, u64 tag,
                                       u64 pollB, u64 pollA, v4u (&wo)[50]) {
  const int r = lane & 15, g = lane >> 4;
  v4u of[2][4][5];
  f32x4 acc[4][5];
  unsigned lane16 = lane * 16;
  PIN(lane16);
  const unsigned foA = fo_of(jt, w, lane, 0), foB = fo_of(jt, w, lane, 1);
  auto o_off = [&](int hb, int a, int i) { return W_OB + ((4 * jt + ((a + w + 1) & 3)) * 40 + (hb ? 0 : 20) + 5 * w + rot5(rj, i)) * 1024; };
  settle_flags(p.ws, foB, tag, pollB);
  STAMP(8);
#pragma unroll
  for (int a = 0; a < 4; ++a)
#pragma unroll
    for (int i = 0; i < 5; ++i) of[0][a][i] = LD128S(lane16, o_off(0, a, i));
  SB0();
  unsigned ra = (unsigned)(uintptr_t)L + lane16;
  unsigned rw[3];
#pragma unroll
  for (int a = 0; a < 3; ++a) { rw[a] = ra + ((((a + w + 1) & 3) * 3 + a) * 5) * 1024; PIN(rw[a]); }
  sfor2<0, 200>([&](auto M) {
    constexpr int m = M, hb = m / 100, a = (m % 100) / 25, i = (m % 25) / 5, n = m % 5;
    if constexpr (m == CHK) { settle_flags(p.ws, foA, tag, pollA); STAMP(9); }
    if constexpr (hb == 0 && i == 0) mm_av0(acc[a][n], wo[n], of[0][a][0]);
    else mm_av(acc[a][n], wo[hb * 25 + i * 5 + n], of[hb][a][i]);
    SB0();
    if constexpr (m == CHK - 25) { if (!__all(pollA == tag)) pollA = ld_flag(p.ws, foA); }
    if constexpr (m >= CHK && m < CHK + 20) of[1][(m - CHK) / 5][(m - CHK) % 5] = LD128S(lane16, o_off(1, (m - CHK) / 5, (m - CHK) % 5));
    if constexpr (m == 120) BAR();
    if constexpr (m >= 130 && (m - 130) % 25 < 20 && (m - 130) % 25 % 4 == 0) {
      constexpr int a2 = (m - 130) / 25, n2 = (m - 130) % 25 / 4;
      *(LAS f32x4*)(uintptr_t)(rw[a2] + n2 * 1024) = acc[a2][n2];
    }
    SB0();
  });
  STAMP(10);
  wait_lgkm();
  BAR();
  LAS f32x4* red = (LAS f32x4*)(uintptr_t)(ra + w * 15 * 1024);
  unsigned o[10];
#pragma unroll
  for (int n = 0; n < 5; ++n) {
    const f32x4 s = (red[n * 64] + red[(5 + n) * 64]) + (red[(10 + n) * 64] + acc[3][n]);
    o[2 * n] = pk2(s[0], s[1]);
    o[2 * n + 1] = pk2(s[2], s[3]);
  }
  const int tok = 64 * jt + 32 * (w >> 1) + 2 * r + (w & 1);
  GAS char* op = (GAS char*)p.out + (size_t)tok * 1280 + c * 160 + g * 40;
  typedef unsigned v4u8 __attribute__((ext_vector_type(4), aligned(8)));
  *(GAS v4u8*)op = v4u8{o[0], o[1], o[2], o[3]};
  *(GAS v4u8*)(op + 16) = v4u8{o[4], o[5], o[6], o[7]};
  *(GAS v2u*)(op + 32) = v2u{o[8], o[9]};
  STAMP(11);
  if (b == 0) {
    const unsigned fa = W_FO + (threadIdx.x * 4) * 128;
    settle_flags(p.ws, fa, tag, ld_flag(p.ws, fa));
    BAR();
    if (w == 0) ST128((v4u{(unsigned)tag, (unsigned)(tag >> 32), (unsigned)tag, (unsigned)(tag >> 32)}), lane < 8 ? W_EPOCH + lane * 16 : OOB);
  }
}

__global__ __launch_bounds__(256, 1) void fused_kernel(Params p) {
  extern __shared__ __attribute__((aligned(16))) char smem[];
  LAS char* L = (LAS char*)smem;
  const int b = blockIdx.x, lane = threadIdx.x & 63;
  const int w = __builtin_amdgcn_readfirstlane(threadIdx.x >> 6);
  STAMP(0);
  const int xcd = b & 7, idx = b >> 3;
  const int rt = (xcd & 1) * 8 + (idx & 7), cg = (xcd >> 1) * 4 + (idx >> 3);
  const int jt = xcd * 4 + (idx & 3), c2 = idx >> 2;
  const int r = lane & 15, g = lane >> 4;
  {
    const char* pc; unsigned len;
    asm volatile("s_getpc_b64 %0\nfk_here:\n\ts_mov_b32 %1, fk_end-fk_here" : "=s"(pc), "=s"(len));
    pc = (const char*)((uintptr_t)pc & ~(uintptr_t)63);
    unsigned co = (unsigned)((idx * 4 + w) * 1024 + lane * 16);
    co = co + 16 <= len ? co : 0u;
    asm volatile("s_mov_b32 m0, %2\n\ts_nop 0\n\tglobal_load_lds_dwordx4 %0, %1" ::"v"(co), "s"(pc), "s"((unsigned)(L_AUX + 2048)) : "memory");
  }
  const __amdgpu_buffer_rsrc_t rs = __builtin_amdgcn_make_buffer_rsrc((void*)p.ws, 0, W_BYTES, 0x00020000);
  Lane q;
  q.rot = idx % 5;
  q.lw = (unsigned)(w * UNIT);
  q.lx = (unsigned)(w * 16384);
#pragma unroll
  for (int i = 0; i < 8; ++i) {
    const int e = i & 1, tt = (i >> 1) & 1, hh = i >> 2;
    q.xo[i] = (unsigned)((16 * e + 2 * (lane >> 3) + tt) * 1280 + hh * 128 + (((lane & 7) ^ (4 * e + g)) * 16));
  }
  q.Wb = (cg < 8 ? p.wA : p.wB) + (cg & 7) * 160 + (size_t)w * 32 * 3840;
  q.xb = p.x + (size_t)(rt * 128 + w * 32) * 1280;
  sfor<0, 8>([&](auto I) { load_x<0, I>(q); });
  STAMP(12);
#pragma unroll
  for (int i = 0; i < 5; ++i) {
    const int c = i * 64 + lane, rho = c / 10, cc = c % 10;
    q.woff[i] = (unsigned)((8 * ((rho >> 2) & 3) + 4 * (rho >> 4) + (rho & 3)) * 3840 + cc * 16);
  }
  sfor<0, 5>([&](auto I) { load_w<0, I>(q); });
  {
    const char* ab = sel4(w, p.gw, p.gw + 1024, (const char*)p.out, (const char*)p.ws);
    const unsigned ao = (unsigned)((w == 1 ? r : lane) * 16);
    asm volatile("s_mov_b32 m0, %2\n\ts_nop 0\n\tglobal_load_lds_dwordx4 %0, %1 sc1" ::"v"(ao), "s"(ab), "s"((unsigned)(L_AUX + w * 1024)) : "memory");
  }
  STAMP(14);
  if (b + 256 * w < 320) {
    const int unit = b + 256 * w, pf = unit % 40, pc = unit / 40, pP = (pf % 20) / 5, pi = pf % 5;
    const int orow = 160 * pP + (pi == 2 ? 16 : (pi >= 3 ? 40 * pi - 40 : 40 * pi)), cgs = pi == 2 ? 40 : 4;
    const char* pb = (pf >= 20 ? p.woB : p.woA) + pc * 160 + orow * 1280;
    sfor<0, 5>([&](auto I) {
      const int c = I * 64 + lane, rho = c / 10, cc = c % 10;
      dma16<L_PACK + I * 1024>(pb, (unsigned)((cgs * ((rho >> 2) & 3) + 20 * (rho >> 4) + (rho & 3)) * 1280 + cc * 16), q.lw);
    });
  }
  STAMP(15);
  sfor<0, 5>([&](auto I) { load_w<1, I>(q); });
  sfor<0, 8>([&](auto I) { load_x<1, I>(q); });
  STAMP(13);
  u64 tag = 0, pollB = 0, pollA = 0;
  v4u wo[50];
  const int rj = (idx & 1) * 2;
  if (cg < 8) phase1<false>(p, q, L, rs, b, rt, cg, w, lane, c2, jt, rj, tag, pollB, pollA, wo);
  else phase1<true>(p, q, L, rs, b, rt, cg - 8, w, lane, c2, jt, rj, tag, pollB, pollA, wo);
  phase2<P2CHK>(p, L, rs, b, w, lane, jt, c2, rj, tag, pollB, pollA, wo);
  asm volatile("fk_end:");
}

extern "C" void kernel_launch(void* const* d_in, const int* in_sizes, int n_in, void* d_out, int out_size, void* d_ws, size_t ws_size,
                              hipStream_t stream) {
  Params p;
  p.x = (const char*)d_in[0];
  p.wA = (const char*)d_in[1];
  p.woA = (const char*)d_in[2];
  p.wB = (const char*)d_in[4];
  p.woB = (const char*)d_in[5];
  p.gw = (const char*)d_in[6];
  p.out = (char*)d_out;
  p.ws = (char*)d_ws;
  (void)hipFuncSetAttribute((const void*)fused_kernel, hipFuncAttributeMaxDynamicSharedMemorySize, LDS_BYTES);
  fused_kernel<<<256, 256, LDS_BYTES, stream>>>(p);
}
```

```cpp
#include <hip/hip_runtime.h>
#include <stdint.h>
#include <type_traits>

#define LAS __attribute__((address_space(3)))
#define GAS __attribute__((address_space(1)))
typedef short s16x4 __attribute__((ext_vector_type(4)));
typedef unsigned v4u __attribute__((ext_vector_type(4)));
typedef unsigned v2u __attribute__((ext_vector_type(2)));
typedef float f32x4 __attribute__((ext_vector_type(4)));
typedef float f32x2 __attribute__((ext_vector_type(2)));
typedef __bf16 bf2 __attribute__((ext_vector_type(2)));
typedef unsigned long long u64;

#define TQM 224
#define PACKM 145
#define P2CHK 50

constexpr int UNIT = 5120, SLOT = 4 * UNIT;
constexpr int L_XST = 4 * SLOT;
constexpr int L_SCR = L_XST;
constexpr int L_SLOT4 = L_SCR + 40960;
constexpr int L_PACK = L_XST + 65536;
constexpr int L_AUX = L_PACK + 2 * UNIT;
constexpr int LDS_BYTES = 163840;
static_assert(L_SLOT4 + SLOT <= L_PACK && L_AUX + 4096 <= LDS_BYTES, "lds map");
constexpr int slot_of(int s) { return s <= 10 ? (s & 3) : s == 11 ? 4 : (s - 9) & 3; }
constexpr int slot_base(int s) { return slot_of(s) == 4 ? L_SLOT4 : slot_of(s) * SLOT; }
constexpr int W_EPOCH = 0;
constexpr int W_FE = 131072, W_FO = 2 * 131072, W_F1 = 3 * 131072;
constexpr int W_PART = 1 << 20, PART_STR = 4096;
constexpr int W_HALO = 2 << 20, HALO_STR = 10240;
constexpr int W_WOP = 4 << 20;
constexpr int W_OB = 6 << 20;
constexpr int W_BYTES = W_OB + 128 * 40 * 1024;
constexpr int SC1 = 16;
constexpr unsigned OOB = 0x40000000u;
constexpr unsigned SPIN_CAP = 1u << 16;

struct Params {
  const char *x, *wA, *woA, *wB, *woB, *gw;
  char *out, *ws;
};

template <int I, int N, class F> __device__ __forceinline__ void sfor(F&& f) {
  if constexpr (I < N) { f(std::integral_constant<int, I>{}); sfor<I + 1, N>(f); }
}
template <int I, int N, class F> __device__ __forceinline__ void sfor2(F&& f) {
  if constexpr (N - I <= 20) sfor<I, N>(f);
  else { sfor<I, I + 20>(f); sfor2<I + 20, N>(f); }
}
#define IC(n) std::integral_constant<int, (n)>{}
__device__ __forceinline__ unsigned pk2(float a, float b) {
  f32x2 t = {a, b};
  return __builtin_bit_cast(unsigned, __builtin_convertvector(t, bf2));
}
#define MF "v_mfma_f32_16x16x32_bf16 "
__device__ __forceinline__ void mm_va(f32x4& d, v4u v, v4u a) { asm volatile(MF "%0, %1, %2, %0" : "+v"(d) : "v"(v), "a"(a)); }
__device__ __forceinline__ void mm_av(f32x4& d, v4u a, v4u v) { asm volatile(MF "%0, %1, %2, %0" : "+v"(d) : "a"(a), "v"(v)); }
__device__ __forceinline__ void mm_vv(f32x4& d, v4u a, v4u b) { asm volatile(MF "%0, %1, %2, %0" : "+v"(d) : "v"(a), "v"(b)); }
__device__ __forceinline__ void mm_va0(f32x4& d, v4u v, v4u a) { asm volatile(MF "%0, %1, %2, 0" : "=&v"(d) : "v"(v), "a"(a)); }
__device__ __forceinline__ void mm_av0(f32x4& d, v4u a, v4u v) { asm volatile(MF "%0, %1, %2, 0" : "=&v"(d) : "a"(a), "v"(v)); }
__device__ __forceinline__ void mm_vv0(f32x4& d, v4u a, v4u b) { asm volatile(MF "%0, %1, %2, 0" : "=&v"(d) : "v"(a), "v"(b)); }
__device__ __forceinline__ void mm_vac(f32x4& d, v4u v, v4u a, f32x4 c) { asm volatile(MF "%0, %1, %2, %3" : "=&v"(d) : "v"(v), "a"(a), "v"(c)); }
__device__ __forceinline__ void mm_avc(f32x4& d, v4u a, v4u v, f32x4 c) { asm volatile(MF "%0, %1, %2, %3" : "=&v"(d) : "a"(a), "v"(v), "v"(c)); }
__device__ __forceinline__ void mm_vvc(f32x4& d, v4u a, v4u b, f32x4 c) { asm volatile(MF "%0, %1, %2, %3" : "=&v"(d) : "v"(a), "v"(b), "v"(c)); }
#define NOPS(n) asm volatile("s_nop " #n)
#define PIN(x) asm volatile("" : "+v"(x))

template <int IMM> __device__ __forceinline__ void dma16(const char* sbase, unsigned voff, unsigned ldsbase) {
  asm volatile("s_add_u32 m0, %2, %3\n\ts_nop 0\n\tglobal_load_lds_dwordx4 %0, %1" ::"v"(voff), "s"(sbase), "s"(ldsbase), "i"(IMM) : "memory", "scc");
}
template <int IMM> __device__ __forceinline__ void dma_m0(unsigned ldsbase) { asm volatile("s_add_u32 m0, %0, %1" ::"s"(ldsbase), "i"(IMM) : "memory", "scc"); }
__device__ __forceinline__ void dma_go(const char* sbase, unsigned voff) { asm volatile("global_load_lds_dwordx4 %0, %1" ::"v"(voff), "s"(sbase) : "memory"); }
template <int OFF> __device__ __forceinline__ void dma_sc1(const char* sbase, unsigned voff) {
  asm volatile("global_load_lds_dwordx4 %0, %1 offset:%2 sc1" ::"v"(voff), "s"(sbase), "i"(OFF) : "memory");
}
__device__ __forceinline__ void touch(const char* sbase, unsigned voff) {
  asm volatile("s_mov_b32 m0, %2\n\ts_nop 0\n\tglobal_load_lds_dword %0, %1" ::"v"(voff), "s"(sbase), "i"(L_AUX + 2048) : "memory");
}
template <int N> __device__ __forceinline__ void wait_vm() { asm volatile("s_waitcnt vmcnt(%0)" ::"i"(N) : "memory"); }
template <int N> __device__ __forceinline__ void wait_vm_p(u64& v) { asm volatile("s_waitcnt vmcnt(%1)" : "+v"(v) : "i"(N) : "memory"); }
__device__ __forceinline__ void wait_lgkm() { asm volatile("s_waitcnt lgkmcnt(0)" ::: "memory"); }
__device__ __forceinline__ v2u trread(LAS char* p) {
  return __builtin_bit_cast(v2u, __builtin_amdgcn_ds_read_tr16_b64_v4i16((LAS s16x4*)p));
}
#define SB0() __builtin_amdgcn_sched_barrier(0)
#define BAR() __builtin_amdgcn_s_barrier()
#define LD128S(voff, soff) __builtin_amdgcn_raw_buffer_load_b128(rs, (voff), (soff), SC1)
#define ST128(v, off) __builtin_amdgcn_raw_buffer_store_b128((v), rs, (off), 0, SC1)
#define STAMP(i) do {} while (0)

__device__ __forceinline__ u64 ld_flag(char* ws, unsigned off) {
  return __hip_atomic_load((GAS u64*)(ws + off), __ATOMIC_RELAXED, __HIP_MEMORY_SCOPE_AGENT);
}
__device__ __forceinline__ void poll_async(u64& v, char* ws, unsigned off) {
  asm volatile("global_load_dwordx2 %0, %1, %2 sc1" : "+v"(v) : "v"(off), "s"(ws) : "memory");
}
__device__ __forceinline__ void settle_flags(char* ws, unsigned off, u64 tag, u64 v) {
  if (!__all(v == tag)) {
    unsigned spins = 0;
    bool ok;
#pragma unroll 1
    do { ok = __all(ld_flag(ws, off) == tag); } while (!ok && ++spins < SPIN_CAP);
  }
  __builtin_amdgcn_fence(__ATOMIC_ACQUIRE, "wavefront");
}
template <class T> __device__ __forceinline__ T sel4(int i, T a, T b, T c, T d) {
  const bool lo = (i & 1) != 0, hi = (i & 2) != 0;
  const T x = lo ? b : a, y = lo ? d : c;
  return hi ? y : x;
}
__device__ __forceinline__ float asf(unsigned u) { return __builtin_bit_cast(float, u); }
__device__ __forceinline__ unsigned asu(float f) { return __builtin_bit_cast(unsigned, f); }
#define vmax(a, b) __builtin_amdgcn_fmed3f((a), (b), vinf)
__device__ __forceinline__ float vmax3(float a, float b, float c) { float r; asm("v_max3_f32 %0, %1, %2, %3" : "=v"(r) : "v"(a), "v"(b), "v"(c)); return r; }
constexpr float EPS = 1e-6f, LOG2E = 1.4426950408889634f;
__device__ __forceinline__ float phi_a(float t) { return __builtin_fmaf(t, LOG2E, -EPS * LOG2E); }
__device__ __forceinline__ float phi_b(float u) { return __builtin_amdgcn_fmed3f(__builtin_amdgcn_exp2f(u), 0.f, 1.f); }
#define phi_c(t) vmax((t), EPS)

constexpr int st_ch(int s) { return s >= 10 ? 2 : (s < 3 ? (s & 1) : s >= 8 ? 1 : 1 - (s & 1)); }
constexpr int st_kb(int s) { return s >= 10 ? s - 10 : (s < 3 ? s / 2 : s == 9 ? 4 : (s & 1) ? (s + 1) / 2 : s / 2 - 1); }
constexpr bool fr_tm(int f) { return f / 20 == 7 || f / 20 == 9; }
constexpr int fr_k(int f) { return f >= 240 ? 8 + (f - 240) % 12 : fr_tm(f) ? 16 + f % 4 : 4 * st_kb(f / 20) + (f % 20) / 5; }
constexpr int fr_j(int f) { return f >= 240 ? ((f - 240) / 12 + 4) % 5 : fr_tm(f) ? ((f % 20) / 4 + 4) % 5 : f % 5; }
constexpr int fr_s(int f) { return f >= 240 ? 10 + fr_k(f) / 4 : f / 20; }
constexpr int AHEAD = 8, RING = 12;

struct Lane {
  unsigned woff[5], xo[8];
  const char *Wb, *xb;
  unsigned lw, lx;
  int rot;
  const char* Wg;
  unsigned tw;
};
__device__ __forceinline__ int rot5(int rot, int pos) { const int k = pos + rot; return k >= 5 ? k - 5 : k; }
template <int S> __device__ __forceinline__ const char* w_src(const Lane& q) {
  return q.Wb + (st_ch(S) == 0 ? 1280 : st_ch(S) == 1 ? 2560 : 0) + (size_t)rot5(q.rot, st_kb(S)) * (128 * 3840);
}
template <int S, int I> __device__ __forceinline__ void load_w(const Lane& q) { dma16<slot_base(S) + I * 1024>(w_src<S>(q), q.woff[I], q.lw); }
template <int S, int I> __device__ __forceinline__ void load_x(const Lane& q) {
  dma16<L_XST + (S & 1) * 8192 + I * 1024>(q.xb + rot5(q.rot, S) * 256, q.xo[I], q.lx);
}
__device__ __forceinline__ unsigned fo_of(int jt, int w, int lane, int br) {
  return W_FO + (((jt >> 1) * 16 + br * 8 + 2 * w + (lane & 1)) * 4 + 2 * (jt & 1) + ((lane >> 1) & 1)) * 128;
}
constexpr int wo_hb(int z) { return z / 5 < 6 ? ((z / 5) & 1) : (z / 5 < 8 ? 0 : 1); }
constexpr int wo_i(int z) { return z / 5 < 6 ? (z / 10) : (z / 5 < 8 ? z / 5 - 3 : z / 5 - 5); }
constexpr int wo_idx(int z) { return wo_hb(z) * 25 + wo_i(z) * 5 + z % 5; }

template <bool ISB>
__device__ __forceinline__ void phase1(const Params& p, const Lane& q, LAS char* L, __amdgpu_buffer_rsrc_t rs, int b, int rt, int hg, int w, int lane, int c2,
                                       int jt2, int rj, u64& tag, u64& pollB, u64& pollA, v4u (&wo)[50]) {
  const int r = lane & 15, g = lane >> 4;
  const int cg = (ISB ? 8 : 0) + hg;
  const unsigned lw = q.lw;
  auto lbase = [&](int off) { unsigned a = (unsigned)(uintptr_t)L + (unsigned)off; PIN(a); return (LAS char*)(uintptr_t)a; };
  const int o1 = (lane >> 2) * 160 + (lane & 3) * 40, o2 = (lane >> 2) * 160 + (lane & 3) * 8;
  LAS char* a1 = lbase(o1);
  LAS char* a2 = lbase(o2);
  LAS char* a1h = lbase(o1 + 3 * SLOT);
  LAS char* a2h = lbase(o2 + 3 * SLOT);
  LAS char* ax0 = lbase(L_XST + w * 16384 + r * 128 + ((g ^ (r >> 1)) * 16));
  LAS char* ax1 = lbase(L_XST + w * 16384 + r * 128 + (((4 + g) ^ (r >> 1)) * 16));
  LAS char* ag = L + L_AUX + g * 16;
  LAS char *a1s, *a2s;
  const int unit = b + 256 * w;
  unsigned lane16 = lane * 16, fl0 = lane < 8 ? lane * 16u : OOB;
  PIN(lane16); PIN(fl0);

  v4u xf[20][2], wf[RING], gf2[2][4];
  f32x4 acc[3][2][5], gacc[2];
  f32x4 eps4 = {EPS, EPS, EPS, EPS};
  PIN(eps4);
  float vinf = __builtin_inff();
  PIN(vinf);

  auto readW = [&](auto F, auto T) {
    constexpr int f = F, t = T, ks = fr_k(f), jt = fr_j(f), sl = slot_of(fr_s(f));
    constexpr int off = (ks % 4) * UNIT + t * 2560 + (jt == 4 ? 32 : 40 * jt);
    v2u v;
    if constexpr (sl == 4) v = trread((jt == 4 ? a1s : a2s) + off);
    else if constexpr (sl == 3) v = trread((jt == 4 ? a1h : a2h) + off);
    else v = trread((jt == 4 ? a1 : a2) + sl * SLOT + off);
    if constexpr (t == 0) { wf[f % RING].x = v.x; wf[f % RING].y = v.y; }
    else { wf[f % RING].z = v.x; wf[f % RING].w = v.y; }
  };
  auto readX = [&](auto S, auto U) {
    constexpr int s = S, u = U, j = u / 3, k = u % 3;
    if constexpr (k < 2) xf[4 * s + j][k] = *(LAS v4u*)(((j & 1) ? ax1 : ax0) + (s & 1) * 8192 + (j >> 1) * 4096 + k * 2048);
    else gf2[s & 1][j] = *(LAS v4u*)(ag + rot5(q.rot, s) * 256 + j * 64);
  };

  v2u ept;
  v4u tag4;
  u64 poll = 0;
  unsigned poll0_off, poll1_off, st_off;
  LAS char *aW, *aR;
  const int rp = rt > 0 ? rt - 1 : 0;
  {
    const int li = lane < 10 ? lane : 0;
    const int pu = c2 * 40 + (li / 5) * 20 + 5 * w + li % 5;
    poll0_off = W_FE + ((pu & 255) * 4 + (pu >> 8)) * 128;
    poll1_off = ISB ? W_F1 + ((cg * 16 + rp) * 4 + 3) * 128 : W_F1 + ((cg * 16 + r) * 4 + w) * 128;
    PIN(poll0_off); PIN(poll1_off);
  }
  const char* halo_src = p.ws + W_HALO + (hg * 16 + rp) * HALO_STR;
  const int wo_b = W_WOP + (c2 * 40 + 5 * w) * 5 * 1024;
  auto wo_soff = [&](int z) { return wo_b + (wo_hb(z) ? 0 : 102400) + rot5(rj, wo_i(z)) * 5120 + ((z % 5) >> 2) * 4096; };
  auto ld_wo = [&](auto Z) { wo[wo_idx(Z)] = LD128S(lane16 + ((Z % 5) & 3) * 1024, wo_soff(Z)); };
  auto flag = [&](int off) { __builtin_amdgcn_raw_buffer_store_b128(tag4, rs, fl0, off, SC1); };
  constexpr int NWO1 = ISB ? 40 : 37;
  float gate[2];
  v4u Kf[5], Vf[5], Ej[5], K4m[4], V4m, pst, gl[16];
  v2u Pq[2][5];
  f32x4 kvT, kvU, kvW, kvX, kvZ, rd[8];
  float ta[2], tb[2], tc[2];
  v4u kx[5], kpv[5], vx5[5], Vb[2][5], Kc[2], Kp[2], Qf[2], Pb[2][2], V4b[2], ones4;
  v2u q4[2];
  f32x4 biasC, biasP, Sc[2], Sp[2];
  float sm[2][4], mx[2], mt;
  unsigned mC[2], mP[2], pp[2][2];
  f32x4 num[2], den[2], num4[2];
  float inv[2][4];
  v4u F[2][2];

  auto fin_op = [&](auto H, auto Z) {
    constexpr int h = H, z = Z;
    if constexpr (z < 2) { inv[z][h] = __builtin_amdgcn_rcpf(den[z][0]); PIN(inv[z][h]); PIN(den[z]); }
    else if constexpr (z < 4) { inv[z - 2][h] *= gate[z - 2]; PIN(inv[z - 2][h]); }
    else if constexpr (z < 12) { constexpr int t2 = (z - 4) >> 2, e = (z - 4) & 3; float u = num[t2][e] * inv[t2][h]; PIN(u); num[t2][e] = u; }
    else { constexpr int t2 = (z - 12) >> 1, e = (z - 12) & 1; unsigned o = pk2(num[t2][2 * e], num[t2][2 * e + 1]); PIN(o); F[t2][h >> 1][2 * (h & 1) + e] = o; }
  };
  constexpr float CS = 0.22360679774997896f * LOG2E;
  auto att_op = [&](auto H, auto Z) {
    constexpr int h = H, z = Z;
    if constexpr (z < 4) { constexpr int t2 = z >> 1, e = z & 1; unsigned o = pk2(acc[2][t2][h][2 * e], acc[2][t2][h][2 * e + 1]); PIN(o); Qf[t2][e] = o; }
    else if constexpr (z == 4) { PIN(Qf[0]); PIN(Qf[1]); NOPS(1); }
    else if constexpr (z < 9) { constexpr int t2 = (z - 5) >> 1; if constexpr ((z - 5) & 1) mm_vvc(Sp[t2], Kp[t2], Qf[t2], biasP); else mm_vvc(Sc[t2], Kc[t2], Qf[t2], biasC); }
    else if constexpr (z < 17) { constexpr int t2 = (z - 9) >> 2, e = (z - 9) & 3; unsigned o = (r >> 2) == h ? Vb[t2][4][e] : 0u; PIN(o); V4b[t2][e] = o; }
    else if constexpr (z < 25) { constexpr int t2 = (z - 17) >> 2, e = (z - 17) & 3; sm[t2][e] = vmax(Sc[t2][e], Sp[t2][e]); PIN(sm[t2][e]); }
    else if constexpr (z < 27) { mx[z - 25] = vmax3(sm[z - 25][0], sm[z - 25][1], sm[z - 25][2]); PIN(mx[z - 25]); }
    else if constexpr (z < 29) { mx[z - 27] = vmax(mx[z - 27], sm[z - 27][3]); PIN(mx[z - 27]); }
    else if constexpr (z == 29) {
      const auto s = __builtin_amdgcn_permlane16_swap(asu(mx[0]), asu(mx[1]), false, false);
      mt = vmax(asf(s[0]), asf(s[1])); PIN(mt);
    } else if constexpr (z == 30) {
      const auto s = __builtin_amdgcn_permlane32_swap(asu(mt), asu(mt), false, false);
      mt = vmax(asf(s[0]), asf(s[1])) * -CS; PIN(mt);
    } else if constexpr (z == 31) {
      const auto s = __builtin_amdgcn_permlane16_swap(asu(mt), asu(mt), false, false);
      mx[0] = asf(s[0]); mx[1] = asf(s[1]); PIN(mx[0]); PIN(mx[1]);
    }
    else if constexpr (z < 40) { constexpr int t2 = (z - 32) >> 2, e = (z - 32) & 3; sm[t2][e] = __builtin_fmaf(sm[t2][e], CS, mx[t2]); PIN(sm[t2][e]); }
    else if constexpr (z < 48) { constexpr int t2 = (z - 40) >> 2, e = (z - 40) & 3; sm[t2][e] = __builtin_amdgcn_exp2f(sm[t2][e]); PIN(sm[t2][e]); }
    else if constexpr (z < 52) { constexpr int t2 = (z - 48) >> 1, e = (z - 48) & 1; pp[t2][e] = pk2(sm[t2][2 * e], sm[t2][2 * e + 1]); PIN(pp[t2][e]); }
    else if constexpr (z < 60) { constexpr int t2 = (z - 52) >> 2, e = (z - 52) & 3; unsigned o = pp[t2][e & 1] & (e < 2 ? mP[e & 1] : mC[e & 1]); PIN(o); Pb[h & 1][t2][e] = o; }
    else if constexpr (z == 60) { PIN(Pb[h & 1][0]); PIN(Pb[h & 1][1]); PIN(V4b[0]); PIN(V4b[1]); PIN(Vb[0][h]); PIN(Vb[1][h]); NOPS(1); }
    else if constexpr (z < 63) mm_vv0(den[z - 61], ones4, Pb[h & 1][z - 61]);
    else if constexpr (z < 65) mm_vv0(num[z - 63], Vb[z - 63][h], Pb[h & 1][z - 63]);
    else if constexpr (z < 67) { if constexpr (h == 0) mm_vv0(num4[z - 65], V4b[z - 65], Pb[h & 1][z - 65]); else mm_vv(num4[z - 65], V4b[z - 65], Pb[h & 1][z - 65]); }
  };
  auto pv_op = [&](auto H, auto U) {
    constexpr int u = U;
    if constexpr (u >= 6 && u < 10) { att_op(H, IC(9 + 2 * (u - 6))); att_op(H, IC(10 + 2 * (u - 6))); }
    if constexpr (u == 10) { att_op(H, IC(60)); att_op(H, IC(61)); }
    if constexpr (u >= 11 && u < 16) att_op(H, IC(51 + u));
    if constexpr (u >= 16) { fin_op(H, IC(2 * (u - 16))); fin_op(H, IC(2 * (u - 16) + 1)); }
  };
  auto att_pre = [&](auto H, auto Z) {
    constexpr int h = H, z = Z;
    if constexpr (z < 2) { Kc[z] = v4u{kx[h][2 * z], kx[h][2 * z + 1], kx[4][2 * z], kx[4][2 * z + 1]}; PIN(Kc[z]); }
    else if constexpr (z < 4) { constexpr int t2 = z - 2; Kp[t2] = v4u{kpv[h][2 * t2], kpv[h][2 * t2 + 1], kpv[4][2 * t2], kpv[4][2 * t2 + 1]}; PIN(Kp[t2]); }
    else { constexpr int t2 = z - 4; unsigned lo = g == h ? q4[t2].x : 0u, hi = g == h ? q4[t2].y : 0u; PIN(lo); PIN(hi); Qf[t2].z = lo; Qf[t2].w = hi; }
  };
  auto phi_op = [&](auto Z, const f32x4& t, auto H, unsigned& out) {
    constexpr int z = Z, h = H;
    if constexpr (z < 2) { ta[z] = phi_a(t[2 * h + z]); PIN(ta[z]); }
    else if constexpr (z < 4) { tb[z - 2] = phi_b(ta[z - 2]); PIN(tb[z - 2]); }
    else if constexpr (z < 6) { tc[z - 4] = phi_c(t[2 * h + z - 4]); PIN(tc[z - 4]); }
    else if constexpr (z < 8) { tc[z - 6] += tb[z - 6]; PIN(tc[z - 6]); }
    else { out = pk2(tc[0], tc[1]); PIN(out); }
  };

  wait_vm<5>();
  BAR();
  STAMP(1);
  ept = *(LAS v2u*)(L + L_AUX + 3072);
  sfor<0, 3>([&](auto U) { readX(IC(0), U); });
  sfor<0, AHEAD>([&](auto F_) { readW(F_, IC(0)); readW(F_, IC(1)); });
  sfor<3, 12>([&](auto U) { readX(IC(0), U); });
  tag = (((u64)(unsigned)__builtin_amdgcn_readfirstlane(ept.y) << 32) | (unsigned)__builtin_amdgcn_readfirstlane(ept.x)) + 1ull;
  tag4 = v4u{(unsigned)tag, (unsigned)(tag >> 32), (unsigned)tag, (unsigned)(tag >> 32)};
  PIN(tag4);
  SB0();

  sfor2<0, 600>([&](auto M) {
    constexpr int m = M, f = m / 2, tt = m & 1, ks = fr_k(f), jt = fr_j(f), ch = st_ch(fr_s(f));
    constexpr int sb = (m + 16) / 40 - 1, mb = (m + 16) % 40;
    constexpr int tv = (m - 368) / 8, uv = (m - 368) % 8, jv = (tv + 4) % 5;
    constexpr int tq = (m - 504) / 24, uq = (m - 504) % 24, jq = (tq + 4) % 5;
    if constexpr (mb == 0 && sb >= 0 && sb <= 11) {
      constexpr int cntA[12] = {13, 13, 9, 13, 9, 13, 7, 6, 10, 14, 18, 6};
      constexpr int cntB[12] = {13, 13, 9, 13, 9, 13, 7, 6, 10, 13, 18, 5};
      constexpr int cnt = ISB ? cntB[sb] : cntA[sb];
      if constexpr (sb == 2 || sb == 4) { if (unit < 320) wait_vm<cnt + 5>(); else wait_vm<cnt>(); }
      else wait_vm_p<cnt>(poll);
      BAR();
      SB0();
    }
    if constexpr (m % 8 == 0 && m > 0 && f + AHEAD <= 300) { __builtin_amdgcn_s_waitcnt(0xC07F | (8 << 8)); SB0(); }
    {
      f32x4& d = acc[ch][tt][jt];
      const v4u& wv = wf[f % RING];
      if constexpr (ch == 2 || (ch == 0 && ISB)) {
        if constexpr (ks != 0) mm_va(d, wv, xf[ks][tt]);
        else if constexpr (ISB) mm_va0(d, wv, xf[ks][tt]);
        else mm_vac(d, wv, xf[ks][tt], eps4);
      } else {
        if constexpr (ks != 0) mm_av(d, xf[ks][tt], wv);
        else if constexpr (ch == 0) mm_avc(d, xf[ks][tt], wv, eps4);
        else mm_av0(d, xf[ks][tt], wv);
      }
    }
    SB0();
    if constexpr (f + AHEAD < 300) readW(IC(f + AHEAD < 300 ? f + AHEAD : 0), IC(tt));
    if constexpr ((sb == 1 || sb == 2 || sb == 4 || sb == 6) && mb < 12) readX(IC(sb == 1 ? 1 : sb / 2 + 1), IC(mb));
    SB0();
    if constexpr (ch == 0 && m % 10 == 9) {
      constexpr int kg = 4 * st_kb(m / 40) + (m % 40) / 10;
      if constexpr (kg == 0) { mm_va0(gacc[0], gf2[0][0], xf[0][0]); mm_va0(gacc[1], gf2[0][0], xf[0][1]); }
      else { mm_va(gacc[0], gf2[(kg >> 2) & 1][kg & 3], xf[kg][0]); mm_va(gacc[1], gf2[(kg >> 2) & 1][kg & 3], xf[kg][1]); }
    }
    auto split_w = [&](auto S, auto P0, auto ST) {
      constexpr int p0 = P0, st = ST;
      if constexpr (mb >= p0 && (mb - p0) / st < 5) {
        if constexpr ((mb - p0) % st == 0) dma_m0<slot_base(S) + (mb - p0) / st * 1024>(lw);
        if constexpr ((mb - p0) % st == 1) dma_go(w_src<S>(q), q.woff[(mb - p0) / st]);
      }
    };
    auto split_x = [&](auto XS, auto I0, auto N, auto P0, auto ST) {
      constexpr int p0 = P0, st = ST, xs = XS;
      if constexpr (mb >= p0 && (mb - p0) / st < N) {
        constexpr int i = I0 + (mb - p0) / st;
        if constexpr ((mb - p0) % st == 0) dma_m0<L_XST + (xs & 1) * 8192 + i * 1024>(q.lx);
        if constexpr ((mb - p0) % st == 1) dma_go(q.xb + rot5(q.rot, xs) * 256, q.xo[i]);
      }
    };
    if constexpr (m < 8) load_x<1, m>(q);
    if constexpr (m >= 10 && m < 20 && m % 2 == 0) load_w<2, (m - 10) / 2>(q);
    if constexpr (sb == 0) { split_x(IC(2), IC(0), IC(8), IC(1), IC(3)); split_w(IC(3), IC(25), IC(3)); }
    if constexpr (m == 65) {
      if (unit < 320) {
        const int pf = unit % 40, pc = unit / 40, pP = (pf % 20) / 5, pi = pf % 5;
        const int orow = 160 * pP + (pi == 2 ? 16 : (pi >= 3 ? 40 * pi - 40 : 40 * pi)), cgs = pi == 2 ? 40 : 4;
        const char* pb = (pf >= 20 ? p.woB : p.woA) + pc * 160 + orow * 1280;
        sfor<0, 5>([&](auto I) {
          const int c = I * 64 + lane, rho = c / 10, cc = c % 10;
          dma16<L_PACK + I * 1024>(pb, (unsigned)((cgs * ((rho >> 2) & 3) + 20 * (rho >> 4) + (rho & 3)) * 1280 + cc * 16), lw);
        });
      }
    }
    if constexpr (sb >= 1 && sb <= 4) { split_w(IC(sb + 3), IC(2), IC(4)); split_x(IC(3 + (sb - 1) / 2), IC(((sb - 1) & 1) * 4), IC(4), IC(25), IC(4)); }
    if constexpr (sb >= 5 && sb <= 6) split_w(IC(sb + 3), IC(2), IC(8));
    if constexpr (sb == 7) { split_w(IC(10), IC(2), IC(4)); split_w(IC(11), IC(21), IC(4)); }
    if constexpr (sb == 8 || sb == 9) split_w(IC(sb + 4), IC(2), IC(8));
    if constexpr (sb == 10) split_w(IC(14), IC(1), IC(3));
    if constexpr (m == TQM) touch(q.Wg, q.tw);
    if constexpr (m == PACKM) {
      if (unit < 320) {
        v4u pv[5];
        sfor<0, 5>([&](auto J) {
          const v2u lo = trread(a1 + L_PACK + w * UNIT + 8 * J), hi = trread(a1 + L_PACK + w * UNIT + 2560 + 8 * J);
          pv[J] = v4u{lo.x, lo.y, hi.x, hi.y};
        });
        sfor<0, 5>([&](auto J) { ST128(pv[J], lane16 + W_WOP + (unit * 5 + J) * 1024); });
      }
    }
    if constexpr (m == 225) flag(W_FE + (b * 4 + w) * 128);
    if constexpr (m == 301) poll_async(poll, p.ws, poll0_off);
    if constexpr (m == 345) settle_flags(p.ws, poll0_off, tag, poll);
    if constexpr (m == 85) { a1s = lbase(o1 + L_SLOT4); a2s = lbase(o2 + L_SLOT4); }
    if constexpr (m == 87) {
      aW = lbase(ISB ? L_SCR + ((w + 1) & 3) * 10240 + lane * 16 : L_SCR + w * 8192 + lane * 16);
      aR = lbase(ISB ? L_SCR + w * 10240 + lane * 16 : L_SCR + w * 2048 + lane * 16);
      st_off = ISB ? W_HALO + (hg * 16 + rt) * HALO_STR + lane * 16 : W_PART + (hg * 16 + rt) * PART_STR + w * 1024 + lane * 16;
      PIN(st_off);
    }
    if constexpr (m == 326) { PIN(gacc[0]); PIN(gacc[1]); }
    if constexpr (m == 483 || m == 487) {
      constexpr int t2 = (m - 483) / 4;
      gate[t2] = __builtin_amdgcn_rcpf(1.f + __builtin_amdgcn_exp2f(gacc[t2][0] * (ISB ? LOG2E : -LOG2E)));
      PIN(gate[t2]);
    }
    if constexpr (sb >= 8 && sb <= 9 && mb % 8 == 6 && mb < 32) ld_wo(IC((sb - 8) * 4 + mb / 8));
    if constexpr (m >= 445 && (m - 445) % 4 == 0 && 8 + (m - 445) / 4 < NWO1) ld_wo(IC(8 + (m - 445) / 4));

    if constexpr (!ISB) {
      if constexpr (m >= 244 && m < 264 && m % 4 == 0) { const unsigned one = (r == (m - 244) / 4) ? 0x3F803F80u : 0u; Ej[(m - 244) / 4] = v4u{one, one, one, one}; PIN(Ej[(m - 244) / 4]); }
      if constexpr (m >= 292 && m < 400) {
        sfor<(m - 292) * 5 / 3, (m - 291) * 5 / 3>([&](auto Z) {
          constexpr int z = Z, kt = (z / 36 + 4) % 5, pr = z % 36 / 9, t2 = pr >> 1, h = pr & 1;
          unsigned o = 0;
          phi_op(IC(z % 9), acc[0][t2][kt], IC(h), o);
          if constexpr (z % 9 == 8) Kf[kt][2 * t2 + h] = o;
          if constexpr (z % 36 == 35) PIN(Kf[kt]);
        });
      }
      if constexpr (m >= 315 && m <= 403 && (m - 315) % 22 == 0) {
        constexpr int kt = ((m - 315) / 22 + 4) % 5;
        if constexpr (m == 315) mm_vv0(kvZ, Kf[kt], Ej[kt]); else mm_vv(kvZ, Kf[kt], Ej[kt]);
      }
      if constexpr (m >= 316 && m < 364 && (m - 316) % 3 == 0) {
        constexpr int z = (m - 316) / 3;
        unsigned o = (r >> 2) == (z >> 2) ? Kf[4][z & 3] : 0u;
        PIN(o);
        K4m[z >> 2][z & 3] = o;
      }
      LAS f32x4* red = (LAS f32x4*)aW;
      if constexpr (m >= 368 && tv < 5) {
        if constexpr (uv == 3 || uv == 4) {
          constexpr int t2 = uv - 3;
          unsigned lo = pk2(acc[1][t2][jv][0], acc[1][t2][jv][1]), hi = pk2(acc[1][t2][jv][2], acc[1][t2][jv][3]);
          PIN(lo); PIN(hi);
          Vf[jv][2 * t2] = lo; Vf[jv][2 * t2 + 1] = hi;
        }
        if constexpr (uv == 5) { PIN(Vf[jv]); if constexpr (tv > 0) PIN(K4m[jv & 3]); }
        if constexpr (tv == 0) { if constexpr (uv == 6) mm_vv0(kvX, Kf[4], Vf[4]); }
        else {
          if constexpr (uv < 2) {
            unsigned lo = (r >> 2) == jv ? Vf[4][2 * uv] : 0u, hi = (r >> 2) == jv ? Vf[4][2 * uv + 1] : 0u;
            PIN(lo); PIN(hi);
            V4m[2 * uv] = lo; V4m[2 * uv + 1] = hi;
          }
          if constexpr (uv == 2) { PIN(V4m); if constexpr (tv == 1) red[6 * 64] = kvX; else red[(jv - 1) * 64] = kvT; }
          if constexpr (uv == 4) { if constexpr (tv == 1) mm_vv0(kvW, Kf[jv], V4m); else mm_vv(kvW, Kf[jv], V4m); }
          if constexpr (uv == 6) mm_vv0(kvT, Kf[jv], Vf[jv]);
          if constexpr (uv == 7) { if constexpr (tv == 1) mm_vv0(kvU, K4m[jv & 3], Vf[jv]); else mm_vv(kvU, K4m[jv & 3], Vf[jv]); }
        }
      }
      if constexpr (m == 408) red[7 * 64] = kvZ;
      if constexpr (m == 410) red[5 * 64] = kvW;
      if constexpr (m == 412) red[3 * 64] = kvT;
      if constexpr (m == 415) red[4 * 64] = kvU;
      if constexpr (m == 423) asm volatile("s_waitcnt lgkmcnt(8)" ::: "memory");
      if constexpr (m >= 424 && m < 432) rd[m - 424] = *(LAS f32x4*)(aR + ((m - 424) >> 1) * 8192 + ((m - 424) & 1) * 1024);
      if constexpr (m >= 433 && m < 441) {
        sfor<0, 3>([&](auto E) {
          constexpr int z = 3 * (m - 433) + E, qq = z >> 2, e = z & 3;
          float s = rd[qq & 1][e] + rd[2 + qq][e]; PIN(s);
          rd[qq & 1][e] = s;
        });
      }
      if constexpr (m == 441 || m == 442) {
        unsigned lo = pk2(rd[m - 441][0], rd[m - 441][1]), hi = pk2(rd[m - 441][2], rd[m - 441][3]);
        PIN(lo); PIN(hi);
        pst[2 * (m - 441)] = lo; pst[2 * (m - 441) + 1] = hi;
      }
      if constexpr (m == 444) ST128(pst, st_off);
      if constexpr (m == 494) { wait_vm<13>(); flag(W_F1 + ((cg * 16 + rt) * 4 + w) * 128); }
      if constexpr (m == 559) poll = ld_flag(p.ws, poll1_off);
      if constexpr (m >= 504 && uq >= 4 && uq < 22) {
        sfor<0, 2>([&](auto E) {
          constexpr int z = 2 * (uq - 4) + E, pr = z / 9, t2 = pr >> 1, h = pr & 1;
          unsigned o = 0;
          phi_op(IC(z % 9), acc[2][t2][jq], IC(h), o);
          if constexpr (z % 9 == 8) Pq[t2][jq][h] = o;
        });
      }
    } else {
      if constexpr (m >= 240 && m < 256 && m % 4 == 0) {
        constexpr int e = (m - 240) / 4;
        const int key = 4 * g + e;
        float bc = (key <= r && key + 7 >= r) ? 0.f : -1e30f, bp = (key >= r + 9 && (rt > 0 || w > 0)) ? 0.f : -1e30f;
        PIN(bc); PIN(bp);
        biasC[e] = bc; biasP[e] = bp;
      }
      if constexpr (m == 256 || m == 260) {
        constexpr int e = (m - 256) / 4;
        mC[e] = (4 * g + 2 * e <= r ? 0xFFFFu : 0u) | (4 * g + 2 * e + 1 <= r ? 0xFFFF0000u : 0u);
        mP[e] = ~mC[e];
        PIN(mC[e]); PIN(mP[e]);
      }
      if constexpr (m == 262) { ones4 = v4u{0x3F803F80u, 0x3F803F80u, 0x3F803F80u, 0x3F803F80u}; PIN(ones4); PIN(biasC); PIN(biasP); }
      if constexpr (m >= 288 && m < 328 && (m - 288) % 8 >= 4) {
        constexpr int kt = ((m - 288) / 8 + 4) % 5, z = (m - 288) % 8 - 4, t2 = z >> 1, e = z & 1;
        unsigned o = pk2(acc[0][t2][kt][2 * e], acc[0][t2][kt][2 * e + 1]);
        PIN(o);
        kx[kt][2 * t2 + e] = o;
      }
      if constexpr (m == 328) { if (w == 3) sfor<0, 5>([&](auto J) { ST128(kx[J], st_off + J * 1024); }); }
      if constexpr (m >= 347 && m < 367 && (m - 347) % 4 == 0) *(LAS v4u*)(aW + (m - 347) / 4 * 1024) = kx[(m - 347) / 4];
      if constexpr (m >= 368 && tv < 5) {
        if constexpr (uv == 3 || uv == 4) {
          constexpr int t2 = uv - 3;
          unsigned lo = pk2(acc[1][t2][jv][0], acc[1][t2][jv][1]), hi = pk2(acc[1][t2][jv][2], acc[1][t2][jv][3]);
          PIN(lo); PIN(hi);
          vx5[jv][2 * t2] = lo; vx5[jv][2 * t2 + 1] = hi;
        }
        if constexpr (uv == 6) *(LAS v4u*)(aW + (5 + jv) * 1024) = vx5[jv];
      }
      if constexpr (m == 408) { if (w == 3) sfor<0, 5>([&](auto J) { ST128(vx5[J], st_off + (5 + J) * 1024); }); }
      if constexpr (m >= 410 && m < 420) { constexpr int j = (m - 410) >> 1; Vb[tt][j].z = vx5[j][2 * tt]; Vb[tt][j].w = vx5[j][2 * tt + 1]; }
      auto halo_in = [&](auto R) {
        if (w == 0) {
          settle_flags(p.ws + R * 131072, poll1_off, tag, poll);
          dma_m0<L_SCR + R * 5120>(0u); NOPS(0);
          sfor<0, 4>([&](auto J) { dma_sc1<J * 1024>(halo_src + R * 5120, lane16); });
          dma_m0<L_SCR + R * 5120 + 4096>(0u); NOPS(0); dma_sc1<0>(halo_src + R * 5120 + 4096, lane16);
        }
      };
      if constexpr (m == 385) flag(W_F1 + ((cg * 16 + rt) * 4 + w) * 128);
      if constexpr (m == 424) poll_async(poll, p.ws, poll1_off);
      if constexpr (m == 466) { halo_in(IC(0)); flag(W_F1 + 131072 + ((cg * 16 + rt) * 4 + w) * 128); }
      if constexpr (m == 494) poll_async(poll, p.ws + 131072, poll1_off);
      if constexpr (m == 506) wait_vm<12>();
      if constexpr (m >= 506 && m < 511) kpv[m - 506] = *(LAS v4u*)(aR + (m - 506) * 1024);
      if constexpr (m == 530) { wait_vm_p<9>(poll); halo_in(IC(1)); }
      if constexpr (m == 566) wait_vm<9>();
      if constexpr (m >= 566 && m < 576) {
        constexpr int j = ((m - 566) / 2 + 4) % 5;
        const v2u v = *(LAS v2u*)(aR + (5 + j) * 1024 + tt * 8);
        Vb[tt][j].x = v.x; Vb[tt][j].y = v.y;
      }
      if constexpr (m == 511 || m == 513) {
        constexpr int t2 = (m - 511) / 2;
        q4[t2] = v2u{pk2(acc[2][t2][4][0], acc[2][t2][4][1]), pk2(acc[2][t2][4][2], acc[2][t2][4][3])};
        PIN(q4[t2]);
      }
      if constexpr (m >= 516 && m < 528 && m % 2 == 0) att_pre(IC(0), IC((m - 516) / 2));
      if constexpr (m >= 528) {
        constexpr int h = tq - 1;
        constexpr int z0[25] = {0, 0, 0, 0, 2, 5, 7, 9, 9, 9, 9, 13, 17, 21, 22, 23, 24, 28, 32, 35, 38, 41, 46, 52, 52};
        sfor<z0[uq], z0[uq + 1]>([&](auto Z) { att_op(IC(h), IC(Z < 9 ? Z : Z + 8)); });
        if constexpr (uq >= 7 && uq < 10) { att_pre(IC(h + 1), IC(2 * (uq - 7))); att_pre(IC(h + 1), IC(2 * (uq - 7) + 1)); }
        if constexpr (h == 2 && uq < 18) pv_op(IC(0), IC(uq + 6));
      }
    }
    SB0();
    if constexpr (m == 199) STAMP(2);
    if constexpr (m == 399) STAMP(3);
  });
  STAMP(4);
  constexpr int WO_PRE = ISB ? 46 : 44;
  if constexpr (!ISB) {
    settle_flags(p.ws, poll1_off, tag, poll);
    sfor<0, 16>([&](auto I) { gl[I] = LD128S(lane16, W_PART + (hg * 16 + I) * PART_STR + w * 1024); SB0(); });
  }
  sfor<NWO1, WO_PRE>([&](auto Z) { ld_wo(Z); });
  NOPS(3);
  SB0();
  if constexpr (!ISB) {
    sfor<0, 36>([&](auto Z) {
      constexpr int z = Z, pr = z / 9, t2 = pr >> 1, h = pr & 1;
      unsigned o = 0;
      phi_op(IC(z % 9), acc[2][t2][3], IC(h), o);
      if constexpr (z % 9 == 8) Pq[t2][3][h] = o;
    });
    v4u Pf[2][4];
#pragma unroll
    for (int h = 0; h < 4; ++h)
#pragma unroll
      for (int tt = 0; tt < 2; ++tt) { Pf[tt][h] = v4u{Pq[tt][h].x, Pq[tt][h].y, Pq[tt][4].x, Pq[tt][4].y}; PIN(Pf[tt][h]); }
    const unsigned sv = (g == (r >> 2)) ? ((r & 1) ? 0x3F800000u : 0x00003F80u) : 0u;
    const unsigned s0_ = (r & 2) ? 0u : sv, s1_ = (r & 2) ? sv : 0u;
    v4u A1 = {s0_, s1_, 0u, 0u}, A2 = {0u, 0u, s0_, s1_};
    LAS v2u* kvs = (LAS v2u*)lbase(L_SCR + 32768 + lane * 8);
    LAS v2u* kvw = (LAS v2u*)lbase(L_SCR + 32768 + w * 1024 + lane * 8);
    LAS v2u* kvz = (LAS v2u*)lbase(L_SCR + 32768 + (448 + g * 16) * 8);
    PIN(A1); PIN(A2); NOPS(1);
    SB0();
    f32x4 d1, d2;
#pragma unroll
    for (int i = 0; i < 16; ++i) {
      if (i == 0) { mm_vv0(d1, A1, gl[0]); mm_vv0(d2, A2, gl[0]); }
      else { mm_vv(d1, A1, gl[i]); mm_vv(d2, A2, gl[i]); }
    }
    NOPS(7);
    SB0();
    kvw[0] = v2u{pk2(d1[0], d1[1]), pk2(d1[2], d1[3])};
    kvw[64] = v2u{pk2(d2[0], d2[1]), pk2(d2[2], d2[3])};
    wait_lgkm();
    BAR();
    STAMP(5);
    const v2u uu = kvs[256], ww = kvs[320], xx = kvs[384], z4 = kvz[4];
    v4u Nh[4], N4[4], Zh[4];
    v2u tl4[4], zl4[4];
#pragma unroll
    for (int h = 0; h < 4; ++h) { zl4[h] = kvz[h]; tl4[h] = kvs[h * 64]; }
    SB0();
#pragma unroll
    for (int h = 0; h < 4; ++h) {
      const v2u tl = tl4[h], zl = zl4[h];
      const bool gh = g == h, rh = (r >> 2) == h;
      Nh[h] = v4u{tl.x, tl.y, gh ? uu.x : 0u, gh ? uu.y : 0u};
      N4[h] = v4u{rh ? ww.x : 0u, rh ? ww.y : 0u, (rh && gh) ? xx.x : 0u, (rh && gh) ? xx.y : 0u};
      Zh[h] = v4u{zl.x, zl.y, gh ? z4.x : 0u, gh ? z4.y : 0u};
      PIN(Nh[h]); PIN(N4[h]); PIN(Zh[h]);
    }
    NOPS(1);
    SB0();
    f32x4 nm[2][4], dn[2][4];
#pragma unroll
    for (int h = 0; h < 4; ++h)
#pragma unroll
      for (int tt = 0; tt < 2; ++tt) mm_vvc(dn[tt][h], Zh[h], Pf[tt][h], eps4);
#pragma unroll
    for (int h = 0; h < 4; ++h)
#pragma unroll
      for (int tt = 0; tt < 2; ++tt) mm_vv0(nm[tt][h], Nh[h], Pf[tt][h]);
    SB0();
#pragma unroll
    for (int h = 0; h < 4; ++h)
#pragma unroll
      for (int tt = 0; tt < 2; ++tt) {
        if (h == 0) mm_vv0(num4[tt], N4[h], Pf[tt][h]); else mm_vv(num4[tt], N4[h], Pf[tt][h]);
        inv[tt][h] = gate[tt] * __builtin_amdgcn_rcpf(dn[tt][h][0]);
        PIN(inv[tt][h]);
        SB0();
      }
#pragma unroll
    for (int tt = 0; tt < 2; ++tt)
#pragma unroll
      for (int hp = 0; hp < 2; ++hp) {
        const f32x4 oa = nm[tt][2 * hp] * inv[tt][2 * hp], ob = nm[tt][2 * hp + 1] * inv[tt][2 * hp + 1];
        F[tt][hp] = v4u{pk2(oa[0], oa[1]), pk2(oa[2], oa[3]), pk2(ob[0], ob[1]), pk2(ob[2], ob[3])};
        PIN(F[tt][hp]);
      }
    SB0();
  } else {
    sfor<0, 9>([&](auto Z) { att_op(IC(3), Z); });
    SB0();
    sfor<9, 17>([&](auto Z) { att_op(IC(1), Z); });
    PIN(biasC); PIN(biasP);
    sfor<60, 67>([&](auto Z) { att_op(IC(1), Z); });
    SB0();
    sfor<17, 32>([&](auto Z) { att_op(IC(3), Z); });
    sfor<0, 16>([&](auto Z) { fin_op(IC(1), Z); });
    sfor<9, 17>([&](auto Z) { att_op(IC(2), Z); });
    sfor<60, 67>([&](auto Z) { att_op(IC(2), Z); });
    SB0();
    STAMP(5);
    sfor<32, 60>([&](auto Z) { att_op(IC(3), Z); });
    sfor<0, 16>([&](auto Z) { fin_op(IC(2), Z); });
    sfor<9, 17>([&](auto Z) { att_op(IC(3), Z); });
    sfor<60, 67>([&](auto Z) { att_op(IC(3), Z); });
    NOPS(3);
    SB0();
    sfor<0, 16>([&](auto Z) { fin_op(IC(3), Z); });
    SB0();
  }
  {
    const int fb = (ISB ? 20 : 0) + (hg >> 1) * 5, f0 = fb + ((hg & 1) ? 3 : 0);
    const int TT = rt * 8 + 2 * w;
#pragma unroll
    for (int tt = 0; tt < 2; ++tt)
#pragma unroll
      for (int hp = 0; hp < 2; ++hp) ST128(F[tt][hp], lane16 + W_OB + ((TT + tt) * 40 + f0 + hp) * 1024);
    v2u F2[2];
#pragma unroll
    for (int tt = 0; tt < 2; ++tt) {
      const f32x4 o4 = num4[tt] * sel4(g, inv[tt][0], inv[tt][1], inv[tt][2], inv[tt][3]);
      F2[tt] = v2u{pk2(o4[0], o4[1]), pk2(o4[2], o4[3])};
    }
    const auto sx = __builtin_amdgcn_permlane16_swap(F2[0].x, F2[1].x, false, false);
    const auto sy = __builtin_amdgcn_permlane16_swap(F2[0].y, F2[1].y, false, false);
    ST128((v4u{sx[0], sy[0], sx[1], sy[1]}), W_OB + ((TT + (g & 1)) * 40 + fb + 2) * 1024 + ((hg & 1) * 2 + (g >> 1)) * 256 + r * 16);
  }
  STAMP(6);
  SB0();
  sfor<WO_PRE, 50>([&](auto Z) { ld_wo(Z); });
  SB0();
  wait_vm<50 - WO_PRE>();
  flag(W_FO + ((rt * 16 + cg) * 4 + w) * 128);
  pollB = ld_flag(p.ws, fo_of(jt2, w, lane, 1));
  pollA = ld_flag(p.ws, fo_of(jt2, w, lane, 0));
  asm volatile("" ::"v"(tag4));
  STAMP(7);
  __builtin_amdgcn_s_waitcnt(0x0F70);
  if constexpr (ISB) asm volatile("; end of phase 1, local branch"); else asm volatile("; end of phase 1, linear branch");
}

template <int CHK>
__device__ __forceinline__ void phase2(const Params& p, LAS char* L, __amdgpu_buffer_rsrc_t rs, int b, int w, int lane, int jt, int c, int rj, u64 tag,
                                       u64 pollB, u64 pollA, v4u (&wo)[50]) {
  const int r = lane & 15, g = lane >> 4;
  v4u of[2][4][5];
  f32x4 acc[4][5];
  unsigned lane16 = lane * 16;
  PIN(lane16);
  const unsigned foA = fo_of(jt, w, lane, 0), foB = fo_of(jt, w, lane, 1);
  auto o_off = [&](int hb, int a, int i) { return W_OB + ((4 * jt + ((a + w + 1) & 3)) * 40 + (hb ? 0 : 20) + 5 * w + rot5(rj, i)) * 1024; };
  settle_flags(p.ws, foB, tag, pollB);
  STAMP(8);
#pragma unroll
  for (int a = 0; a < 4; ++a)
#pragma unroll
    for (int i = 0; i < 5; ++i) of[0][a][i] = LD128S(lane16, o_off(0, a, i));
  SB0();
  unsigned ra = (unsigned)(uintptr_t)L + lane16;
  unsigned rw[3];
#pragma unroll
  for (int a = 0; a < 3; ++a) { rw[a] = ra + ((((a + w + 1) & 3) * 3 + a) * 5) * 1024; PIN(rw[a]); }
  sfor2<0, 200>([&](auto M) {
    constexpr int m = M, hb = m / 100, a = (m % 100) / 25, i = (m % 25) / 5, n = m % 5;
    if constexpr (m == CHK) { settle_flags(p.ws, foA, tag, pollA); STAMP(9); }
    if constexpr (hb == 0 && i == 0) mm_av0(acc[a][n], wo[n], of[0][a][0]);
    else mm_av(acc[a][n], wo[hb * 25 + i * 5 + n], of[hb][a][i]);
    SB0();
    if constexpr (m == CHK - 25) { if (!__all(pollA == tag)) pollA = ld_flag(p.ws, foA); }
    if constexpr (m >= CHK && m < CHK + 20) of[1][(m - CHK) / 5][(m - CHK) % 5] = LD128S(lane16, o_off(1, (m - CHK) / 5, (m - CHK) % 5));
    if constexpr (m == 120) BAR();
    if constexpr (m >= 130 && (m - 130) % 25 < 20 && (m - 130) % 25 % 4 == 0) {
      constexpr int a2 = (m - 130) / 25, n2 = (m - 130) % 25 / 4;
      *(LAS f32x4*)(uintptr_t)(rw[a2] + n2 * 1024) = acc[a2][n2];
    }
    SB0();
  });
  STAMP(10);
  wait_lgkm();
  BAR();
  LAS f32x4* red = (LAS f32x4*)(uintptr_t)(ra + w * 15 * 1024);
  unsigned o[10];
#pragma unroll
  for (int n = 0; n < 5; ++n) {
    const f32x4 s = (red[n * 64] + red[(5 + n) * 64]) + (red[(10 + n) * 64] + acc[3][n]);
    o[2 * n] = pk2(s[0], s[1]);
    o[2 * n + 1] = pk2(s[2], s[3]);
  }
  const int tok = 64 * jt + 32 * (w >> 1) + 2 * r + (w & 1);
  GAS char* op = (GAS char*)p.out + (size_t)tok * 1280 + c * 160 + g * 40;
  typedef unsigned v4u8 __attribute__((ext_vector_type(4), aligned(8)));
  *(GAS v4u8*)op = v4u8{o[0], o[1], o[2], o[3]};
  *(GAS v4u8*)(op + 16) = v4u8{o[4], o[5], o[6], o[7]};
  *(GAS v2u*)(op + 32) = v2u{o[8], o[9]};
  STAMP(11);
  if (b == 0) {
    const unsigned fa = W_FO + (threadIdx.x * 4) * 128;
    settle_flags(p.ws, fa, tag, ld_flag(p.ws, fa));
    BAR();
    if (w == 0) ST128((v4u{(unsigned)tag, (unsigned)(tag >> 32), (unsigned)tag, (unsigned)(tag >> 32)}), lane < 8 ? W_EPOCH + lane * 16 : OOB);
  }
}

__global__ __launch_bounds__(256, 1) void fused_kernel(Params p) {
  extern __shared__ __attribute__((aligned(16))) char smem[];
  LAS char* L = (LAS char*)smem;
  const int b = blockIdx.x, lane = threadIdx.x & 63;
  const int w = __builtin_amdgcn_readfirstlane(threadIdx.x >> 6);
  STAMP(0);
  const int xcd = b & 7, idx = b >> 3;
  const int rt = (xcd & 1) * 8 + (idx & 7), cg = (xcd >> 1) * 4 + (idx >> 3);
  const int jt = xcd * 4 + (idx & 3), c2 = idx >> 2;
  const int r = lane & 15, g = lane >> 4;
  {
    const char* pc; unsigned len;
    asm volatile("s_getpc_b64 %0\nfk_here:\n\ts_mov_b32 %1, fk_end-fk_here" : "=s"(pc), "=s"(len));
    pc = (const char*)((uintptr_t)pc & ~(uintptr_t)63);
    unsigned co = (unsigned)((idx * 4 + w) * 1024 + lane * 16);
    co = co + 16 <= len ? co : 0u;
    asm volatile("s_mov_b32 m0, %2\n\ts_nop 0\n\tglobal_load_lds_dwordx4 %0, %1" ::"v"(co), "s"(pc), "s"((unsigned)(L_AUX + 2048)) : "memory");
  }
  const __amdgpu_buffer_rsrc_t rs = __builtin_amdgcn_make_buffer_rsrc((void*)p.ws, 0, W_BYTES, 0x00020000);
  Lane q;
  q.rot = idx % 5;
  q.lw = (unsigned)(w * UNIT);
  q.lx = (unsigned)(w * 16384);
#pragma unroll
  for (int i = 0; i < 8; ++i) {
    const int e = i & 1, tt = (i >> 1) & 1, hh = i >> 2;
    q.xo[i] = (unsigned)((16 * e + 2 * (lane >> 3) + tt) * 1280 + hh * 128 + (((lane & 7) ^ (4 * e + g)) * 16));
  }
  q.Wb = (cg < 8 ? p.wA : p.wB) + (cg & 7) * 160 + (size_t)w * 32 * 3840;
  q.xb = p.x + (size_t)(rt * 128 + w * 32) * 1280;
  sfor<0, 8>([&](auto I) { load_x<0, I>(q); });
  STAMP(12);
#pragma unroll
  for (int i = 0; i < 5; ++i) {
    const int c = i * 64 + lane, rho = c / 10, cc = c % 10;
    q.woff[i] = (unsigned)((8 * ((rho >> 2) & 3) + 4 * (rho >> 4) + (rho & 3)) * 3840 + cc * 16);
  }
  sfor<0, 5>([&](auto I) { load_w<0, I>(q); });
  {
    const char* ab = sel4(w, p.gw, p.gw + 1024, (const char*)p.out, (const char*)p.ws);
    const unsigned ao = (unsigned)((w == 1 ? r : lane) * 16);
    asm volatile("s_mov_b32 m0, %2\n\ts_nop 0\n\tglobal_load_lds_dwordx4 %0, %1 sc1" ::"v"(ao), "s"(ab), "s"((unsigned)(L_AUX + w * 1024)) : "memory");
  }
  {
    const int ln = 25 * (idx * 4 + w) + (lane < 25 ? lane : 0);
    q.Wg = (cg < 8 ? p.wA : p.wB) + ((xcd >> 1) & 1) * 640;
    q.tw = (unsigned)((ln / 5) * 3840 + (ln % 5) * 128);
  }
  STAMP(14);
  STAMP(15);
  sfor<0, 5>([&](auto I) { load_w<1, I>(q); });
  STAMP(13);
  u64 tag = 0, pollB = 0, pollA = 0;
  v4u wo[50];
  const int rj = (idx & 1) * 2;
  if (cg < 8) phase1<false>(p, q, L, rs, b, rt, cg, w, lane, c2, jt, rj, tag, pollB, pollA, wo);
  else phase1<true>(p, q, L, rs, b, rt, cg - 8, w, lane, c2, jt, rj, tag, pollB, pollA, wo);
  phase2<P2CHK>(p, L, rs, b, w, lane, jt, c2, rj, tag, pollB, pollA, wo);
  asm volatile("fk_end:");
}

extern "C" void kernel_launch(void* const* d_in, const int* in_sizes, int n_in, void* d_out, int out_size, void* d_ws, size_t ws_size,
                              hipStream_t stream) {
  Params p;
  p.x = (const char*)d_in[0];
  p.wA = (const char*)d_in[1];
  p.woA = (const char*)d_in[2];
  p.wB = (const char*)d_in[4];
  p.woB = (const char*)d_in[5];
  p.gw = (const char*)d_in[6];
  p.out = (char*)d_out;
  p.ws = (char*)d_ws;
  (void)hipFuncSetAttribute((const void*)fused_kernel, hipFuncAttributeMaxDynamicSharedMemorySize, LDS_BYTES);
  fused_kernel<<<256, 256, LDS_BYTES, stream>>>(p);
}
```

```cpp
#include <hip/hip_runtime.h>
#include <stdint.h>
#include <type_traits>

#define LAS __attribute__((address_space(3)))
#define GAS __attribute__((address_space(1)))
typedef short s16x4 __attribute__((ext_vector_type(4)));
typedef unsigned v4u __attribute__((ext_vector_type(4)));
typedef unsigned v2u __attribute__((ext_vector_type(2)));
typedef float f32x4 __attribute__((ext_vector_type(4)));
typedef float f32x2 __attribute__((ext_vector_type(2)));
typedef __bf16 bf2 __attribute__((ext_vector_type(2)));
typedef unsigned long long u64;

#define SLP 2
#define BDLY 8
#define GPOLL 546
#define P2CHKB 75
#define OPRE 5
#define TQM 184
#define PACKM 105
#define P2CHK 60

constexpr int UNIT = 5120, SLOT = 4 * UNIT;
constexpr int L_XST = 4 * SLOT;
constexpr int L_SCR = L_XST;
constexpr int L_SLOT4 = L_SCR + 40960;
constexpr int L_PACK = L_XST + 65536;
constexpr int L_AUX = L_PACK + 2 * UNIT;
constexpr int LDS_BYTES = 163840;
static_assert(L_SLOT4 + SLOT <= L_PACK && L_AUX + 4096 <= LDS_BYTES, "lds map");
constexpr int slot_of(int s) { return s <= 10 ? (s & 3) : s == 11 ? 4 : (s - 9) & 3; }
constexpr int slot_base(int s) { return slot_of(s) == 4 ? L_SLOT4 : slot_of(s) * SLOT; }
constexpr int W_EPOCH = 0;
constexpr int W_FE = 131072, W_FO = 2 * 131072, W_F1 = 3 * 131072;
constexpr int W_PART = 1 << 20, PART_STR = 4096;
constexpr int W_HALO = 2 << 20, HALO_STR = 10240;
constexpr int W_WOP = 4 << 20;
constexpr int W_OB = 6 << 20;
constexpr int W_BYTES = W_OB + 128 * 40 * 1024;
constexpr int SC1 = 16;
constexpr unsigned OOB = 0x40000000u;
constexpr unsigned SPIN_CAP = 1u << 16;

struct Params {
  const char *x, *wA, *woA, *wB, *woB, *gw;
  char *out, *ws;
};

template <int I, int N, class F> __device__ __forceinline__ void sfor(F&& f) {
  if constexpr (I < N) { f(std::integral_constant<int, I>{}); sfor<I + 1, N>(f); }
}
template <int I, int N, class F> __device__ __forceinline__ void sfor2(F&& f) {
  if constexpr (N - I <= 20) sfor<I, N>(f);
  else { sfor<I, I + 20>(f); sfor2<I + 20, N>(f); }
}
#define IC(n) std::integral_constant<int, (n)>{}
__device__ __forceinline__ unsigned pk2(float a, float b) {
  f32x2 t = {a, b};
  return __builtin_bit_cast(unsigned, __builtin_convertvector(t, bf2));
}
#define MF "v_mfma_f32_16x16x32_bf16 "
__device__ __forceinline__ void mm_va(f32x4& d, v4u v, v4u a) { asm volatile(MF "%0, %1, %2, %0" : "+v"(d) : "v"(v), "a"(a)); }
__device__ __forceinline__ void mm_av(f32x4& d, v4u a, v4u v) { asm volatile(MF "%0, %1, %2, %0" : "+v"(d) : "a"(a), "v"(v)); }
__device__ __forceinline__ void mm_vv(f32x4& d, v4u a, v4u b) { asm volatile(MF "%0, %1, %2, %0" : "+v"(d) : "v"(a), "v"(b)); }
__device__ __forceinline__ void mm_va0(f32x4& d, v4u v, v4u a) { asm volatile(MF "%0, %1, %2, 0" : "=&v"(d) : "v"(v), "a"(a)); }
__device__ __forceinline__ void mm_av0(f32x4& d, v4u a, v4u v) { asm volatile(MF "%0, %1, %2, 0" : "=&v"(d) : "a"(a), "v"(v)); }
__device__ __forceinline__ void mm_vv0(f32x4& d, v4u a, v4u b) { asm volatile(MF "%0, %1, %2, 0" : "=&v"(d) : "v"(a), "v"(b)); }
__device__ __forceinline__ void mm_vac(f32x4& d, v4u v, v4u a, f32x4 c) { asm volatile(MF "%0, %1, %2, %3" : "=&v"(d) : "v"(v), "a"(a), "v"(c)); }
__device__ __forceinline__ void mm_avc(f32x4& d, v4u a, v4u v, f32x4 c) { asm volatile(MF "%0, %1, %2, %3" : "=&v"(d) : "a"(a), "v"(v), "v"(c)); }
__device__ __forceinline__ void mm_vvc(f32x4& d, v4u a, v4u b, f32x4 c) { asm volatile(MF "%0, %1, %2, %3" : "=&v"(d) : "v"(a), "v"(b), "v"(c)); }
#define NOPS(n) asm volatile("s_nop " #n)
#define PIN(x) asm volatile("" : "+v"(x))

template <int IMM> __device__ __forceinline__ void dma16(const char* sbase, unsigned voff, unsigned ldsbase) {
  asm volatile("s_add_u32 m0, %2, %3\n\ts_nop 0\n\tglobal_load_lds_dwordx4 %0, %1" ::"v"(voff), "s"(sbase), "s"(ldsbase), "i"(IMM) : "memory", "scc");
}
template <int IMM> __device__ __forceinline__ void dma_m0(unsigned ldsbase) { asm volatile("s_add_u32 m0, %0, %1" ::"s"(ldsbase), "i"(IMM) : "memory", "scc"); }
__device__ __forceinline__ void dma_go(const char* sbase, unsigned voff) { asm volatile("global_load_lds_dwordx4 %0, %1" ::"v"(voff), "s"(sbase) : "memory"); }
template <int OFF> __device__ __forceinline__ void dma_sc1(const char* sbase, unsigned voff) {
  asm volatile("global_load_lds_dwordx4 %0, %1 offset:%2 sc1" ::"v"(voff), "s"(sbase), "i"(OFF) : "memory");
}
__device__ __forceinline__ void touch(const char* sbase, unsigned voff) {
  asm volatile("s_mov_b32 m0, %2\n\ts_nop 0\n\tglobal_load_lds_dword %0, %1" ::"v"(voff), "s"(sbase), "i"(L_AUX + 2048) : "memory");
}
template <int N> __device__ __forceinline__ void wait_vm() { asm volatile("s_waitcnt vmcnt(%0)" ::"i"(N) : "memory"); }
template <int N> __device__ __forceinline__ void wait_vm_p(u64& v) { asm volatile("s_waitcnt vmcnt(%1)" : "+v"(v) : "i"(N) : "memory"); }
__device__ __forceinline__ void wait_lgkm() { asm volatile("s_waitcnt lgkmcnt(0)" ::: "memory"); }
__device__ __forceinline__ v2u trread(LAS char* p) {
  return __builtin_bit_cast(v2u, __builtin_amdgcn_ds_read_tr16_b64_v4i16((LAS s16x4*)p));
}
#define SB0() __builtin_amdgcn_sched_barrier(0)
#define BAR() __builtin_amdgcn_s_barrier()
#define LD128S(voff, soff) __builtin_amdgcn_raw_buffer_load_b128(rs, (voff), (soff), SC1)
#define ST128(v, off) __builtin_amdgcn_raw_buffer_store_b128((v), rs, (off), 0, SC1)
#define STAMP(i) do {} while (0)
#define STUSE
#define STARG
#define STPASS

__device__ __forceinline__ u64 ld_flag(char* ws, unsigned off) {
  return __hip_atomic_load((GAS u64*)(ws + off), __ATOMIC_RELAXED, __HIP_MEMORY_SCOPE_AGENT);
}
__device__ __forceinline__ void poll_async(u64& v, char* ws, unsigned off) {
  asm volatile("global_load_dwordx2 %0, %1, %2 sc1" : "+v"(v) : "v"(off), "s"(ws) : "memory");
}
__device__ __forceinline__ void settle_flags(char* ws, unsigned off, u64 tag, u64 v) {
  if (!__all(v == tag)) {
    unsigned spins = 0;
    bool ok;
#pragma unroll 1
    do { __builtin_amdgcn_s_sleep(SLP); ok = __all(ld_flag(ws, off) == tag); } while (!ok && ++spins < SPIN_CAP);
  }
  __builtin_amdgcn_fence(__ATOMIC_ACQUIRE, "wavefront");
}
template <class T> __device__ __forceinline__ T sel4(int i, T a, T b, T c, T d) {
  const bool lo = (i & 1) != 0, hi = (i & 2) != 0;
  const T x = lo ? b : a, y = lo ? d : c;
  return hi ? y : x;
}
__device__ __forceinline__ float asf(unsigned u) { return __builtin_bit_cast(float, u); }
__device__ __forceinline__ unsigned asu(float f) { return __builtin_bit_cast(unsigned, f); }
#define vmax(a, b) __builtin_amdgcn_fmed3f((a), (b), vinf)
__device__ __forceinline__ float vmax3(float a, float b, float c) { float r; asm("v_max3_f32 %0, %1, %2, %3" : "=v"(r) : "v"(a), "v"(b), "v"(c)); return r; }
constexpr float EPS = 1e-6f, LOG2E = 1.4426950408889634f;
__device__ __forceinline__ float phi_a(float t) { return __builtin_fmaf(t, LOG2E, -EPS * LOG2E); }
__device__ __forceinline__ float phi_b(float u) { return __builtin_amdgcn_fmed3f(__builtin_amdgcn_exp2f(u), 0.f, 1.f); }
#define phi_c(t) vmax((t), EPS)

constexpr int st_ch(int s) { return s >= 10 ? 2 : (s == 1 || s == 5 || s >= 7) ? 1 : 0; }
constexpr int st_kb(int s) { return s >= 10 ? s - 10 : s >= 7 ? s - 5 : s == 6 ? 4 : s == 5 ? 1 : s >= 2 ? s - 1 : 0; }
constexpr bool fr_tm(int f) { return f / 20 == 6 || f / 20 == 9; }
constexpr int fr_k(int f) { return f >= 240 ? 8 + (f - 240) % 12 : fr_tm(f) ? 16 + f % 4 : 4 * st_kb(f / 20) + (f % 20) / 5; }
constexpr int fr_j(int f) { return f >= 240 ? ((f - 240) / 12 + 4) % 5 : fr_tm(f) ? ((f % 20) / 4 + 4) % 5 : f % 5; }
constexpr int fr_s(int f) { return f >= 240 ? 10 + fr_k(f) / 4 : f / 20; }
constexpr int AHEAD = 8, RING = 12;

struct Lane {
  unsigned woff[5], xo[8];
  const char *Wb, *xb;
  unsigned lw, lx;
  int rot;
  const char* Wg;
  unsigned tw;
};
__device__ __forceinline__ int rot5(int rot, int pos) { const int k = pos + rot; return k >= 5 ? k - 5 : k; }
template <int S> __device__ __forceinline__ const char* w_src(const Lane& q) {
  return q.Wb + (st_ch(S) == 0 ? 1280 : st_ch(S) == 1 ? 2560 : 0) + (size_t)rot5(q.rot, st_kb(S)) * (128 * 3840);
}
template <int S, int I> __device__ __forceinline__ void load_w(const Lane& q) { dma16<slot_base(S) + I * 1024>(w_src<S>(q), q.woff[I], q.lw); }
template <int S, int I> __device__ __forceinline__ void load_x(const Lane& q) {
  dma16<L_XST + (S & 1) * 8192 + I * 1024>(q.xb + rot5(q.rot, S) * 256, q.xo[I], q.lx);
}
__device__ __forceinline__ unsigned fo_of(int jt, int w, int lane, int br) {
  return W_FO + (((jt >> 1) * 16 + br * 8 + 2 * w + (lane & 1)) * 4 + 2 * (jt & 1) + ((lane >> 1) & 1)) * 128;
}
__device__ __forceinline__ int o_off(int jt, int w, int rj, int hb, int a, int i) {
  return W_OB + ((4 * jt + ((a + w + 1) & 3)) * 40 + (hb ? 0 : 20) + 5 * w + rot5(rj, i)) * 1024;
}
constexpr int wo_hb(int z) { return z / 5 < 6 ? ((z / 5) & 1) : (z / 5 < 8 ? 0 : 1); }
constexpr int wo_i(int z) { return z / 5 < 6 ? (z / 10) : (z / 5 < 8 ? z / 5 - 3 : z / 5 - 5); }
constexpr int wo_idx(int z) { return wo_hb(z) * 25 + wo_i(z) * 5 + z % 5; }

template <bool ISB>
__device__ __forceinline__ void phase1(const Params& p, const Lane& q, LAS char* L, __amdgpu_buffer_rsrc_t rs, int b, int rt, int hg, int w, int lane, int c2,
                                       int jt2, int rj, u64& tag, v4u (&of0)[4][5], v4u (&wo)[50] STARG) {
  const int r = lane & 15, g = lane >> 4;
  const int cg = (ISB ? 8 : 0) + hg;
  const unsigned lw = q.lw;
  auto lbase = [&](int off) { unsigned a = (unsigned)(uintptr_t)L + (unsigned)off; PIN(a); return (LAS char*)(uintptr_t)a; };
  const int o1 = (lane >> 2) * 160 + (lane & 3) * 40, o2 = (lane >> 2) * 160 + (lane & 3) * 8;
  LAS char* a1 = lbase(o1);
  LAS char* a2 = lbase(o2);
  LAS char* a1h = lbase(o1 + 3 * SLOT);
  LAS char* a2h = lbase(o2 + 3 * SLOT);
  LAS char* ax0 = lbase(L_XST + w * 16384 + r * 128 + ((g ^ (r >> 1)) * 16));
  LAS char* ax1 = lbase(L_XST + w * 16384 + r * 128 + (((4 + g) ^ (r >> 1)) * 16));
  LAS char* ag = L + L_AUX + g * 16;
  LAS char *a1s, *a2s;
  const int unit = b + 256 * w;
  unsigned lane16 = lane * 16, fl0 = lane < 8 ? lane * 16u : OOB;
  PIN(lane16); PIN(fl0);

  v4u xf[20][2], wf[RING], gf2[2][4];
  f32x4 acc[3][2][5], gacc[2];
  f32x4 eps4 = {EPS, EPS, EPS, EPS};
  PIN(eps4);
  float vinf = __builtin_inff();
  PIN(vinf);

  auto readW = [&](auto F, auto T) {
    constexpr int f = F, t = T, ks = fr_k(f), jt = fr_j(f), sl = slot_of(fr_s(f));
    constexpr int off = (ks % 4) * UNIT + t * 2560 + (jt == 4 ? 32 : 40 * jt);
    v2u v;
    if constexpr (sl == 4) v = trread((jt == 4 ? a1s : a2s) + off);
    else if constexpr (sl == 3) v = trread((jt == 4 ? a1h : a2h) + off);
    else v = trread((jt == 4 ? a1 : a2) + sl * SLOT + off);
    if constexpr (t == 0) { wf[f % RING].x = v.x; wf[f % RING].y = v.y; }
    else { wf[f % RING].z = v.x; wf[f % RING].w = v.y; }
  };
  auto readX = [&](auto S, auto U) {
    constexpr int s = S, u = U, j = u / 3, k = u % 3;
    if constexpr (k < 2) xf[4 * s + j][k] = *(LAS v4u*)(((j & 1) ? ax1 : ax0) + (s & 1) * 8192 + (j >> 1) * 4096 + k * 2048);
    else gf2[s & 1][j] = *(LAS v4u*)(ag + rot5(q.rot, s) * 256 + j * 64);
  };

  v2u ept;
  v4u tag4;
  u64 poll = 0;
  unsigned poll0_off, poll1_off, st_off, o_st, o_st1, o_st2, foB;
  LAS char *aW, *aR;
  const int rp = rt > 0 ? rt - 1 : 0;
  {
    const int li = lane < 10 ? lane : 0;
    const int pu = c2 * 40 + (li / 5) * 20 + 5 * w + li % 5;
    poll0_off = W_FE + ((pu & 255) * 4 + (pu >> 8)) * 128;
    poll1_off = ISB ? W_F1 + ((cg * 16 + rp) * 4 + 3) * 128 : W_F1 + ((cg * 16 + r) * 4 + w) * 128;
    PIN(poll0_off); PIN(poll1_off);
  }
  const char* halo_src = p.ws + W_HALO + (hg * 16 + rp) * HALO_STR;
  const int wo_b = W_WOP + (c2 * 40 + 5 * w) * 5 * 1024;
  auto wo_soff = [&](int z) { return wo_b + (wo_hb(z) ? 0 : 102400) + rot5(rj, wo_i(z)) * 5120 + ((z % 5) >> 2) * 4096; };
  auto ld_wo = [&](auto Z) { wo[wo_idx(Z)] = LD128S(lane16 + ((Z % 5) & 3) * 1024, wo_soff(Z)); };
  auto flag = [&](int off) { __builtin_amdgcn_raw_buffer_store_b128(tag4, rs, fl0, off, SC1); };
  constexpr int NWO1 = 40;
  float gate[2];
  v4u Kf[5], Vf[5], Ej[5], K4m[4], V4m, pst, gl[16];
  v2u Pq[2][5];
  f32x4 kvT, kvU, kvW, kvX, kvZ, rd[8];
  float ta[2], tb[2], tc[2];
  constexpr int KVS = L_SCR + 32768, KZERO = KVS + 4096;
  LAS v2u *kvs, *kvw, *kvz, *aU[4], *aWt[4], *aX[4], *aZ[4];
  v4u A1, A2;
  v4u kx[5], kpv[5], vx5[5], Vb[2][5], Kc[2], Kp[2], Qf[2], Pb[2][2], V4b[2], ones4;
  v2u q4[2];
  f32x4 biasC, biasP, Sc[2], Sp[2];
  float sm[2][4], mx[2], mt;
  unsigned mC[2], mP[2], pp[2][2];
  f32x4 num[2], den[2], num4[2];
  float inv[2][4];
  v4u F[2][2];

  auto fin_op = [&](auto H, auto Z) {
    constexpr int h = H, z = Z;
    if constexpr (z < 2) { inv[z][h] = __builtin_amdgcn_rcpf(den[z][0]); PIN(inv[z][h]); PIN(den[z]); }
    else if constexpr (z < 4) { inv[z - 2][h] *= gate[z - 2]; PIN(inv[z - 2][h]); }
    else if constexpr (z < 12) { constexpr int t2 = (z - 4) >> 2, e = (z - 4) & 3; float u = num[t2][e] * inv[t2][h]; PIN(u); num[t2][e] = u; }
    else { constexpr int t2 = (z - 12) >> 1, e = (z - 12) & 1; unsigned o = pk2(num[t2][2 * e], num[t2][2 * e + 1]); PIN(o); F[t2][h >> 1][2 * (h & 1) + e] = o; }
  };
  constexpr float CS = 0.22360679774997896f * LOG2E;
  auto att_op = [&](auto H, auto Z) {
    constexpr int h = H, z = Z;
    if constexpr (z < 4) { constexpr int t2 = z >> 1, e = z & 1; unsigned o = pk2(acc[2][t2][h][2 * e], acc[2][t2][h][2 * e + 1]); PIN(o); Qf[t2][e] = o; }
    else if constexpr (z == 4) { PIN(Qf[0]); PIN(Qf[1]); NOPS(1); }
    else if constexpr (z < 9) { constexpr int t2 = (z - 5) >> 1; if constexpr ((z - 5) & 1) mm_vvc(Sp[t2], Kp[t2], Qf[t2], biasP); else mm_vvc(Sc[t2], Kc[t2], Qf[t2], biasC); }
    else if constexpr (z < 17) { constexpr int t2 = (z - 9) >> 2, e = (z - 9) & 3; unsigned o = (r >> 2) == h ? Vb[t2][4][e] : 0u; PIN(o); V4b[t2][e] = o; }
    else if constexpr (z < 25) { constexpr int t2 = (z - 17) >> 2, e = (z - 17) & 3; sm[t2][e] = vmax(Sc[t2][e], Sp[t2][e]); PIN(sm[t2][e]); }
    else if constexpr (z < 27) { mx[z - 25] = vmax3(sm[z - 25][0], sm[z - 25][1], sm[z - 25][2]); PIN(mx[z - 25]); }
    else if constexpr (z < 29) { mx[z - 27] = vmax(mx[z - 27], sm[z - 27][3]); PIN(mx[z - 27]); }
    else if constexpr (z == 29) {
      const auto s = __builtin_amdgcn_permlane16_swap(asu(mx[0]), asu(mx[1]), false, false);
      mt = vmax(asf(s[0]), asf(s[1])); PIN(mt);
    } else if constexpr (z == 30) {
      const auto s = __builtin_amdgcn_permlane32_swap(asu(mt), asu(mt), false, false);
      mt = vmax(asf(s[0]), asf(s[1])) * -CS; PIN(mt);
    } else if constexpr (z == 31) {
      const auto s = __builtin_amdgcn_permlane16_swap(asu(mt), asu(mt), false, false);
      mx[0] = asf(s[0]); mx[1] = asf(s[1]); PIN(mx[0]); PIN(mx[1]);
    }
    else if constexpr (z < 40) { constexpr int t2 = (z - 32) >> 2, e = (z - 32) & 3; sm[t2][e] = __builtin_fmaf(sm[t2][e], CS, mx[t2]); PIN(sm[t2][e]); }
    else if constexpr (z < 48) { constexpr int t2 = (z - 40) >> 2, e = (z - 40) & 3; sm[t2][e] = __builtin_amdgcn_exp2f(sm[t2][e]); PIN(sm[t2][e]); }
    else if constexpr (z < 52) { constexpr int t2 = (z - 48) >> 1, e = (z - 48) & 1; pp[t2][e] = pk2(sm[t2][2 * e], sm[t2][2 * e + 1]); PIN(pp[t2][e]); }
    else if constexpr (z < 60) { constexpr int t2 = (z - 52) >> 2, e = (z - 52) & 3; unsigned o = pp[t2][e & 1] & (e < 2 ? mP[e & 1] : mC[e & 1]); PIN(o); Pb[h & 1][t2][e] = o; }
    else if constexpr (z == 60) { PIN(Pb[h & 1][0]); PIN(Pb[h & 1][1]); PIN(V4b[0]); PIN(V4b[1]); PIN(Vb[0][h]); PIN(Vb[1][h]); NOPS(1); }
    else if constexpr (z < 63) mm_vv0(den[z - 61], ones4, Pb[h & 1][z - 61]);
    else if constexpr (z < 65) mm_vv0(num[z - 63], Vb[z - 63][h], Pb[h & 1][z - 63]);
    else if constexpr (z < 67) { if constexpr (h == 0) mm_vv0(num4[z - 65], V4b[z - 65], Pb[h & 1][z - 65]); else mm_vv(num4[z - 65], V4b[z - 65], Pb[h & 1][z - 65]); }
  };
  auto pv_op = [&](auto H, auto U) {
    constexpr int u = U;
    if constexpr (u >= 6 && u < 10) { att_op(H, IC(9 + 2 * (u - 6))); att_op(H, IC(10 + 2 * (u - 6))); }
    if constexpr (u == 10) { att_op(H, IC(60)); att_op(H, IC(61)); }
    if constexpr (u >= 11 && u < 16) att_op(H, IC(51 + u));
    if constexpr (u >= 16) { fin_op(H, IC(2 * (u - 16))); fin_op(H, IC(2 * (u - 16) + 1)); }
  };
  auto att_pre = [&](auto H, auto Z) {
    constexpr int h = H, z = Z;
    if constexpr (z < 2) { Kc[z] = v4u{kx[h][2 * z], kx[h][2 * z + 1], kx[4][2 * z], kx[4][2 * z + 1]}; PIN(Kc[z]); }
    else if constexpr (z < 4) { constexpr int t2 = z - 2; Kp[t2] = v4u{kpv[h][2 * t2], kpv[h][2 * t2 + 1], kpv[4][2 * t2], kpv[4][2 * t2 + 1]}; PIN(Kp[t2]); }
    else { constexpr int t2 = z - 4; unsigned lo = g == h ? q4[t2].x : 0u, hi = g == h ? q4[t2].y : 0u; PIN(lo); PIN(hi); Qf[t2].z = lo; Qf[t2].w = hi; }
  };
  auto phi_op = [&](auto Z, const f32x4& t, auto H, unsigned& out) {
    constexpr int z = Z, h = H;
    if constexpr (z < 2) { ta[z] = phi_a(t[2 * h + z]); PIN(ta[z]); }
    else if constexpr (z < 4) { tb[z - 2] = phi_b(ta[z - 2]); PIN(tb[z - 2]); }
    else if constexpr (z < 6) { tc[z - 4] = phi_c(t[2 * h + z - 4]); PIN(tc[z - 4]); }
    else if constexpr (z < 8) { tc[z - 6] += tb[z - 6]; PIN(tc[z - 6]); }
    else { out = pk2(tc[0], tc[1]); PIN(out); }
  };

  LAS char* apk = lbase(o1 + L_PACK + (w & 1) * UNIT);
  unsigned pk_dst = lane16 + W_WOP + unit * 5120;
  PIN(pk_dst);
  if (unit < 320) wait_vm<10>(); else wait_vm<5>();
  BAR();
  STAMP(1);
  ept = *(LAS v2u*)(L + L_AUX + 3072);
  sfor<0, 3>([&](auto U) { readX(IC(0), U); });
  sfor<0, AHEAD>([&](auto F_) { readW(F_, IC(0)); readW(F_, IC(1)); });
  sfor<3, 12>([&](auto U) { readX(IC(0), U); });
  tag = (((u64)(unsigned)__builtin_amdgcn_readfirstlane(ept.y) << 32) | (unsigned)__builtin_amdgcn_readfirstlane(ept.x)) + 1ull;
  tag4 = v4u{(unsigned)tag, (unsigned)(tag >> 32), (unsigned)tag, (unsigned)(tag >> 32)};
  PIN(tag4);
  SB0();

  sfor2<0, 600>([&](auto M) {
    constexpr int m = M, f = m / 2, tt = m & 1, ks = fr_k(f), jt = fr_j(f), ch = st_ch(fr_s(f));
    constexpr int sb = (m + 16) / 40 - 1, mb = (m + 16) % 40;
    constexpr int tv = (m - 368) / 8, uv = (m - 368) % 8, jv = (tv + 4) % 5;
    constexpr int tq = (m - 504) / 24, uq = (m - 504) % 24, jq = (tq + 4) % 5;
    if constexpr (mb == 0 && sb >= 0 && sb <= 11) {
      constexpr int cntA[12] = {13, 13, 13, 9, 13, 7, 7, 6, 10, 14, 18, 6};
      constexpr int cntB[12] = {13, 13, 13, 9, 13, 7, 7, 6, 10, 15, 20, 5};
      constexpr int cnt = ISB ? cntB[sb] : cntA[sb];
      if constexpr (sb == 3) { if (unit < 320) wait_vm<cnt + 5>(); else wait_vm<cnt>(); }
      else if constexpr (ISB && sb == 7) { if (w == 3) wait_vm<cnt + 5>(); else wait_vm<cnt>(); }
      else wait_vm_p<cnt>(poll);
      BAR();
      SB0();
    }
    if constexpr (m % 8 == 0 && m > 0 && f + AHEAD <= 300) { __builtin_amdgcn_s_waitcnt(0xC07F | (8 << 8)); SB0(); }
    {
      f32x4& d = acc[ch][tt][jt];
      const v4u& wv = wf[f % RING];
      if constexpr (ch == 2 || (ch == 0 && ISB)) {
        if constexpr (ks != 0) mm_va(d, wv, xf[ks][tt]);
        else if constexpr (ISB) mm_va0(d, wv, xf[ks][tt]);
        else mm_vac(d, wv, xf[ks][tt], eps4);
      } else {
        if constexpr (ks != 0) mm_av(d, xf[ks][tt], wv);
        else if constexpr (ch == 0) mm_avc(d, xf[ks][tt], wv, eps4);
        else mm_av0(d, xf[ks][tt], wv);
      }
    }
    SB0();
    if constexpr (f + AHEAD < 300) readW(IC(f + AHEAD < 300 ? f + AHEAD : 0), IC(tt));
    if constexpr (((sb >= 1 && sb <= 3) || sb == 5) && mb < 12) readX(IC(sb == 5 ? 4 : sb), IC(mb));
    SB0();
    if constexpr (ch == 0 && m % 10 == 9) {
      constexpr int kg = 4 * st_kb(m / 40) + (m % 40) / 10;
      if constexpr (kg == 0) { mm_va0(gacc[0], gf2[0][0], xf[0][0]); mm_va0(gacc[1], gf2[0][0], xf[0][1]); }
      else { mm_va(gacc[0], gf2[(kg >> 2) & 1][kg & 3], xf[kg][0]); mm_va(gacc[1], gf2[(kg >> 2) & 1][kg & 3], xf[kg][1]); }
    }
    auto split_w = [&](auto S, auto P0, auto ST) {
      constexpr int p0 = P0, st = ST;
      if constexpr (mb >= p0 && (mb - p0) / st < 5) {
        if constexpr ((mb - p0) % st == 0) dma_m0<slot_base(S) + (mb - p0) / st * 1024>(lw);
        if constexpr ((mb - p0) % st == 1) dma_go(w_src<S>(q), q.woff[(mb - p0) / st]);
      }
    };
    auto split_x = [&](auto XS, auto I0, auto N, auto P0, auto ST) {
      constexpr int p0 = P0, st = ST, xs = XS;
      if constexpr (mb >= p0 && (mb - p0) / st < N) {
        constexpr int i = I0 + (mb - p0) / st;
        if constexpr ((mb - p0) % st == 0) dma_m0<L_XST + (xs & 1) * 8192 + i * 1024>(q.lx);
        if constexpr ((mb - p0) % st == 1) dma_go(q.xb + rot5(q.rot, xs) * 256, q.xo[i]);
      }
    };
    if constexpr (m < 8) load_x<1, m>(q);
    if constexpr (m >= 10 && m < 20 && m % 2 == 0) load_w<2, (m - 10) / 2>(q);
    if constexpr (sb == 0) { split_x(IC(2), IC(0), IC(8), IC(1), IC(3)); split_w(IC(3), IC(25), IC(3)); }
    if constexpr (sb == 1) { split_w(IC(4), IC(2), IC(4)); split_x(IC(3), IC(0), IC(7), IC(25), IC(2)); if constexpr (mb == 39) load_x<3, 7>(q); }
    if constexpr (sb == 2 || sb == 3) { split_w(IC(sb + 3), IC(2), IC(4)); split_x(IC(4), IC((sb - 2) * 4), IC(4), IC(25), IC(4)); }
    if constexpr (sb >= 4 && sb <= 6) split_w(IC(sb + 3), IC(2), IC(8));
    if constexpr (sb == 7) { split_w(IC(10), IC(2), IC(4)); split_w(IC(11), IC(21), IC(4)); }
    if constexpr (sb == 8 || sb == 9) split_w(IC(sb + 4), IC(2), IC(8));
    if constexpr (sb == 10) split_w(IC(14), IC(1), IC(3));
    if constexpr (m == TQM) touch(q.Wg, q.tw);
    if constexpr (m == PACKM) {
      if (unit < 320) {
        v4u pv[5];
        sfor<0, 5>([&](auto J) {
          const v2u lo = trread(apk + 8 * J), hi = trread(apk + 2560 + 8 * J);
          pv[J] = v4u{lo.x, lo.y, hi.x, hi.y};
        });
        sfor<0, 5>([&](auto J) { ST128(pv[J], pk_dst + J * 1024); });
      }
    }
    if constexpr (m == 185) flag(W_FE + (b * 4 + w) * 128);
    if constexpr (m == 225) __builtin_amdgcn_raw_buffer_store_b128(~tag4, rs, fl0, W_FO + ((rt * 16 + cg) * 4 + w) * 128, SC1);
    if constexpr (m == 229) __builtin_amdgcn_raw_buffer_store_b128(~tag4, rs, fl0, W_F1 + (ISB ? 131072 : 0) + ((cg * 16 + rt) * 4 + w) * 128, SC1);
    if constexpr (m == 265) poll_async(poll, p.ws, poll0_off);
    if constexpr (m == 345) settle_flags(p.ws, poll0_off, tag, poll);
    if constexpr (m == 85) { a1s = lbase(o1 + L_SLOT4); a2s = lbase(o2 + L_SLOT4); }
    if constexpr (m == 87) {
      aW = lbase(ISB ? L_SCR + ((w + 1) & 3) * 10240 + lane * 16 : L_SCR + w * 8192 + lane * 16);
      aR = lbase(ISB ? L_SCR + w * 10240 + lane * 16 : L_SCR + w * 2048 + lane * 16);
      st_off = ISB ? W_HALO + (hg * 16 + rt) * HALO_STR + lane * 16 : W_PART + (hg * 16 + rt) * PART_STR + w * 1024 + lane * 16;
      PIN(st_off);
    }
    if constexpr (m == 450) {
      const int fb = (ISB ? 20 : 0) + (hg >> 1) * 5, TT = rt * 8 + 2 * w;
      o_st = lane16 + W_OB + (TT * 40 + fb + ((hg & 1) ? 3 : 0)) * 1024;
      o_st2 = W_OB + ((TT + (g & 1)) * 40 + fb + 2) * 1024 + ((hg & 1) * 2 + (g >> 1)) * 256 + r * 16;
      foB = fo_of(jt2, w, lane, 1);
      PIN(o_st); PIN(o_st2); PIN(foB);
    }
    if constexpr (m == 454) { o_st1 = o_st + 40960; PIN(o_st1); }
    if constexpr (m == 286) { PIN(gacc[0]); PIN(gacc[1]); }
    if constexpr (m == 483 || m == 487) {
      constexpr int t2 = (m - 483) / 4;
      gate[t2] = __builtin_amdgcn_rcpf(1.f + __builtin_amdgcn_exp2f(gacc[t2][0] * (ISB ? LOG2E : -LOG2E)));
      PIN(gate[t2]);
    }
    if constexpr (sb >= 8 && sb <= 9 && mb % 8 == 6 && mb < 32) ld_wo(IC((sb - 8) * 4 + mb / 8));
    if constexpr (m >= 445 && (m - 445) % 4 == 0 && 8 + (m - 445) / 4 < NWO1) ld_wo(IC(8 + (m - 445) / 4));

    if constexpr (!ISB) {
      if constexpr (m >= 204 && m < 224 && m % 4 == 0) { const unsigned one = (r == (m - 204) / 4) ? 0x3F803F80u : 0u; Ej[(m - 204) / 4] = v4u{one, one, one, one}; PIN(Ej[(m - 204) / 4]); }
      if constexpr (m >= 252 && m < 387) {
        sfor<(m - 252) * 4 / 3, (m - 251) * 4 / 3>([&](auto Z) {
          constexpr int z = Z, kt = (z / 36 + 4) % 5, pr = z % 36 / 9, t2 = pr >> 1, h = pr & 1;
          unsigned o = 0;
          phi_op(IC(z % 9), acc[0][t2][kt], IC(h), o);
          if constexpr (z % 9 == 8) Kf[kt][2 * t2 + h] = o;
          if constexpr (z % 36 == 35) PIN(Kf[kt]);
        });
      }
      if constexpr (m >= 281 && m <= 389 && (m - 281) % 27 == 0) {
        constexpr int kt = ((m - 281) / 27 + 4) % 5;
        if constexpr (m == 281) mm_vv0(kvZ, Kf[kt], Ej[kt]); else mm_vv(kvZ, Kf[kt], Ej[kt]);
      }
      if constexpr (m >= 282 && m < 362 && (m - 282) % 5 == 0) {
        constexpr int z = (m - 282) / 5;
        unsigned o = (r >> 2) == (z >> 2) ? Kf[4][z & 3] : 0u;
        PIN(o);
        K4m[z >> 2][z & 3] = o;
      }
      LAS f32x4* red = (LAS f32x4*)aW;
      if constexpr (m >= 368 && tv < 5) {
        if constexpr (uv == 3 || uv == 4) {
          constexpr int t2 = uv - 3;
          unsigned lo = pk2(acc[1][t2][jv][0], acc[1][t2][jv][1]), hi = pk2(acc[1][t2][jv][2], acc[1][t2][jv][3]);
          PIN(lo); PIN(hi);
          Vf[jv][2 * t2] = lo; Vf[jv][2 * t2 + 1] = hi;
        }
        if constexpr (uv == 5) { PIN(Vf[jv]); if constexpr (tv > 0) PIN(K4m[jv & 3]); }
        if constexpr (tv == 0) { if constexpr (uv == 6) mm_vv0(kvX, Kf[4], Vf[4]); }
        else {
          if constexpr (uv < 2) {
            unsigned lo = (r >> 2) == jv ? Vf[4][2 * uv] : 0u, hi = (r >> 2) == jv ? Vf[4][2 * uv + 1] : 0u;
            PIN(lo); PIN(hi);
            V4m[2 * uv] = lo; V4m[2 * uv + 1] = hi;
          }
          if constexpr (uv == 2) { PIN(V4m); if constexpr (tv == 1) red[6 * 64] = kvX; else red[(jv - 1) * 64] = kvT; }
          if constexpr (uv == 4) { if constexpr (tv == 1) mm_vv0(kvW, Kf[jv], V4m); else mm_vv(kvW, Kf[jv], V4m); }
          if constexpr (uv == 6) mm_vv0(kvT, Kf[jv], Vf[jv]);
          if constexpr (uv == 7) { if constexpr (tv == 1) mm_vv0(kvU, K4m[jv & 3], Vf[jv]); else mm_vv(kvU, K4m[jv & 3], Vf[jv]); }
        }
      }
      if constexpr (m == 397) red[7 * 64] = kvZ;
      if constexpr (m == 409) red[5 * 64] = kvW;
      if constexpr (m == 412) red[3 * 64] = kvT;
      if constexpr (m == 415) red[4 * 64] = kvU;
      if constexpr (m == 423) asm volatile("s_waitcnt lgkmcnt(8)" ::: "memory");
      if constexpr (m >= 424 && m < 432) rd[m - 424] = *(LAS f32x4*)(aR + ((m - 424) >> 1) * 8192 + ((m - 424) & 1) * 1024);
      if constexpr (m >= 433 && m < 441) {
        sfor<0, 3>([&](auto E) {
          constexpr int z = 3 * (m - 433) + E, qq = z >> 2, e = z & 3;
          float s = rd[qq & 1][e] + rd[2 + qq][e]; PIN(s);
          rd[qq & 1][e] = s;
        });
      }
      if constexpr (m == 441 || m == 442) {
        unsigned lo = pk2(rd[m - 441][0], rd[m - 441][1]), hi = pk2(rd[m - 441][2], rd[m - 441][3]);
        PIN(lo); PIN(hi);
        pst[2 * (m - 441)] = lo; pst[2 * (m - 441) + 1] = hi;
      }
      if constexpr (m == 444) ST128(pst, st_off);
      if constexpr (m == 494) { wait_vm<13>(); flag(W_F1 + ((cg * 16 + rt) * 4 + w) * 128); }
      if constexpr (m == GPOLL) poll = ld_flag(p.ws, poll1_off);
      if constexpr (m == GPOLL + 37) settle_flags(p.ws, poll1_off, tag, poll);
      if constexpr (m >= GPOLL + 38 && m < GPOLL + 54) gl[m - GPOLL - 38] = LD128S(lane16, W_PART + (hg * 16 + m - GPOLL - 38) * PART_STR + w * 1024);
      if constexpr (m >= 466 && m < 482) {
        constexpr int h = (m - 466) >> 2, k = (m - 466) & 3;
        const bool gh = g == h, rh = (r >> 2) == h;
        if constexpr (k == 0) aU[h] = (LAS v2u*)lbase(gh ? KVS + (256 + lane) * 8 : KZERO);
        if constexpr (k == 1) aWt[h] = (LAS v2u*)lbase(rh ? KVS + (320 + lane) * 8 : KZERO);
        if constexpr (k == 2) aX[h] = (LAS v2u*)lbase(rh && gh ? KVS + (384 + lane) * 8 : KZERO);
        if constexpr (k == 3) aZ[h] = (LAS v2u*)lbase(gh ? KVS + (448 + g * 16 + 4) * 8 : KZERO);
      }
      if constexpr (m == 490) {
        kvs = (LAS v2u*)lbase(KVS + lane * 8); kvw = (LAS v2u*)lbase(KVS + w * 1024 + lane * 8); kvz = (LAS v2u*)lbase(KVS + (448 + g * 16) * 8);
        *(LAS v2u*)(L + KZERO) = v2u{0u, 0u};
      }
      if constexpr (m == 498) {
        const unsigned sv = (g == (r >> 2)) ? ((r & 1) ? 0x3F800000u : 0x00003F80u) : 0u;
        const unsigned s0_ = (r & 2) ? 0u : sv, s1_ = (r & 2) ? sv : 0u;
        A1 = v4u{s0_, s1_, 0u, 0u}; A2 = v4u{0u, 0u, s0_, s1_};
        PIN(A1); PIN(A2);
      }
      if constexpr (m >= 504 && uq >= 4 && uq < 22) {
        sfor<0, 2>([&](auto E) {
          constexpr int z = 2 * (uq - 4) + E, pr = z / 9, t2 = pr >> 1, h = pr & 1;
          unsigned o = 0;
          phi_op(IC(z % 9), acc[2][t2][jq], IC(h), o);
          if constexpr (z % 9 == 8) Pq[t2][jq][h] = o;
        });
      }
    } else {
      if constexpr (m >= 200 && m < 216 && m % 4 == 0) {
        constexpr int e = (m - 200) / 4;
        const int key = 4 * g + e;
        float bc = (key <= r && key + 7 >= r) ? 0.f : -1e30f, bp = (key >= r + 9 && (rt > 0 || w > 0)) ? 0.f : -1e30f;
        PIN(bc); PIN(bp);
        biasC[e] = bc; biasP[e] = bp;
      }
      if constexpr (m == 216 || m == 220) {
        constexpr int e = (m - 216) / 4;
        mC[e] = (4 * g + 2 * e <= r ? 0xFFFFu : 0u) | (4 * g + 2 * e + 1 <= r ? 0xFFFF0000u : 0u);
        mP[e] = ~mC[e];
        PIN(mC[e]); PIN(mP[e]);
      }
      if constexpr (m == 222) { ones4 = v4u{0x3F803F80u, 0x3F803F80u, 0x3F803F80u, 0x3F803F80u}; PIN(ones4); PIN(biasC); PIN(biasP); }
      if constexpr (m >= 248 && m < 288 && (m - 248) % 8 >= 4) {
        constexpr int kt = ((m - 248) / 8 + 4) % 5, z = (m - 248) % 8 - 4, t2 = z >> 1, e = z & 1;
        unsigned o = pk2(acc[0][t2][kt][2 * e], acc[0][t2][kt][2 * e + 1]);
        PIN(o);
        kx[kt][2 * t2 + e] = o;
      }
      if constexpr (m == 288) { if (w == 3) sfor<0, 5>([&](auto J) { ST128(kx[J], st_off + J * 1024); }); }
      if constexpr (m >= 348 && m < 368 && (m - 348) % 4 == 0) *(LAS v4u*)(aW + (m - 348) / 4 * 1024) = kx[(m - 348) / 4];
      if constexpr (m >= 368 && tv < 5) {
        if constexpr (uv == 3 || uv == 4) {
          constexpr int t2 = uv - 3;
          unsigned lo = pk2(acc[1][t2][jv][0], acc[1][t2][jv][1]), hi = pk2(acc[1][t2][jv][2], acc[1][t2][jv][3]);
          PIN(lo); PIN(hi);
          vx5[jv][2 * t2] = lo; vx5[jv][2 * t2 + 1] = hi;
        }
        if constexpr (uv == 6) *(LAS v4u*)(aW + (5 + jv) * 1024) = vx5[jv];
      }
      if constexpr (m == 391) { if (w == 3) { ST128(vx5[4], st_off + 9 * 1024); ST128(vx5[0], st_off + 5 * 1024); ST128(vx5[1], st_off + 6 * 1024); } }
      if constexpr (m == 406) { if (w == 3) { ST128(vx5[2], st_off + 7 * 1024); ST128(vx5[3], st_off + 8 * 1024); } }
      if constexpr (m >= 410 && m < 420) { constexpr int j = (m - 410) >> 1; Vb[tt][j].z = vx5[j][2 * tt]; Vb[tt][j].w = vx5[j][2 * tt + 1]; }
      auto halo_in = [&](auto R) {
        if (w == 0) {
          if constexpr (R == 1) wait_vm_p<9>(poll);
          settle_flags(p.ws + R * 131072, poll1_off, tag, poll);
          dma_m0<L_SCR + R * 5120>(0u); NOPS(0);
          sfor<0, 4>([&](auto J) { dma_sc1<J * 1024>(halo_src + R * 5120, lane16); });
          dma_m0<L_SCR + R * 5120 + 4096>(0u); NOPS(0); dma_sc1<0>(halo_src + R * 5120 + 4096, lane16);
        }
      };
      if constexpr (m == 346) flag(W_F1 + ((cg * 16 + rt) * 4 + w) * 128);
      if constexpr (m == 405) poll_async(poll, p.ws, poll1_off);
      if constexpr (m == 466) { halo_in(IC(0)); flag(W_F1 + 131072 + ((cg * 16 + rt) * 4 + w) * 128); }
      if constexpr (m == 494) poll_async(poll, p.ws + 131072, poll1_off);
      if constexpr (m == 506) wait_vm<12>();
      if constexpr (m >= 506 && m < 511) kpv[m - 506] = *(LAS v4u*)(aR + (m - 506) * 1024);
      if constexpr (m == 530) halo_in(IC(1));
      if constexpr (m == 566) wait_vm_p<9>(poll);
      if constexpr (m >= 566 && m < 576) {
        constexpr int j = ((m - 566) / 2 + 4) % 5;
        const v2u v = *(LAS v2u*)(aR + (5 + j) * 1024 + tt * 8);
        Vb[tt][j].x = v.x; Vb[tt][j].y = v.y;
      }
      if constexpr (m == 511 || m == 513) {
        constexpr int t2 = (m - 511) / 2;
        q4[t2] = v2u{pk2(acc[2][t2][4][0], acc[2][t2][4][1]), pk2(acc[2][t2][4][2], acc[2][t2][4][3])};
        PIN(q4[t2]);
      }
      if constexpr (m >= 516 && m < 528 && m % 2 == 0) att_pre(IC(0), IC((m - 516) / 2));
      if constexpr (m >= 528) {
        constexpr int h = tq - 1;
        constexpr int z0[25] = {0, 0, 0, 0, 2, 5, 7, 9, 9, 9, 9, 13, 17, 21, 22, 23, 24, 28, 32, 35, 38, 41, 46, 52, 52};
        sfor<z0[uq], z0[uq + 1]>([&](auto Z) { att_op(IC(h), IC(Z < 9 ? Z : Z + 8)); });
        if constexpr (uq >= 7 && uq < 10) { att_pre(IC(h + 1), IC(2 * (uq - 7))); att_pre(IC(h + 1), IC(2 * (uq - 7) + 1)); }
        if constexpr (h == 2 && uq < 18) pv_op(IC(0), IC(uq + 6));
      }
    }
    SB0();
    if constexpr (m == 199) { STUSE; STAMP(2); }
    if constexpr (m == 399) { STUSE; STAMP(3); }
  });
  STAMP(4);
  constexpr int WO_PRE = ISB ? 46 : 40;
  sfor<NWO1, WO_PRE>([&](auto Z) { ld_wo(Z); });
  NOPS(3);
  SB0();
  if constexpr (!ISB) {
    sfor<0, 36>([&](auto Z) {
      constexpr int z = Z, pr = z / 9, t2 = pr >> 1, h = pr & 1;
      unsigned o = 0;
      phi_op(IC(z % 9), acc[2][t2][3], IC(h), o);
      if constexpr (z % 9 == 8) Pq[t2][3][h] = o;
    });
    v4u Pf[2][4];
#pragma unroll
    for (int h = 0; h < 4; ++h)
#pragma unroll
      for (int tt = 0; tt < 2; ++tt) { Pf[tt][h] = v4u{Pq[tt][h].x, Pq[tt][h].y, Pq[tt][4].x, Pq[tt][4].y}; PIN(Pf[tt][h]); }
    SB0();
    STAMP(16);
    f32x4 d1, d2;
#pragma unroll
    for (int i = 0; i < 16; ++i) {
      if (i == 0) { mm_vv0(d1, A1, gl[0]); mm_vv0(d2, A2, gl[0]); }
      else { mm_vv(d1, A1, gl[i]); mm_vv(d2, A2, gl[i]); }
    }
    NOPS(7);
    SB0();
    STAMP(17);
    kvw[0] = v2u{pk2(d1[0], d1[1]), pk2(d1[2], d1[3])};
    kvw[64] = v2u{pk2(d2[0], d2[1]), pk2(d2[2], d2[3])};
    wait_lgkm();
    BAR();
    STAMP(5);
    v4u Nh[4], N4[4], Zh[4];
#pragma unroll
    for (int h = 0; h < 4; ++h) { const v2u lo = kvz[h], hi = *aZ[h]; Zh[h] = v4u{lo.x, lo.y, hi.x, hi.y}; SB0(); }
#pragma unroll
    for (int h = 0; h < 4; ++h) { const v2u lo = kvs[h * 64], hi = *aU[h]; Nh[h] = v4u{lo.x, lo.y, hi.x, hi.y}; SB0(); }
#pragma unroll
    for (int h = 0; h < 4; ++h) { const v2u lo = *aWt[h], hi = *aX[h]; N4[h] = v4u{lo.x, lo.y, hi.x, hi.y}; SB0(); }
    poll = ld_flag(p.ws, foB);
    SB0();
    f32x4 nm[2][4], dn[2][4];
#pragma unroll
    for (int h = 0; h < 4; ++h)
#pragma unroll
      for (int tt = 0; tt < 2; ++tt) mm_vvc(dn[tt][h], Zh[h], Pf[tt][h], eps4);
    SB0();
#pragma unroll
    for (int h = 0; h < 4; ++h)
#pragma unroll
      for (int tt = 0; tt < 2; ++tt) {
        mm_vv0(nm[tt][h], Nh[h], Pf[tt][h]);
        inv[tt][h] = __builtin_amdgcn_rcpf(dn[tt][h][0]);
        PIN(inv[tt][h]); PIN(dn[tt][h]);
        SB0();
      }
#pragma unroll
    for (int h = 0; h < 4; ++h)
#pragma unroll
      for (int tt = 0; tt < 2; ++tt) {
        if (h == 0) mm_vv0(num4[tt], N4[h], Pf[tt][h]); else mm_vv(num4[tt], N4[h], Pf[tt][h]);
        inv[tt][h] *= gate[tt];
        PIN(inv[tt][h]);
        SB0();
      }
#pragma unroll
    for (int hp = 0; hp < 2; ++hp)
#pragma unroll
      for (int tt = 0; tt < 2; ++tt) {
        const f32x4 oa = nm[tt][2 * hp] * inv[tt][2 * hp], ob = nm[tt][2 * hp + 1] * inv[tt][2 * hp + 1];
        F[tt][hp] = v4u{pk2(oa[0], oa[1]), pk2(oa[2], oa[3]), pk2(ob[0], ob[1]), pk2(ob[2], ob[3])};
        ST128(F[tt][hp], (tt ? o_st1 : o_st) + hp * 1024);
        SB0();
      }
  } else {
    sfor<0, 9>([&](auto Z) { att_op(IC(3), Z); });
    SB0();
    sfor<9, 17>([&](auto Z) { att_op(IC(1), Z); });
    PIN(biasC); PIN(biasP);
    sfor<60, 67>([&](auto Z) { att_op(IC(1), Z); });
    SB0();
    sfor<17, 32>([&](auto Z) { att_op(IC(3), Z); });
    sfor<0, 16>([&](auto Z) { fin_op(IC(1), Z); });
    ST128(F[0][0], o_st); ST128(F[1][0], o_st1);
    sfor<9, 17>([&](auto Z) { att_op(IC(2), Z); });
    sfor<60, 67>([&](auto Z) { att_op(IC(2), Z); });
    SB0();
    STAMP(5);
    sfor<32, 60>([&](auto Z) { att_op(IC(3), Z); });
    sfor<0, 16>([&](auto Z) { fin_op(IC(2), Z); });
    sfor<9, 17>([&](auto Z) { att_op(IC(3), Z); });
    sfor<60, 67>([&](auto Z) { att_op(IC(3), Z); });
    NOPS(3);
    SB0();
    sfor<0, 16>([&](auto Z) { fin_op(IC(3), Z); });
    ST128(F[0][1], o_st + 1024); ST128(F[1][1], o_st1 + 1024);
    SB0();
  }
  {
    v2u F2[2];
#pragma unroll
    for (int tt = 0; tt < 2; ++tt) {
      const f32x4 o4 = num4[tt] * sel4(g, inv[tt][0], inv[tt][1], inv[tt][2], inv[tt][3]);
      F2[tt] = v2u{pk2(o4[0], o4[1]), pk2(o4[2], o4[3])};
    }
    const auto sx = __builtin_amdgcn_permlane16_swap(F2[0].x, F2[1].x, false, false);
    const auto sy = __builtin_amdgcn_permlane16_swap(F2[0].y, F2[1].y, false, false);
    ST128((v4u{sx[0], sy[0], sx[1], sy[1]}), o_st2);
  }
  STAMP(6);
  SB0();
  auto load_o0 = [&](auto Z0, auto Z1) { sfor<Z0, Z1>([&](auto Z) { of0[Z / 5][Z % 5] = LD128S(lane16, o_off(jt2, w, rj, 0, Z / 5, Z % 5)); SB0(); }); };
  if constexpr (ISB) {
    sfor<WO_PRE, 50>([&](auto Z) { ld_wo(Z); });
    SB0();
    wait_vm<50 - WO_PRE>();
    flag(W_FO + ((rt * 16 + cg) * 4 + w) * 128);
    STAMP(7);
    __builtin_amdgcn_s_sleep(BDLY);
    settle_flags(p.ws, foB, tag, ld_flag(p.ws, foB));
    load_o0(IC(0), IC(OPRE));
  } else {
    if (__all(poll == tag)) {
      __builtin_amdgcn_fence(__ATOMIC_ACQUIRE, "wavefront");
      load_o0(IC(0), IC(OPRE));
      wait_vm<OPRE>();
      flag(W_FO + ((rt * 16 + cg) * 4 + w) * 128);
      STAMP(7);
    } else {
      poll = ld_flag(p.ws, foB);
      SB0();
      wait_vm<1>();
      flag(W_FO + ((rt * 16 + cg) * 4 + w) * 128);
      STAMP(7);
      settle_flags(p.ws, foB, tag, poll);
      load_o0(IC(0), IC(OPRE));
    }
  }
  load_o0(IC(OPRE), IC(10));
  asm volatile("" ::"v"(tag4));
  STAMP(8);
}

template <int CHK, bool ISB>
__device__ __forceinline__ void phase2(const Params& p, LAS char* L, __amdgpu_buffer_rsrc_t rs, int b, int w, int lane, int jt, int c, int rj, u64 tag,
                                       v4u (&of0)[4][5], v4u (&wo)[50] STARG) {
  const int r = lane & 15, g = lane >> 4;
  v4u of1[4][5];
  f32x4 acc[4][5];
  unsigned lane16 = lane * 16;
  PIN(lane16);
  const unsigned foA = fo_of(jt, w, lane, 0);
  u64 pollA = 0;
  unsigned ra = (unsigned)(uintptr_t)L + lane16;
  unsigned rw[3];
#pragma unroll
  for (int a = 0; a < 3; ++a) { rw[a] = ra + ((((a + w + 1) & 3) * 3 + a) * 5) * 1024; PIN(rw[a]); }
  sfor2<0, 200>([&](auto M) {
    constexpr int m = M, hb = m / 100, a = (m % 100) / 25, i = (m % 25) / 5, n = m % 5;
    if constexpr (m == CHK) { settle_flags(p.ws, foA, tag, pollA); STUSE; STAMP(9); }
    if constexpr (hb == 0 && i == 0) mm_av0(acc[a][n], wo[n], of0[a][0]);
    else mm_av(acc[a][n], wo[hb * 25 + i * 5 + n], hb ? of1[a][i] : of0[a][i]);
    SB0();
    if constexpr (m < 20 && m % 2 == 0) of0[2 + m / 10][m / 2 % 5] = LD128S(lane16, o_off(jt, w, rj, 0, 2 + m / 10, m / 2 % 5));
    if constexpr (!ISB && m >= 26 && m < 56 && (m - 26) % 3 == 0) {
      constexpr int z = 40 + (m - 26) / 3;
      wo[wo_idx(z)] = LD128S(lane16 + ((z % 5) & 3) * 1024, W_WOP + (c * 40 + 5 * w) * 5120 + rot5(rj, wo_i(z)) * 5120 + ((z % 5) >> 2) * 4096);
    }
    if constexpr (m == CHK - 35) pollA = ld_flag(p.ws, foA);
    if constexpr (m >= CHK && m < CHK + 40 && (m - CHK) % 2 == 0) of1[(m - CHK) / 10][(m - CHK) / 2 % 5] = LD128S(lane16, o_off(jt, w, rj, 1, (m - CHK) / 10, (m - CHK) / 2 % 5));
    if constexpr (m == 120) BAR();
    if constexpr (m >= 130 && (m - 130) % 25 < 20 && (m - 130) % 25 % 4 == 0) {
      constexpr int a2 = (m - 130) / 25, n2 = (m - 130) % 25 / 4;
      *(LAS f32x4*)(uintptr_t)(rw[a2] + n2 * 1024) = acc[a2][n2];
    }
    SB0();
  });
  STAMP(10);
  wait_lgkm();
  BAR();
  LAS f32x4* red = (LAS f32x4*)(uintptr_t)(ra + w * 15 * 1024);
  unsigned o[10];
#pragma unroll
  for (int n = 0; n < 5; ++n) {
    const f32x4 s = (red[n * 64] + red[(5 + n) * 64]) + (red[(10 + n) * 64] + acc[3][n]);
    o[2 * n] = pk2(s[0], s[1]);
    o[2 * n + 1] = pk2(s[2], s[3]);
  }
  const int tok = 64 * jt + 32 * (w >> 1) + 2 * r + (w & 1);
  GAS char* op = (GAS char*)p.out + (size_t)tok * 1280 + c * 160 + g * 40;
  typedef unsigned v4u8 __attribute__((ext_vector_type(4), aligned(8)));
  *(GAS v4u8*)op = v4u8{o[0], o[1], o[2], o[3]};
  *(GAS v4u8*)(op + 16) = v4u8{o[4], o[5], o[6], o[7]};
  *(GAS v2u*)(op + 32) = v2u{o[8], o[9]};
  STAMP(11);
  if (b == 0) {
    const unsigned fa = W_FO + (threadIdx.x * 4) * 128;
    settle_flags(p.ws, fa, tag, ld_flag(p.ws, fa));
    BAR();
    if (w == 0) ST128((v4u{(unsigned)tag, (unsigned)(tag >> 32), (unsigned)tag, (unsigned)(tag >> 32)}), lane < 8 ? W_EPOCH + lane * 16 : OOB);
  }
}

__global__ __launch_bounds__(256, 1) void fused_kernel(Params p) {
  extern __shared__ __attribute__((aligned(16))) char smem[];
  LAS char* L = (LAS char*)smem;
  const int b = blockIdx.x, lane = threadIdx.x & 63;
  const int w = __builtin_amdgcn_readfirstlane(threadIdx.x >> 6);
  STAMP(0);
  const int xcd = b & 7, idx = b >> 3;
  const int rt = (xcd & 1) * 8 + (idx & 7), cg = (xcd >> 1) * 4 + (idx >> 3);
  const int jt = xcd * 4 + (idx & 3), c2 = idx >> 2;
  const int r = lane & 15, g = lane >> 4;
  {
    const char* pc; unsigned len;
    asm volatile("s_getpc_b64 %0\nfk_here:\n\ts_mov_b32 %1, fk_end-fk_here" : "=s"(pc), "=s"(len));
    pc = (const char*)((uintptr_t)pc & ~(uintptr_t)63);
    unsigned co = (unsigned)((idx * 4 + w) * 1024 + lane * 16);
    co = co + 16 <= len ? co : 0u;
    asm volatile("s_mov_b32 m0, %2\n\ts_nop 0\n\tglobal_load_lds_dwordx4 %0, %1" ::"v"(co), "s"(pc), "s"((unsigned)(L_AUX + 2048)) : "memory");
  }
  const __amdgpu_buffer_rsrc_t rs = __builtin_amdgcn_make_buffer_rsrc((void*)p.ws, 0, W_BYTES, 0x00020000);
  Lane q;
  q.rot = idx % 5;
  q.lw = (unsigned)(w * UNIT);
  q.lx = (unsigned)(w * 16384);
#pragma unroll
  for (int i = 0; i < 8; ++i) {
    const int e = i & 1, tt = (i >> 1) & 1, hh = i >> 2;
    q.xo[i] = (unsigned)((16 * e + 2 * (lane >> 3) + tt) * 1280 + hh * 128 + (((lane & 7) ^ (4 * e + g)) * 16));
  }
  q.Wb = (cg < 8 ? p.wA : p.wB) + (cg & 7) * 160 + (size_t)w * 32 * 3840;
  q.xb = p.x + (size_t)(rt * 128 + w * 32) * 1280;
  sfor<0, 8>([&](auto I) { load_x<0, I>(q); });
  STAMP(12);
#pragma unroll
  for (int i = 0; i < 5; ++i) {
    const int c = i * 64 + lane, rho = c / 10, cc = c % 10;
    q.woff[i] = (unsigned)((8 * ((rho >> 2) & 3) + 4 * (rho >> 4) + (rho & 3)) * 3840 + cc * 16);
  }
  sfor<0, 5>([&](auto I) { load_w<0, I>(q); });
  {
    const char* ab = sel4(w, p.gw, p.gw + 1024, (const char*)p.out, (const char*)p.ws);
    const unsigned ao = (unsigned)((w == 1 ? r : lane) * 16);
    asm volatile("s_mov_b32 m0, %2\n\ts_nop 0\n\tglobal_load_lds_dwordx4 %0, %1 sc1" ::"v"(ao), "s"(ab), "s"((unsigned)(L_AUX + w * 1024)) : "memory");
  }
  {
    const int ln = 25 * (idx * 4 + w) + (lane < 25 ? lane : 0);
    q.Wg = (cg < 8 ? p.wA : p.wB) + ((xcd >> 1) & 1) * 640;
    q.tw = (unsigned)((ln / 5) * 3840 + (ln % 5) * 128);
  }
  unsigned pk_off[5];
  const char* pk_src;
  {
    const int unit = b + 256 * w, pf = unit % 40, pc = (unit / 40) & 7, pP = (pf % 20) / 5, pi = pf % 5;
    const int orow = 160 * pP + (pi == 2 ? 16 : (pi >= 3 ? 40 * pi - 40 : 40 * pi)), cgs = pi == 2 ? 40 : 4;
    pk_src = (pf >= 20 ? p.woB : p.woA) + pc * 160 + orow * 1280;
#pragma unroll
    for (int i = 0; i < 5; ++i) {
      const int c = i * 64 + lane, rho = c / 10, cc = c % 10;
      pk_off[i] = (unsigned)((cgs * ((rho >> 2) & 3) + 20 * (rho >> 4) + (rho & 3)) * 1280 + cc * 16);
      PIN(pk_off[i]);
    }
  }
  STAMP(14);
  STAMP(15);
  sfor<0, 5>([&](auto I) { load_w<1, I>(q); });
  if (b + 256 * w < 320) {
    sfor<0, 5>([&](auto I) { dma16<L_PACK + I * 1024>(pk_src, pk_off[I], q.lw); });
  }
  STAMP(13);
  u64 tag = 0;
  v4u wo[50], of0[4][5];
  const int rj = (idx & 1) * 2;
  if (cg < 8) {
    phase1<false>(p, q, L, rs, b, rt, cg, w, lane, c2, jt, rj, tag, of0, wo STPASS);
    phase2<P2CHK, false>(p, L, rs, b, w, lane, jt, c2, rj, tag, of0, wo STPASS);
  } else {
    phase1<true>(p, q, L, rs, b, rt, cg - 8, w, lane, c2, jt, rj, tag, of0, wo STPASS);
    phase2<P2CHKB, true>(p, L, rs, b, w, lane, jt, c2, rj, tag, of0, wo STPASS);
  }
  asm volatile("fk_end:");
}

extern "C" void kernel_launch(void* const* d_in, const int* in_sizes, int n_in, void* d_out, int out_size, void* d_ws, size_t ws_size,
                              hipStream_t stream) {
  Params p;
  p.x = (const char*)d_in[0];
  p.wA = (const char*)d_in[1];
  p.woA = (const char*)d_in[2];
  p.wB = (const char*)d_in[4];
  p.woB = (const char*)d_in[5];
  p.gw = (const char*)d_in[6];
  p.out = (char*)d_out;
  p.ws = (char*)d_ws;
  (void)hipFuncSetAttribute((const void*)fused_kernel, hipFuncAttributeMaxDynamicSharedMemorySize, LDS_BYTES);
  fused_kernel<<<256, 256, LDS_BYTES, stream>>>(p);
}
```

```cpp
#include <hip/hip_runtime.h>
#include <stdint.h>
#include <type_traits>

#define LAS __attribute__((address_space(3)))
#define GAS __attribute__((address_space(1)))
typedef short s16x4 __attribute__((ext_vector_type(4)));
typedef unsigned v4u __attribute__((ext_vector_type(4)));
typedef unsigned v2u __attribute__((ext_vector_type(2)));
typedef float f32x4 __attribute__((ext_vector_type(4)));
typedef float f32x2 __attribute__((ext_vector_type(2)));
typedef __bf16 bf2 __attribute__((ext_vector_type(2)));
typedef unsigned long long u64;

#define SLP 2
#define BDLY 4
#define GPOLL 546
#define P2CHKB 75
#define OPRE 5
#define X1M 11
#define S1PRE 5
#define TQM 184
#define PACKM 105
#define P2CHK 60

constexpr int UNIT = 5120, SLOT = 4 * UNIT;
constexpr int L_XST = 4 * SLOT;
constexpr int L_SCR = L_XST;
constexpr int L_SLOT4 = L_SCR + 40960;
constexpr int L_PACK = L_XST + 65536;
constexpr int L_AUX = L_PACK + 2 * UNIT;
constexpr int LDS_BYTES = 163840;
static_assert(L_SLOT4 + SLOT <= L_PACK && L_AUX + 4096 <= LDS_BYTES, "lds map");
constexpr int slot_of(int s) { return s <= 10 ? (s & 3) : s == 11 ? 4 : (s - 9) & 3; }
constexpr int slot_base(int s) { return slot_of(s) == 4 ? L_SLOT4 : slot_of(s) * SLOT; }
constexpr int W_EPOCH = 0;
constexpr int W_FE = 131072, W_FO = 2 * 131072, W_F1 = 3 * 131072;
constexpr int W_PART = 1 << 20, PART_STR = 4096;
constexpr int W_HALO = 2 << 20, HALO_STR = 10240;
constexpr int W_WOP = 4 << 20;
constexpr int W_OB = 6 << 20;
constexpr int W_BYTES = W_OB + 128 * 40 * 1024;
constexpr int SC1 = 16;
constexpr unsigned OOB = 0x40000000u;
constexpr unsigned SPIN_CAP = 1u << 16;

struct Params {
  const char *x, *wA, *woA, *wB, *woB, *gw;
  char *out, *ws;
};

template <int I, int N, class F> __device__ __forceinline__ void sfor(F&& f) {
  if constexpr (I < N) { f(std::integral_constant<int, I>{}); sfor<I + 1, N>(f); }
}
template <int I, int N, class F> __device__ __forceinline__ void sfor2(F&& f) {
  if constexpr (N - I <= 20) sfor<I, N>(f);
  else { sfor<I, I + 20>(f); sfor2<I + 20, N>(f); }
}
#define IC(n) std::integral_constant<int, (n)>{}
__device__ __forceinline__ unsigned pk2(float a, float b) {
  f32x2 t = {a, b};
  return __builtin_bit_cast(unsigned, __builtin_convertvector(t, bf2));
}
#define MF "v_mfma_f32_16x16x32_bf16 "
__device__ __forceinline__ void mm_va(f32x4& d, v4u v, v4u a) { asm volatile(MF "%0, %1, %2, %0" : "+v"(d) : "v"(v), "a"(a)); }
__device__ __forceinline__ void mm_av(f32x4& d, v4u a, v4u v) { asm volatile(MF "%0, %1, %2, %0" : "+v"(d) : "a"(a), "v"(v)); }
__device__ __forceinline__ void mm_vv(f32x4& d, v4u a, v4u b) { asm volatile(MF "%0, %1, %2, %0" : "+v"(d) : "v"(a), "v"(b)); }
__device__ __forceinline__ void mm_va0(f32x4& d, v4u v, v4u a) { asm volatile(MF "%0, %1, %2, 0" : "=&v"(d) : "v"(v), "a"(a)); }
__device__ __forceinline__ void mm_av0(f32x4& d, v4u a, v4u v) { asm volatile(MF "%0, %1, %2, 0" : "=&v"(d) : "a"(a), "v"(v)); }
__device__ __forceinline__ void mm_vv0(f32x4& d, v4u a, v4u b) { asm volatile(MF "%0, %1, %2, 0" : "=&v"(d) : "v"(a), "v"(b)); }
__device__ __forceinline__ void mm_vac(f32x4& d, v4u v, v4u a, f32x4 c) { asm volatile(MF "%0, %1, %2, %3" : "=&v"(d) : "v"(v), "a"(a), "v"(c)); }
__device__ __forceinline__ void mm_avc(f32x4& d, v4u a, v4u v, f32x4 c) { asm volatile(MF "%0, %1, %2, %3" : "=&v"(d) : "a"(a), "v"(v), "v"(c)); }
__device__ __forceinline__ void mm_vvc(f32x4& d, v4u a, v4u b, f32x4 c) { asm volatile(MF "%0, %1, %2, %3" : "=&v"(d) : "v"(a), "v"(b), "v"(c)); }
#define NOPS(n) asm volatile("s_nop " #n)
#define PIN(x) asm volatile("" : "+v"(x))

template <int IMM> __device__ __forceinline__ void dma16(const char* sbase, unsigned voff, unsigned ldsbase) {
  asm volatile("s_add_u32 m0, %2, %3\n\ts_nop 0\n\tglobal_load_lds_dwordx4 %0, %1" ::"v"(voff), "s"(sbase), "s"(ldsbase), "i"(IMM) : "memory", "scc");
}
template <int IMM> __device__ __forceinline__ void dma_m0(unsigned ldsbase) { asm volatile("s_add_u32 m0, %0, %1" ::"s"(ldsbase), "i"(IMM) : "memory", "scc"); }
template <int OFF> __device__ __forceinline__ void dma_go(const char* sbase, unsigned voff) {
  asm volatile("global_load_lds_dwordx4 %0, %1 offset:%2" ::"v"(voff), "s"(sbase), "i"(OFF) : "memory");
}
template <int OFF> __device__ __forceinline__ void dma_sc1(const char* sbase, unsigned voff) {
  asm volatile("global_load_lds_dwordx4 %0, %1 offset:%2 sc1" ::"v"(voff), "s"(sbase), "i"(OFF) : "memory");
}
__device__ __forceinline__ void touch(const char* sbase, unsigned voff) {
  asm volatile("s_mov_b32 m0, %2\n\ts_nop 0\n\tglobal_load_lds_dword %0, %1" ::"v"(voff), "s"(sbase), "i"(L_AUX + 2048) : "memory");
}
template <int N> __device__ __forceinline__ void wait_vm() { asm volatile("s_waitcnt vmcnt(%0)" ::"i"(N) : "memory"); }
template <int N> __device__ __forceinline__ void wait_vm_p(u64& v) { asm volatile("s_waitcnt vmcnt(%1)" : "+v"(v) : "i"(N) : "memory"); }
__device__ __forceinline__ void wait_lgkm() { asm volatile("s_waitcnt lgkmcnt(0)" ::: "memory"); }
__device__ __forceinline__ v2u trread(LAS char* p) {
  return __builtin_bit_cast(v2u, __builtin_amdgcn_ds_read_tr16_b64_v4i16((LAS s16x4*)p));
}
#define SB0() __builtin_amdgcn_sched_barrier(0)
#define BAR() __builtin_amdgcn_s_barrier()
#define LD128S(voff, soff) __builtin_amdgcn_raw_buffer_load_b128(rs, (voff), (soff), SC1)
#define ST128(v, off) __builtin_amdgcn_raw_buffer_store_b128((v), rs, (off), 0, SC1)
#define STAMP(i) do {} while (0)
#define STUSE
#define STARG
#define STPASS

__device__ __forceinline__ u64 ld_flag(char* ws, unsigned off) {
  return __hip_atomic_load((GAS u64*)(ws + off), __ATOMIC_RELAXED, __HIP_MEMORY_SCOPE_AGENT);
}
__device__ __forceinline__ void poll_async(u64& v, char* ws, unsigned off) {
  asm volatile("global_load_dwordx2 %0, %1, %2 sc1" : "+v"(v) : "v"(off), "s"(ws) : "memory");
}
__device__ __forceinline__ void settle_flags(char* ws, unsigned off, u64 tag, u64 v) {
  if (!__all(v == tag)) {
    unsigned spins = 0;
    bool ok;
#pragma unroll 1
    do { __builtin_amdgcn_s_sleep(SLP); ok = __all(ld_flag(ws, off) == tag); } while (!ok && ++spins < SPIN_CAP);
  }
  __builtin_amdgcn_fence(__ATOMIC_ACQUIRE, "wavefront");
}
template <class T> __device__ __forceinline__ T sel4(int i, T a, T b, T c, T d) {
  const bool lo = (i & 1) != 0, hi = (i & 2) != 0;
  const T x = lo ? b : a, y = lo ? d : c;
  return hi ? y : x;
}
__device__ __forceinline__ float asf(unsigned u) { return __builtin_bit_cast(float, u); }
__device__ __forceinline__ unsigned asu(float f) { return __builtin_bit_cast(unsigned, f); }
#define vmax(a, b) __builtin_amdgcn_fmed3f((a), (b), vinf)
__device__ __forceinline__ float vmax3(float a, float b, float c) { float r; asm("v_max3_f32 %0, %1, %2, %3" : "=v"(r) : "v"(a), "v"(b), "v"(c)); return r; }
constexpr float EPS = 1e-6f, LOG2E = 1.4426950408889634f;
__device__ __forceinline__ float phi_a(float t) { return __builtin_fmaf(t, LOG2E, -EPS * LOG2E); }
__device__ __forceinline__ float phi_b(float u) { return __builtin_amdgcn_fmed3f(__builtin_amdgcn_exp2f(u), 0.f, 1.f); }
#define phi_c(t) vmax((t), EPS)

constexpr int st_ch(int s) { return s >= 10 ? 2 : (s == 1 || s == 5 || s >= 7) ? 1 : 0; }
constexpr int st_kb(int s) { return s >= 10 ? s - 10 : s >= 7 ? s - 5 : s == 6 ? 4 : s == 5 ? 1 : s >= 2 ? s - 1 : 0; }
constexpr bool fr_tm(int f) { return f / 20 == 6 || f / 20 == 9; }
constexpr int fr_k(int f) { return f >= 240 ? 8 + (f - 240) % 12 : fr_tm(f) ? 16 + f % 4 : 4 * st_kb(f / 20) + (f % 20) / 5; }
constexpr int fr_j(int f) { return f >= 240 ? ((f - 240) / 12 + 4) % 5 : fr_tm(f) ? ((f % 20) / 4 + 4) % 5 : f % 5; }
constexpr int fr_s(int f) { return f >= 240 ? 10 + fr_k(f) / 4 : f / 20; }
constexpr int AHEAD = 8, RING = 12;

struct Lane {
  unsigned woff[5], xo[8];
  const char *Wb, *xb;
  unsigned lw, lx;
  int rot;
  int idx, xcd;
};
__device__ __forceinline__ int rot5(int rot, int pos) { const int k = pos + rot; return k >= 5 ? k - 5 : k; }
template <int S> __device__ __forceinline__ const char* w_src(const Lane& q) {
  return q.Wb + (st_ch(S) == 0 ? 1280 : st_ch(S) == 1 ? 2560 : 0) + (size_t)rot5(q.rot, st_kb(S)) * (128 * 3840);
}
template <int S, int I> __device__ __forceinline__ void w_m0(const Lane& q) { if constexpr ((I & 3) == 0) dma_m0<slot_base(S) + I * 1024>(q.lw); }
template <int S, int I> __device__ __forceinline__ void w_go(const Lane& q) { dma_go<(I & 3) * 1024>(w_src<S>(q), q.woff[I]); }
template <int S, int I> __device__ __forceinline__ void x_m0(const Lane& q) { if constexpr ((I & 3) == 0) dma_m0<L_XST + (S & 1) * 8192 + I * 1024>(q.lx); }
template <int S, int I> __device__ __forceinline__ void x_go(const Lane& q) { dma_go<(I & 3) * 1024>(q.xb + rot5(q.rot, S) * 256, q.xo[I]); }
template <int S, int I> __device__ __forceinline__ void load_w(const Lane& q) { w_m0<S, I>(q); if constexpr ((I & 3) == 0) NOPS(0); w_go<S, I>(q); }
template <int S, int I> __device__ __forceinline__ void load_x(const Lane& q) { x_m0<S, I>(q); if constexpr ((I & 3) == 0) NOPS(0); x_go<S, I>(q); }
__device__ __forceinline__ unsigned fo_of(int jt, int w, int lane, int br) {
  return W_FO + (((jt >> 1) * 16 + br * 8 + 2 * w + (lane & 1)) * 4 + 2 * (jt & 1) + ((lane >> 1) & 1)) * 128;
}
struct OAddr { unsigned vo[5]; int so[4]; };
__device__ __forceinline__ void o_addr(OAddr& o, int jt, int w, int rj, int lane) {
#pragma unroll
  for (int i = 0; i < 5; ++i) { o.vo[i] = lane * 16 + rot5(rj, i) * 1024; PIN(o.vo[i]); }
#pragma unroll
  for (int a = 0; a < 4; ++a) { o.so[a] = W_OB + ((4 * jt + ((a + w + 1) & 3)) * 40 + 20 + 5 * w) * 1024; asm volatile("" : "+s"(o.so[a])); }
}
constexpr int wo_hb(int z) { return z / 5 < 6 ? ((z / 5) & 1) : (z / 5 < 8 ? 0 : 1); }
constexpr int wo_i(int z) { return z / 5 < 6 ? (z / 10) : (z / 5 < 8 ? z / 5 - 3 : z / 5 - 5); }
constexpr int wo_idx(int z) { return wo_hb(z) * 25 + wo_i(z) * 5 + z % 5; }

template <bool ISB>
__device__ __forceinline__ void phase1(const Params& p, const Lane& q, LAS char* L, __amdgpu_buffer_rsrc_t rs, int b, int rt, int hg, int w, int lane, int c2,
                                       int jt2, int rj, u64& tag, OAddr& oa, v4u (&of0)[4][5], v4u (&wo)[50] STARG) {
  const int r = lane & 15, g = lane >> 4;
  const int cg = (ISB ? 8 : 0) + hg;
  const unsigned lw = q.lw;
  auto lbase = [&](int off) { unsigned a = (unsigned)(uintptr_t)L + (unsigned)off; PIN(a); return (LAS char*)(uintptr_t)a; };
  const int o1 = (lane >> 2) * 160 + (lane & 3) * 40, o2 = (lane >> 2) * 160 + (lane & 3) * 8;
  LAS char* a1 = lbase(o1);
  LAS char* a2 = lbase(o2);
  LAS char* a1h = lbase(o1 + 3 * SLOT);
  LAS char* a2h = lbase(o2 + 3 * SLOT);
  LAS char* ax0 = lbase(L_XST + w * 16384 + r * 128 + ((g ^ (r >> 1)) * 16));
  LAS char* ax1 = lbase(L_XST + w * 16384 + r * 128 + (((4 + g) ^ (r >> 1)) * 16));
  LAS char* ag = L + L_AUX + g * 16;
  LAS char *a1s, *a2s;
  const int unit = b + 256 * w;
  unsigned lane16 = lane * 16, fl0 = lane < 8 ? lane * 16u : OOB;
  PIN(lane16); PIN(fl0);

  v4u xf[20][2], wf[RING], gf2[2][4];
  f32x4 acc[3][2][5], gacc[2];
  f32x4 eps4 = {EPS, EPS, EPS, EPS};
  PIN(eps4);
  float vinf = __builtin_inff();
  PIN(vinf);

  auto readW = [&](auto F, auto T) {
    constexpr int f = F, t = T, ks = fr_k(f), jt = fr_j(f), sl = slot_of(fr_s(f));
    constexpr int off = (ks % 4) * UNIT + t * 2560 + (jt == 4 ? 32 : 40 * jt);
    v2u v;
    if constexpr (sl == 4) v = trread((jt == 4 ? a1s : a2s) + off);
    else if constexpr (sl == 3) v = trread((jt == 4 ? a1h : a2h) + off);
    else v = trread((jt == 4 ? a1 : a2) + sl * SLOT + off);
    if constexpr (t == 0) { wf[f % RING].x = v.x; wf[f % RING].y = v.y; }
    else { wf[f % RING].z = v.x; wf[f % RING].w = v.y; }
  };
  auto readX = [&](auto S, auto U) {
    constexpr int s = S, u = U, j = u / 3, k = u % 3;
    if constexpr (k < 2) xf[4 * s + j][k] = *(LAS v4u*)(((j & 1) ? ax1 : ax0) + (s & 1) * 8192 + (j >> 1) * 4096 + k * 2048);
    else gf2[s & 1][j] = *(LAS v4u*)(ag + rot5(q.rot, s) * 256 + j * 64);
  };

  v2u ept;
  v4u tag4;
  u64 poll = 0;
  unsigned poll0_off, poll1_off, st_off, o_st, o_st1, o_st2, foB;
  LAS char *aW, *aR;
  const int rp = rt > 0 ? rt - 1 : 0;
  {
    const int li = lane < 10 ? lane : 0;
    const int pu = c2 * 40 + (li / 5) * 20 + 5 * w + li % 5;
    poll0_off = W_FE + ((pu & 255) * 4 + (pu >> 8)) * 128;
    poll1_off = ISB ? W_F1 + ((cg * 16 + rp) * 4 + 3) * 128 : W_F1 + ((cg * 16 + r) * 4 + w) * 128;
    PIN(poll0_off); PIN(poll1_off);
  }
  const char* halo_src = p.ws + W_HALO + (hg * 16 + rp) * HALO_STR;
  const int wo_b = W_WOP + (c2 * 40 + 5 * w) * 5 * 1024;
  auto wo_soff = [&](int z) { return wo_b + (wo_hb(z) ? 0 : 102400) + rot5(rj, wo_i(z)) * 5120 + ((z % 5) >> 2) * 4096; };
  auto ld_wo = [&](auto Z) { wo[wo_idx(Z)] = LD128S(lane16 + ((Z % 5) & 3) * 1024, wo_soff(Z)); };
  auto flag = [&](int off) { __builtin_amdgcn_raw_buffer_store_b128(tag4, rs, fl0, off, SC1); };
  constexpr int NWO1 = 40;
  float gate[2];
  v4u Kf[5], Vf[5], Ej[5], K4m[4], V4m, pst, gl[16];
  v2u Pq[2][5];
  f32x4 kvT, kvU, kvW, kvX, kvZ, rd[8];
  float ta[2], tb[2], tc[2];
  constexpr int KVS = L_SCR + 32768, KZERO = KVS + 4096;
  LAS v2u *kvs, *kvw, *kvz, *aU[4], *aWt[4], *aX[4], *aZ[4];
  unsigned sel0, sel1;
  v4u kx[5], kpv[5], vx5[5], Vb[2][5], Kc[2], Kp[2], Qf[2], Pb[2][2], V4b[2], ones4;
  v2u q4[2];
  f32x4 biasC, biasP, Sc[2], Sp[2];
  float sm[2][4], mx[2], mt;
  unsigned mC[2], mP[2], pp[2][2];
  f32x4 num[2], den[2], num4[2];
  float inv[2][4];
  v4u F[2][2];

  auto fin_op = [&](auto H, auto Z) {
    constexpr int h = H, z = Z;
    if constexpr (z < 2) { inv[z][h] = __builtin_amdgcn_rcpf(den[z][0]); PIN(inv[z][h]); PIN(den[z]); }
    else if constexpr (z < 4) { inv[z - 2][h] *= gate[z - 2]; PIN(inv[z - 2][h]); }
    else if constexpr (z < 12) { constexpr int t2 = (z - 4) >> 2, e = (z - 4) & 3; float u = num[t2][e] * inv[t2][h]; PIN(u); num[t2][e] = u; }
    else { constexpr int t2 = (z - 12) >> 1, e = (z - 12) & 1; unsigned o = pk2(num[t2][2 * e], num[t2][2 * e + 1]); PIN(o); F[t2][h >> 1][2 * (h & 1) + e] = o; }
  };
  constexpr float CS = 0.22360679774997896f * LOG2E;
  auto att_op = [&](auto H, auto Z) {
    constexpr int h = H, z = Z;
    if constexpr (z < 4) { constexpr int t2 = z >> 1, e = z & 1; unsigned o = pk2(acc[2][t2][h][2 * e], acc[2][t2][h][2 * e + 1]); PIN(o); Qf[t2][e] = o; }
    else if constexpr (z == 4) { PIN(Qf[0]); PIN(Qf[1]); NOPS(1); }
    else if constexpr (z < 9) { constexpr int t2 = (z - 5) >> 1; if constexpr ((z - 5) & 1) mm_vvc(Sp[t2], Kp[t2], Qf[t2], biasP); else mm_vvc(Sc[t2], Kc[t2], Qf[t2], biasC); }
    else if constexpr (z < 17) { constexpr int t2 = (z - 9) >> 2, e = (z - 9) & 3; unsigned o = (r >> 2) == h ? Vb[t2][4][e] : 0u; PIN(o); V4b[t2][e] = o; }
    else if constexpr (z < 25) { constexpr int t2 = (z - 17) >> 2, e = (z - 17) & 3; sm[t2][e] = vmax(Sc[t2][e], Sp[t2][e]); PIN(sm[t2][e]); }
    else if constexpr (z < 27) { mx[z - 25] = vmax3(sm[z - 25][0], sm[z - 25][1], sm[z - 25][2]); PIN(mx[z - 25]); }
    else if constexpr (z < 29) { mx[z - 27] = vmax(mx[z - 27], sm[z - 27][3]); PIN(mx[z - 27]); }
    else if constexpr (z == 29) {
      const auto s = __builtin_amdgcn_permlane16_swap(asu(mx[0]), asu(mx[1]), false, false);
      mt = vmax(asf(s[0]), asf(s[1])); PIN(mt);
    } else if constexpr (z == 30) {
      const auto s = __builtin_amdgcn_permlane32_swap(asu(mt), asu(mt), false, false);
      mt = vmax(asf(s[0]), asf(s[1])) * -CS; PIN(mt);
    } else if constexpr (z == 31) {
      const auto s = __builtin_amdgcn_permlane16_swap(asu(mt), asu(mt), false, false);
      mx[0] = asf(s[0]); mx[1] = asf(s[1]); PIN(mx[0]); PIN(mx[1]);
    }
    else if constexpr (z < 40) { constexpr int t2 = (z - 32) >> 2, e = (z - 32) & 3; sm[t2][e] = __builtin_fmaf(sm[t2][e], CS, mx[t2]); PIN(sm[t2][e]); }
    else if constexpr (z < 48) { constexpr int t2 = (z - 40) >> 2, e = (z - 40) & 3; sm[t2][e] = __builtin_amdgcn_exp2f(sm[t2][e]); PIN(sm[t2][e]); }
    else if constexpr (z < 52) { constexpr int t2 = (z - 48) >> 1, e = (z - 48) & 1; pp[t2][e] = pk2(sm[t2][2 * e], sm[t2][2 * e + 1]); PIN(pp[t2][e]); }
    else if constexpr (z < 60) { constexpr int t2 = (z - 52) >> 2, e = (z - 52) & 3; unsigned o = pp[t2][e & 1] & (e < 2 ? mP[e & 1] : mC[e & 1]); PIN(o); Pb[h & 1][t2][e] = o; }
    else if constexpr (z == 60) { PIN(Pb[h & 1][0]); PIN(Pb[h & 1][1]); PIN(V4b[0]); PIN(V4b[1]); PIN(Vb[0][h]); PIN(Vb[1][h]); NOPS(1); }
    else if constexpr (z < 63) mm_vv0(den[z - 61], ones4, Pb[h & 1][z - 61]);
    else if constexpr (z < 65) mm_vv0(num[z - 63], Vb[z - 63][h], Pb[h & 1][z - 63]);
    else if constexpr (z < 67) { if constexpr (h == 0) mm_vv0(num4[z - 65], V4b[z - 65], Pb[h & 1][z - 65]); else mm_vv(num4[z - 65], V4b[z - 65], Pb[h & 1][z - 65]); }
  };
  auto pv_op = [&](auto H, auto U) {
    constexpr int u = U;
    if constexpr (u >= 6 && u < 10) { att_op(H, IC(9 + 2 * (u - 6))); att_op(H, IC(10 + 2 * (u - 6))); }
    if constexpr (u == 10) { att_op(H, IC(60)); att_op(H, IC(61)); }
    if constexpr (u >= 11 && u < 16) att_op(H, IC(51 + u));
    if constexpr (u >= 16) { fin_op(H, IC(2 * (u - 16))); fin_op(H, IC(2 * (u - 16) + 1)); }
  };
  auto att_pre = [&](auto H, auto Z) {
    constexpr int h = H, z = Z;
    if constexpr (z < 2) { Kc[z] = v4u{kx[h][2 * z], kx[h][2 * z + 1], kx[4][2 * z], kx[4][2 * z + 1]}; PIN(Kc[z]); }
    else if constexpr (z < 4) { constexpr int t2 = z - 2; Kp[t2] = v4u{kpv[h][2 * t2], kpv[h][2 * t2 + 1], kpv[4][2 * t2], kpv[4][2 * t2 + 1]}; PIN(Kp[t2]); }
    else { constexpr int t2 = z - 4; unsigned lo = g == h ? q4[t2].x : 0u, hi = g == h ? q4[t2].y : 0u; PIN(lo); PIN(hi); Qf[t2].z = lo; Qf[t2].w = hi; }
  };
  auto phi_op = [&](auto Z, const f32x4& t, auto H, unsigned& out) {
    constexpr int z = Z, h = H;
    if constexpr (z < 2) { ta[z] = phi_a(t[2 * h + z]); PIN(ta[z]); }
    else if constexpr (z < 4) { tb[z - 2] = phi_b(ta[z - 2]); PIN(tb[z - 2]); }
    else if constexpr (z < 6) { tc[z - 4] = phi_c(t[2 * h + z - 4]); PIN(tc[z - 4]); }
    else if constexpr (z < 8) { tc[z - 6] += tb[z - 6]; PIN(tc[z - 6]); }
    else { out = pk2(tc[0], tc[1]); PIN(out); }
  };

  a1s = lbase(o1 + L_SLOT4); a2s = lbase(o2 + L_SLOT4);
  aW = lbase(ISB ? L_SCR + ((w + 1) & 3) * 10240 + lane * 16 : L_SCR + w * 8192 + lane * 16);
  aR = lbase(ISB ? L_SCR + w * 10240 + lane * 16 : L_SCR + w * 2048 + lane * 16);
  st_off = ISB ? W_HALO + (hg * 16 + rt) * HALO_STR + lane * 16 : W_PART + (hg * 16 + rt) * PART_STR + w * 1024 + lane * 16;
  {
    const int fb = (ISB ? 20 : 0) + (hg >> 1) * 5, TT = rt * 8 + 2 * w;
    o_st = lane16 + W_OB + (TT * 40 + fb + ((hg & 1) ? 3 : 0)) * 1024;
    o_st1 = o_st + 40960;
    o_st2 = W_OB + ((TT + (g & 1)) * 40 + fb + 2) * 1024 + ((hg & 1) * 2 + (g >> 1)) * 256 + r * 16;
    foB = fo_of(jt2, w, lane, 1);
  }
  o_addr(oa, jt2, w, rj, lane);
  PIN(st_off); PIN(o_st); PIN(o_st1); PIN(o_st2); PIN(foB);
  LAS v2u* aZ0 = nullptr;
  if constexpr (!ISB) {
#pragma unroll
    for (int j = 0; j < 5; ++j) { const unsigned one = r == j ? 0x3F803F80u : 0u; Ej[j] = v4u{one, one, one, one}; PIN(Ej[j]); }
    kvs = (LAS v2u*)lbase(KVS + lane * 8); kvw = (LAS v2u*)lbase(KVS + w * 1024 + lane * 8); kvz = (LAS v2u*)lbase(KVS + (448 + g * 16) * 8);
    aZ0 = (LAS v2u*)lbase(KZERO);
    const unsigned sv = (g == (r >> 2)) ? ((r & 1) ? 0x3F800000u : 0x00003F80u) : 0u;
    sel0 = (r & 2) ? 0u : sv; sel1 = (r & 2) ? sv : 0u;
    PIN(sel0); PIN(sel1);
  } else {
#pragma unroll
    for (int e = 0; e < 4; ++e) {
      const int key = 4 * g + e;
      biasC[e] = (key <= r && key + 7 >= r) ? 0.f : -1e30f;
      biasP[e] = (key >= r + 9 && (rt > 0 || w > 0)) ? 0.f : -1e30f;
    }
#pragma unroll
    for (int e = 0; e < 2; ++e) {
      mC[e] = (4 * g + 2 * e <= r ? 0xFFFFu : 0u) | (4 * g + 2 * e + 1 <= r ? 0xFFFF0000u : 0u);
      mP[e] = ~mC[e];
      PIN(mC[e]); PIN(mP[e]);
    }
    ones4 = v4u{0x3F803F80u, 0x3F803F80u, 0x3F803F80u, 0x3F803F80u};
    PIN(ones4); PIN(biasC); PIN(biasP);
  }
  LAS char* apk = lbase(o1 + L_PACK + (w & 1) * UNIT);
  unsigned pk_dst = lane16 + W_WOP + unit * 5120;
  PIN(pk_dst);
  const char* Wg = (ISB ? p.wB : p.wA) + ((q.xcd >> 1) & 1) * 640;
  unsigned tw;
  { const int ln = 25 * (q.idx * 4 + w) + (lane < 25 ? lane : 0); tw = (unsigned)((ln / 5) * 3840 + (ln % 5) * 128); PIN(tw); }
  unsigned pk_off[5];
  const char* pk_src;
  {
    const int pf = unit % 40, pc = (unit / 40) & 7, pP = (pf % 20) / 5, pi = pf % 5;
    const int orow = 160 * pP + (pi == 2 ? 16 : (pi >= 3 ? 40 * pi - 40 : 40 * pi)), cgs = pi == 2 ? 40 : 4;
    pk_src = (pf >= 20 ? p.woB : p.woA) + pc * 160 + orow * 1280;
#pragma unroll
    for (int i = 0; i < 5; ++i) {
      const int c = i * 64 + lane, rho = c / 10, cc = c % 10;
      pk_off[i] = (unsigned)((cgs * ((rho >> 2) & 3) + 20 * (rho >> 4) + (rho & 3)) * 1280 + cc * 16);
      PIN(pk_off[i]);
    }
  }
  unsigned fl3 = lane < 8 ? W_FE + (b * 4 + w) * 128 : lane < 16 ? W_FO + ((rt * 16 + cg) * 4 + w) * 128 : W_F1 + (ISB ? 131072 : 0) + ((cg * 16 + rt) * 4 + w) * 128;
  fl3 = lane < 24 ? fl3 + (lane & 7) * 16 : OOB;
  PIN(fl3);
  STAMP(14);
  STAMP(15);
  sfor<S1PRE, 5>([&](auto I) { load_w<1, I>(q); });
  if (unit < 320) sfor<0, 5>([&](auto I) { dma16<L_PACK + I * 1024>(pk_src, pk_off[I], lw); });
  STAMP(13);
  if (unit < 320) wait_vm<10>(); else wait_vm<5>();
  BAR();
  STAMP(1);
  ept = *(LAS v2u*)(L + L_AUX + 3072);
  sfor<0, 3>([&](auto U) { readX(IC(0), U); });
  sfor<0, AHEAD>([&](auto F_) { readW(F_, IC(0)); readW(F_, IC(1)); });
  sfor<3, 12>([&](auto U) { readX(IC(0), U); });
  tag = (((u64)(unsigned)__builtin_amdgcn_readfirstlane(ept.y) << 32) | (unsigned)__builtin_amdgcn_readfirstlane(ept.x)) + 1ull;
  tag4 = v4u{(unsigned)tag, (unsigned)(tag >> 32), (unsigned)tag, (unsigned)(tag >> 32)};
  PIN(tag4);
  SB0();

  sfor2<0, 600>([&](auto M) {
    constexpr int m = M, f = m / 2, tt = m & 1, ks = fr_k(f), jt = fr_j(f), ch = st_ch(fr_s(f));
    constexpr int sb = (m + 16) / 40 - 1, mb = (m + 16) % 40;
    constexpr int tv = (m - 368) / 8, uv = (m - 368) % 8, jv = (tv + 4) % 5;
    constexpr int tq = (m - 504) / 24, uq = (m - 504) % 24, jq = (tq + 4) % 5;
    if constexpr (mb == 0 && sb >= 0 && sb <= 11) {
      constexpr int cntA[12] = {13, 13, 13, 9, 13, 7, 5, 6, 10, 14, 18, 6};
      constexpr int cntB[12] = {13, 13, 13, 9, 13, 7, 5, 6, 10, 14, 20, 5};
      constexpr int cnt = ISB ? cntB[sb] : cntA[sb];
      if constexpr (sb == 3) { if (unit < 320) wait_vm<cnt + 5>(); else wait_vm<cnt>(); }
      else if constexpr (ISB && (sb == 7 || sb == 8)) { if (w == 3) wait_vm<cnt + 5>(); else wait_vm<cnt>(); PIN(poll); }
      else wait_vm_p<cnt>(poll);
      BAR();
      SB0();
    }
    if constexpr (m % 8 == 0 && m > 0 && f + AHEAD <= 300) { __builtin_amdgcn_s_waitcnt(0xC07F | (8 << 8)); SB0(); }
    {
      f32x4& d = acc[ch][tt][jt];
      const v4u& wv = wf[f % RING];
      if constexpr (ch == 2 || (ch == 0 && ISB)) {
        if constexpr (ks != 0) mm_va(d, wv, xf[ks][tt]);
        else if constexpr (ISB) mm_va0(d, wv, xf[ks][tt]);
        else mm_vac(d, wv, xf[ks][tt], eps4);
      } else {
        if constexpr (ks != 0) mm_av(d, xf[ks][tt], wv);
        else if constexpr (ch == 0) mm_avc(d, xf[ks][tt], wv, eps4);
        else mm_av0(d, xf[ks][tt], wv);
      }
    }
    SB0();
    if constexpr (f + AHEAD < 300) readW(IC(f + AHEAD < 300 ? f + AHEAD : 0), IC(tt));
    if constexpr (((sb >= 1 && sb <= 3) || sb == 5) && mb < 12) readX(IC(sb == 5 ? 4 : sb), IC(mb));
    SB0();
    if constexpr (ch == 0 && m % 10 == 9) {
      constexpr int kg = 4 * st_kb(m / 40) + (m % 40) / 10;
      if constexpr (kg == 0) { mm_va0(gacc[0], gf2[0][0], xf[0][0]); mm_va0(gacc[1], gf2[0][0], xf[0][1]); }
      else { mm_va(gacc[0], gf2[(kg >> 2) & 1][kg & 3], xf[kg][0]); mm_va(gacc[1], gf2[(kg >> 2) & 1][kg & 3], xf[kg][1]); }
    }
    auto split_w = [&](auto S, auto P0, auto ST) {
      constexpr int p0 = P0, st = ST;
      if constexpr (mb >= p0 && (mb - p0) / st < 5) {
        if constexpr ((mb - p0) % st == 0) w_m0<S, (mb - p0) / st>(q);
        if constexpr ((mb - p0) % st == 1) w_go<S, (mb - p0) / st>(q);
      }
    };
    auto split_x = [&](auto XS, auto I0, auto N, auto P0, auto ST) {
      constexpr int p0 = P0, st = ST, xs = XS;
      if constexpr (mb >= p0 && (mb - p0) / st < N) {
        constexpr int i = I0 + (mb - p0) / st;
        if constexpr ((mb - p0) % st == 0) x_m0<xs, i>(q);
        if constexpr ((mb - p0) % st == 1) x_go<xs, i>(q);
      }
    };
    if constexpr (m >= X1M && m < X1M + 8) load_x<1, m - X1M>(q);
    if constexpr (m >= 19 && m < 24) load_w<2, m - 19>(q);
    if constexpr (sb == 0) { split_x(IC(2), IC(0), IC(8), IC(1), IC(3)); split_w(IC(3), IC(25), IC(3)); }
    if constexpr (sb == 1) { split_w(IC(4), IC(2), IC(4)); split_x(IC(3), IC(0), IC(7), IC(25), IC(2)); if constexpr (mb == 39) load_x<3, 7>(q); }
    if constexpr (sb == 2 || sb == 3) { split_w(IC(sb + 3), IC(2), IC(4)); split_x(IC(4), IC((sb - 2) * 4), IC(4), IC(25), IC(4)); }
    if constexpr (sb >= 4 && sb <= 6) split_w(IC(sb + 3), IC(2), IC(8));
    if constexpr (sb == 7) { split_w(IC(10), IC(2), IC(4)); split_w(IC(11), IC(21), IC(4)); }
    if constexpr (sb == 8 || sb == 9) split_w(IC(sb + 4), IC(2), IC(8));
    if constexpr (sb == 10) split_w(IC(14), IC(1), IC(3));
    if constexpr (m == TQM) touch(Wg, tw);
    if constexpr (m == PACKM) {
      if (unit < 320) {
        v4u pv[5];
        sfor<0, 5>([&](auto J) {
          const v2u lo = trread(apk + 8 * J), hi = trread(apk + 2560 + 8 * J);
          pv[J] = v4u{lo.x, lo.y, hi.x, hi.y};
        });
        sfor<0, 5>([&](auto J) { ST128(pv[J], pk_dst + J * 1024); });
      }
    }
    if constexpr (m == 185) {
      const unsigned x = lane < 8 ? 0u : ~0u;
      ST128((tag4 ^ v4u{x, x, x, x}), fl3);
    }
    if constexpr (m == 265) poll_async(poll, p.ws, poll0_off);
    if constexpr (m == 345) settle_flags(p.ws, poll0_off, tag, poll);
    if constexpr (m == 286) { PIN(gacc[0]); PIN(gacc[1]); }
    if constexpr (m == 483 || m == 487) {
      constexpr int t2 = (m - 483) / 4;
      gate[t2] = __builtin_amdgcn_rcpf(1.f + __builtin_amdgcn_exp2f(gacc[t2][0] * (ISB ? LOG2E : -LOG2E)));
      PIN(gate[t2]);
    }
    if constexpr (sb >= 8 && sb <= 9 && mb % 8 == 6 && mb < 32) ld_wo(IC((sb - 8) * 4 + mb / 8));
    if constexpr (m >= 445 && (m - 445) % 4 == 0 && 8 + (m - 445) / 4 < NWO1) ld_wo(IC(8 + (m - 445) / 4));

    if constexpr (!ISB) {
      if constexpr (m >= 252 && m < 387) {
        sfor<(m - 252) * 4 / 3, (m - 251) * 4 / 3>([&](auto Z) {
          constexpr int z = Z, kt = (z / 36 + 4) % 5, pr = z % 36 / 9, t2 = pr >> 1, h = pr & 1;
          unsigned o = 0;
          phi_op(IC(z % 9), acc[0][t2][kt], IC(h), o);
          if constexpr (z % 9 == 8) Kf[kt][2 * t2 + h] = o;
          if constexpr (z % 36 == 35) PIN(Kf[kt]);
        });
      }
      if constexpr (m >= 281 && m <= 389 && (m - 281) % 27 == 0) {
        constexpr int kt = ((m - 281) / 27 + 4) % 5;
        if constexpr (m == 281) mm_vv0(kvZ, Kf[kt], Ej[kt]); else mm_vv(kvZ, Kf[kt], Ej[kt]);
      }
      if constexpr (m >= 282 && m < 362 && (m - 282) % 5 == 0) {
        constexpr int z = (m - 282) / 5;
        unsigned o = (r >> 2) == (z >> 2) ? Kf[4][z & 3] : 0u;
        PIN(o);
        K4m[z >> 2][z & 3] = o;
      }
      LAS f32x4* red = (LAS f32x4*)aW;
      if constexpr (m >= 368 && tv < 5) {
        if constexpr (uv == 3 || uv == 4) {
          constexpr int t2 = uv - 3;
          unsigned lo = pk2(acc[1][t2][jv][0], acc[1][t2][jv][1]), hi = pk2(acc[1][t2][jv][2], acc[1][t2][jv][3]);
          PIN(lo); PIN(hi);
          Vf[jv][2 * t2] = lo; Vf[jv][2 * t2 + 1] = hi;
        }
        if constexpr (uv == 5) { PIN(Vf[jv]); if constexpr (tv > 0) PIN(K4m[jv & 3]); }
        if constexpr (tv == 0) { if constexpr (uv == 6) mm_vv0(kvX, Kf[4], Vf[4]); }
        else {
          if constexpr (uv < 2) {
            unsigned lo = (r >> 2) == jv ? Vf[4][2 * uv] : 0u, hi = (r >> 2) == jv ? Vf[4][2 * uv + 1] : 0u;
            PIN(lo); PIN(hi);
            V4m[2 * uv] = lo; V4m[2 * uv + 1] = hi;
          }
          if constexpr (uv == 2) { PIN(V4m); if constexpr (tv == 1) red[6 * 64] = kvX; else red[(jv - 1) * 64] = kvT; }
          if constexpr (uv == 4) { if constexpr (tv == 1) mm_vv0(kvW, Kf[jv], V4m); else mm_vv(kvW, Kf[jv], V4m); }
          if constexpr (uv == 6) mm_vv0(kvT, Kf[jv], Vf[jv]);
          if constexpr (uv == 7) { if constexpr (tv == 1) mm_vv0(kvU, K4m[jv & 3], Vf[jv]); else mm_vv(kvU, K4m[jv & 3], Vf[jv]); }
        }
      }
      if constexpr (m == 397) red[7 * 64] = kvZ;
      if constexpr (m == 409) red[5 * 64] = kvW;
      if constexpr (m == 412) red[3 * 64] = kvT;
      if constexpr (m == 415) red[4 * 64] = kvU;
      if constexpr (m == 423) asm volatile("s_waitcnt lgkmcnt(8)" ::: "memory");
      if constexpr (m >= 424 && m < 432) rd[m - 424] = *(LAS f32x4*)(aR + ((m - 424) >> 1) * 8192 + ((m - 424) & 1) * 1024);
      if constexpr (m >= 433 && m < 441) {
        sfor<0, 3>([&](auto E) {
          constexpr int z = 3 * (m - 433) + E, qq = z >> 2, e = z & 3;
          float s = rd[qq & 1][e] + rd[2 + qq][e]; PIN(s);
          rd[qq & 1][e] = s;
        });
      }
      if constexpr (m == 441 || m == 442) {
        unsigned lo = pk2(rd[m - 441][0], rd[m - 441][1]), hi = pk2(rd[m - 441][2], rd[m - 441][3]);
        PIN(lo); PIN(hi);
        pst[2 * (m - 441)] = lo; pst[2 * (m - 441) + 1] = hi;
      }
      if constexpr (m == 444) ST128(pst, st_off);
      if constexpr (m == 446) *aZ0 = v2u{0u, 0u};
      if constexpr (m >= 448 && m < 480 && m % 2 == 0) {
        constexpr int h = (m - 448) >> 3, k = ((m - 448) >> 1) & 3;
        const bool gh = g == h, rh = (r >> 2) == h;
        auto pick = [&](bool c, LAS v2u* a) { unsigned v = (unsigned)(uintptr_t)(c ? a : aZ0); PIN(v); return (LAS v2u*)(uintptr_t)v; };
        if constexpr (k == 0) aU[h] = pick(gh, kvs + 256);
        if constexpr (k == 1) aWt[h] = pick(rh, kvs + 320);
        if constexpr (k == 2) aX[h] = pick(rh && gh, kvs + 384);
        if constexpr (k == 3) aZ[h] = pick(gh, kvz + 4);
      }
      if constexpr (m == 494) { wait_vm<13>(); flag(W_F1 + ((cg * 16 + rt) * 4 + w) * 128); }
      if constexpr (m == GPOLL) poll = ld_flag(p.ws, poll1_off);
      if constexpr (m == GPOLL + 37) settle_flags(p.ws, poll1_off, tag, poll);
      if constexpr (m >= GPOLL + 38 && m < GPOLL + 54) gl[m - GPOLL - 38] = LD128S(lane16, W_PART + (hg * 16 + m - GPOLL - 38) * PART_STR + w * 1024);
      if constexpr (m >= 504 && uq >= 4 && uq < 22) {
        sfor<0, 2>([&](auto E) {
          constexpr int z = 2 * (uq - 4) + E, pr = z / 9, t2 = pr >> 1, h = pr & 1;
          unsigned o = 0;
          phi_op(IC(z % 9), acc[2][t2][jq], IC(h), o);
          if constexpr (z % 9 == 8) Pq[t2][jq][h] = o;
        });
      }
    } else {
      if constexpr (m >= 248 && m < 288 && (m - 248) % 8 >= 4) {
        constexpr int kt = ((m - 248) / 8 + 4) % 5, z = (m - 248) % 8 - 4, t2 = z >> 1, e = z & 1;
        unsigned o = pk2(acc[0][t2][kt][2 * e], acc[0][t2][kt][2 * e + 1]);
        PIN(o);
        kx[kt][2 * t2 + e] = o;
      }
      if constexpr (m == 302) { if (w == 3) sfor<0, 5>([&](auto J) { ST128(kx[J], st_off + J * 1024); }); }
      if constexpr (m >= 348 && m < 368 && (m - 348) % 4 == 0) *(LAS v4u*)(aW + (m - 348) / 4 * 1024) = kx[(m - 348) / 4];
      if constexpr (m >= 368 && tv < 5) {
        if constexpr (uv == 3 || uv == 4) {
          constexpr int t2 = uv - 3;
          unsigned lo = pk2(acc[1][t2][jv][0], acc[1][t2][jv][1]), hi = pk2(acc[1][t2][jv][2], acc[1][t2][jv][3]);
          PIN(lo); PIN(hi);
          vx5[jv][2 * t2] = lo; vx5[jv][2 * t2 + 1] = hi;
        }
        if constexpr (uv == 6) *(LAS v4u*)(aW + (5 + jv) * 1024) = vx5[jv];
      }
      if constexpr (m == 391) { if (w == 3) { ST128(vx5[4], st_off + 9 * 1024); ST128(vx5[0], st_off + 5 * 1024); ST128(vx5[1], st_off + 6 * 1024); } }
      if constexpr (m == 406) { if (w == 3) { ST128(vx5[2], st_off + 7 * 1024); ST128(vx5[3], st_off + 8 * 1024); } }
      if constexpr (m >= 410 && m < 420) { constexpr int j = (m - 410) >> 1; Vb[tt][j].z = vx5[j][2 * tt]; Vb[tt][j].w = vx5[j][2 * tt + 1]; }
      auto halo_in = [&](auto R) {
        if (w == 0) {
          if constexpr (R == 1) wait_vm_p<9>(poll);
          settle_flags(p.ws + R * 131072, poll1_off, tag, poll);
          dma_m0<L_SCR + R * 5120>(0u); NOPS(0);
          sfor<0, 4>([&](auto J) { dma_sc1<J * 1024>(halo_src + R * 5120, lane16); });
          dma_m0<L_SCR + R * 5120 + 4096>(0u); NOPS(0); dma_sc1<0>(halo_src + R * 5120 + 4096, lane16);
        }
      };
      if constexpr (m == 385) flag(W_F1 + ((cg * 16 + rt) * 4 + w) * 128);
      if constexpr (m == 421) poll_async(poll, p.ws, poll1_off);
      if constexpr (m == 466) { halo_in(IC(0)); flag(W_F1 + 131072 + ((cg * 16 + rt) * 4 + w) * 128); }
      if constexpr (m == 494) poll_async(poll, p.ws + 131072, poll1_off);
      if constexpr (m == 506) wait_vm<12>();
      if constexpr (m >= 506 && m < 511) kpv[m - 506] = *(LAS v4u*)(aR + (m - 506) * 1024);
      if constexpr (m == 530) halo_in(IC(1));
      if constexpr (m == 566) wait_vm_p<9>(poll);
      if constexpr (m >= 566 && m < 576) {
        constexpr int j = ((m - 566) / 2 + 4) % 5;
        const v2u v = *(LAS v2u*)(aR + (5 + j) * 1024 + tt * 8);
        Vb[tt][j].x = v.x; Vb[tt][j].y = v.y;
      }
      if constexpr (m == 511 || m == 513) {
        constexpr int t2 = (m - 511) / 2;
        q4[t2] = v2u{pk2(acc[2][t2][4][0], acc[2][t2][4][1]), pk2(acc[2][t2][4][2], acc[2][t2][4][3])};
        PIN(q4[t2]);
      }
      if constexpr (m >= 516 && m < 528 && m % 2 == 0) att_pre(IC(0), IC((m - 516) / 2));
      if constexpr (m >= 528) {
        constexpr int h = tq - 1;
        constexpr int z0[25] = {0, 0, 0, 0, 2, 5, 7, 9, 9, 9, 9, 13, 17, 21, 22, 23, 24, 28, 32, 35, 38, 41, 46, 52, 52};
        sfor<z0[uq], z0[uq + 1]>([&](auto Z) { att_op(IC(h), IC(Z < 9 ? Z : Z + 8)); });
        if constexpr (uq >= 7 && uq < 10) { att_pre(IC(h + 1), IC(2 * (uq - 7))); att_pre(IC(h + 1), IC(2 * (uq - 7) + 1)); }
        if constexpr (h == 2 && uq < 18) pv_op(IC(0), IC(uq + 6));
      }
    }
    SB0();
    if constexpr (m == 199) { STUSE; STAMP(2); }
    if constexpr (m == 399) { STUSE; STAMP(3); }
  });
  STAMP(4);
  constexpr int WO_PRE = ISB ? 46 : 40;
  sfor<NWO1, WO_PRE>([&](auto Z) { ld_wo(Z); });
  NOPS(3);
  SB0();
  if constexpr (!ISB) {
    sfor<0, 36>([&](auto Z) {
      constexpr int z = Z, pr = z / 9, t2 = pr >> 1, h = pr & 1;
      unsigned o = 0;
      phi_op(IC(z % 9), acc[2][t2][3], IC(h), o);
      if constexpr (z % 9 == 8) Pq[t2][3][h] = o;
    });
    v4u Pf[2][4];
#pragma unroll
    for (int h = 0; h < 4; ++h)
#pragma unroll
      for (int tt = 0; tt < 2; ++tt) { Pf[tt][h] = v4u{Pq[tt][h].x, Pq[tt][h].y, Pq[tt][4].x, Pq[tt][4].y}; PIN(Pf[tt][h]); }
    v4u A1 = {sel0, sel1, 0u, 0u}, A2 = {0u, 0u, sel0, sel1};
    PIN(A1); PIN(A2); NOPS(1);
    SB0();
    STAMP(16);
    f32x4 d1, d2;
#pragma unroll
    for (int i = 0; i < 16; ++i) {
      if (i == 0) { mm_vv0(d1, A1, gl[0]); mm_vv0(d2, A2, gl[0]); }
      else { mm_vv(d1, A1, gl[i]); mm_vv(d2, A2, gl[i]); }
    }
    NOPS(7);
    SB0();
    STAMP(17);
    kvw[0] = v2u{pk2(d1[0], d1[1]), pk2(d1[2], d1[3])};
    kvw[64] = v2u{pk2(d2[0], d2[1]), pk2(d2[2], d2[3])};
    wait_lgkm();
    BAR();
    STAMP(5);
    v4u Nh[4], N4[4], Zh[4];
#pragma unroll
    for (int h = 0; h < 4; ++h) { const v2u lo = kvz[h], hi = *aZ[h]; Zh[h] = v4u{lo.x, lo.y, hi.x, hi.y}; SB0(); }
#pragma unroll
    for (int h = 0; h < 4; ++h) { const v2u lo = kvs[h * 64], hi = *aU[h]; Nh[h] = v4u{lo.x, lo.y, hi.x, hi.y}; SB0(); }
#pragma unroll
    for (int h = 0; h < 4; ++h) { const v2u lo = *aWt[h], hi = *aX[h]; N4[h] = v4u{lo.x, lo.y, hi.x, hi.y}; SB0(); }
    poll = ld_flag(p.ws, foB);
    SB0();
    f32x4 nm[2][4], dn[2][4];
#pragma unroll
    for (int h = 0; h < 4; ++h)
#pragma unroll
      for (int tt = 0; tt < 2; ++tt) mm_vvc(dn[tt][h], Zh[h], Pf[tt][h], eps4);
    SB0();
#pragma unroll
    for (int h = 0; h < 4; ++h)
#pragma unroll
      for (int tt = 0; tt < 2; ++tt) {
        mm_vv0(nm[tt][h], Nh[h], Pf[tt][h]);
        inv[tt][h] = __builtin_amdgcn_rcpf(dn[tt][h][0]);
        PIN(inv[tt][h]); PIN(dn[tt][h]);
        SB0();
      }
#pragma unroll
    for (int h = 0; h < 4; ++h)
#pragma unroll
      for (int tt = 0; tt < 2; ++tt) {
        if (h == 0) mm_vv0(num4[tt], N4[h], Pf[tt][h]); else mm_vv(num4[tt], N4[h], Pf[tt][h]);
        inv[tt][h] *= gate[tt];
        PIN(inv[tt][h]);
        SB0();
      }
#pragma unroll
    for (int hp = 0; hp < 2; ++hp)
#pragma unroll
      for (int tt = 0; tt < 2; ++tt) {
        const f32x4 oa = nm[tt][2 * hp] * inv[tt][2 * hp], ob = nm[tt][2 * hp + 1] * inv[tt][2 * hp + 1];
        F[tt][hp] = v4u{pk2(oa[0], oa[1]), pk2(oa[2], oa[3]), pk2(ob[0], ob[1]), pk2(ob[2], ob[3])};
        ST128(F[tt][hp], (tt ? o_st1 : o_st) + hp * 1024);
        SB0();
      }
  } else {
    sfor<0, 9>([&](auto Z) { att_op(IC(3), Z); });
    SB0();
    sfor<9, 17>([&](auto Z) { att_op(IC(1), Z); });
    PIN(biasC); PIN(biasP);
    sfor<60, 67>([&](auto Z) { att_op(IC(1), Z); });
    SB0();
    sfor<17, 32>([&](auto Z) { att_op(IC(3), Z); });
    sfor<0, 16>([&](auto Z) { fin_op(IC(1), Z); });
    ST128(F[0][0], o_st); ST128(F[1][0], o_st1);
    sfor<9, 17>([&](auto Z) { att_op(IC(2), Z); });
    sfor<60, 67>([&](auto Z) { att_op(IC(2), Z); });
    SB0();
    STAMP(5);
    sfor<32, 60>([&](auto Z) { att_op(IC(3), Z); });
    sfor<0, 16>([&](auto Z) { fin_op(IC(2), Z); });
    sfor<9, 17>([&](auto Z) { att_op(IC(3), Z); });
    sfor<60, 67>([&](auto Z) { att_op(IC(3), Z); });
    NOPS(3);
    SB0();
    sfor<0, 16>([&](auto Z) { fin_op(IC(3), Z); });
    ST128(F[0][1], o_st + 1024); ST128(F[1][1], o_st1 + 1024);
    SB0();
  }
  {
    v2u F2[2];
#pragma unroll
    for (int tt = 0; tt < 2; ++tt) {
      const f32x4 o4 = num4[tt] * sel4(g, inv[tt][0], inv[tt][1], inv[tt][2], inv[tt][3]);
      F2[tt] = v2u{pk2(o4[0], o4[1]), pk2(o4[2], o4[3])};
    }
    const auto sx = __builtin_amdgcn_permlane16_swap(F2[0].x, F2[1].x, false, false);
    const auto sy = __builtin_amdgcn_permlane16_swap(F2[0].y, F2[1].y, false, false);
    ST128((v4u{sx[0], sy[0], sx[1], sy[1]}), o_st2);
  }
  STAMP(6);
  SB0();
  auto load_o0 = [&](auto Z0, auto Z1) { sfor<Z0, Z1>([&](auto Z) { of0[Z / 5][Z % 5] = LD128S(oa.vo[Z % 5], oa.so[Z / 5]); SB0(); }); };
  if constexpr (ISB) {
    sfor<WO_PRE, 50>([&](auto Z) { ld_wo(Z); });
    SB0();
    wait_vm<50 - WO_PRE>();
    flag(W_FO + ((rt * 16 + cg) * 4 + w) * 128);
    STAMP(7);
    if (b == 4) {
      const unsigned fa = W_FE + (w * 64 + lane) * 512;
      settle_flags(p.ws, fa, tag, ld_flag(p.ws, fa));
    }
    BAR();
    if (b == 4 && w == 0) flag(W_EPOCH);
    __builtin_amdgcn_s_sleep(BDLY);
    settle_flags(p.ws, foB, tag, ld_flag(p.ws, foB));
    load_o0(IC(0), IC(OPRE));
  } else {
    if (__all(poll == tag)) {
      __builtin_amdgcn_fence(__ATOMIC_ACQUIRE, "wavefront");
      load_o0(IC(0), IC(OPRE));
      wait_vm<OPRE>();
      flag(W_FO + ((rt * 16 + cg) * 4 + w) * 128);
      STAMP(7);
    } else {
      poll = ld_flag(p.ws, foB);
      SB0();
      wait_vm<1>();
      flag(W_FO + ((rt * 16 + cg) * 4 + w) * 128);
      STAMP(7);
      settle_flags(p.ws, foB, tag, poll);
      load_o0(IC(0), IC(OPRE));
    }
  }
  load_o0(IC(OPRE), IC(10));
  asm volatile("" ::"v"(tag4));
  STAMP(8);
}

template <int CHK, bool ISB>
__device__ __forceinline__ void phase2(const Params& p, LAS char* L, __amdgpu_buffer_rsrc_t rs, int b, int w, int lane, int jt, int c, int rj, u64 tag,
                                       OAddr& oa, v4u (&of0)[4][5], v4u (&wo)[50] STARG) {
  const int r = lane & 15, g = lane >> 4;
  v4u of1[4][5];
  f32x4 acc[4][5];
  unsigned lane16 = lane * 16;
  PIN(lane16);
  const unsigned foA = fo_of(jt, w, lane, 0);
  u64 pollA = 0;
  unsigned ra = (unsigned)(uintptr_t)L + lane16;
  unsigned rw[3];
#pragma unroll
  for (int a = 0; a < 3; ++a) { rw[a] = ra + ((((a + w + 1) & 3) * 3 + a) * 5) * 1024; PIN(rw[a]); }
  sfor2<0, 200>([&](auto M) {
    constexpr int m = M, hb = m / 100, a = (m % 100) / 25, i = (m % 25) / 5, n = m % 5;
    if constexpr (m == CHK) { settle_flags(p.ws, foA, tag, pollA); STUSE; STAMP(9); }
    if constexpr (hb == 0 && i == 0) mm_av0(acc[a][n], wo[n], of0[a][0]);
    else mm_av(acc[a][n], wo[hb * 25 + i * 5 + n], hb ? of1[a][i] : of0[a][i]);
    SB0();
    if constexpr (m < 20 && m % 2 == 0) of0[2 + m / 10][m / 2 % 5] = LD128S(oa.vo[m / 2 % 5], oa.so[2 + m / 10]);
    if constexpr (!ISB && m >= 26 && m < 56 && (m - 26) % 3 == 0) {
      constexpr int z = 40 + (m - 26) / 3;
      wo[wo_idx(z)] = LD128S(lane16 + ((z % 5) & 3) * 1024, W_WOP + (c * 40 + 5 * w) * 5120 + rot5(rj, wo_i(z)) * 5120 + ((z % 5) >> 2) * 4096);
    }
    if constexpr (m == CHK - 35) pollA = ld_flag(p.ws, foA);
    if constexpr (m >= CHK && m < CHK + 40 && (m - CHK) % 2 == 0) of1[(m - CHK) / 10][(m - CHK) / 2 % 5] = LD128S(oa.vo[(m - CHK) / 2 % 5], oa.so[(m - CHK) / 10] - 20480);
    if constexpr (m >= 130 && (m - 130) % 25 < 20 && (m - 130) % 25 % 4 == 0) {
      constexpr int a2 = (m - 130) / 25, n2 = (m - 130) % 25 / 4;
      *(LAS f32x4*)(uintptr_t)(rw[a2] + n2 * 1024) = acc[a2][n2];
    }
    SB0();
  });
  STAMP(10);
  wait_lgkm();
  BAR();
  LAS f32x4* red = (LAS f32x4*)(uintptr_t)(ra + w * 15 * 1024);
  unsigned o[10];
#pragma unroll
  for (int n = 0; n < 5; ++n) {
    const f32x4 s = (red[n * 64] + red[(5 + n) * 64]) + (red[(10 + n) * 64] + acc[3][n]);
    o[2 * n] = pk2(s[0], s[1]);
    o[2 * n + 1] = pk2(s[2], s[3]);
  }
  const int tok = 64 * jt + 32 * (w >> 1) + 2 * r + (w & 1);
  GAS char* op = (GAS char*)p.out + (size_t)tok * 1280 + c * 160 + g * 40;
  typedef unsigned v4u8 __attribute__((ext_vector_type(4), aligned(8)));
  *(GAS v4u8*)op = v4u8{o[0], o[1], o[2], o[3]};
  *(GAS v4u8*)(op + 16) = v4u8{o[4], o[5], o[6], o[7]};
  *(GAS v2u*)(op + 32) = v2u{o[8], o[9]};
  STAMP(11);
}

__global__ __launch_bounds__(256, 1) void fused_kernel(Params p) {
  extern __shared__ __attribute__((aligned(16))) char smem[];
  LAS char* L = (LAS char*)smem;
  const int b = blockIdx.x, lane = threadIdx.x & 63;
  const int w = __builtin_amdgcn_readfirstlane(threadIdx.x >> 6);
  STAMP(0);
  const int xcd = b & 7, idx = b >> 3;
  const int rt = (xcd & 1) * 8 + (idx & 7), cg = (xcd >> 1) * 4 + (idx >> 3);
  const int jt = xcd * 4 + (idx & 3), c2 = idx >> 2;
  const int r = lane & 15, g = lane >> 4;
  {
    const char* pc; unsigned len;
    asm volatile("s_getpc_b64 %0\nfk_here:\n\ts_mov_b32 %1, fk_end-fk_here" : "=s"(pc), "=s"(len));
    pc = (const char*)((uintptr_t)pc & ~(uintptr_t)63);
    unsigned co = (unsigned)((idx * 4 + w) * 1024 + lane * 16);
    co = co + 16 <= len ? co : 0u;
    asm volatile("s_mov_b32 m0, %2\n\ts_nop 0\n\tglobal_load_lds_dwordx4 %0, %1" ::"v"(co), "s"(pc), "s"((unsigned)(L_AUX + 2048)) : "memory");
  }
  const __amdgpu_buffer_rsrc_t rs = __builtin_amdgcn_make_buffer_rsrc((void*)p.ws, 0, W_BYTES, 0x00020000);
  Lane q;
  q.rot = idx % 5;
  q.lw = (unsigned)(w * UNIT);
  q.lx = (unsigned)(w * 16384);
#pragma unroll
  for (int i = 0; i < 8; ++i) {
    const int e = i & 1, tt = (i >> 1) & 1, hh = i >> 2;
    q.xo[i] = (unsigned)((16 * e + 2 * (lane >> 3) + tt) * 1280 + hh * 128 + (((lane & 7) ^ (4 * e + g)) * 16) + 4096 - (i & 3) * 1024);
  }
  q.Wb = (cg < 8 ? p.wA : p.wB) + (cg & 7) * 160 + (size_t)w * 32 * 3840 - 4096;
  q.xb = p.x + (size_t)(rt * 128 + w * 32) * 1280 - 4096;
  sfor<0, 8>([&](auto I) { load_x<0, I>(q); });
  STAMP(12);
#pragma unroll
  for (int i = 0; i < 5; ++i) {
    const int c = i * 64 + lane, rho = c / 10, cc = c % 10;
    q.woff[i] = (unsigned)((8 * ((rho >> 2) & 3) + 4 * (rho >> 4) + (rho & 3)) * 3840 + cc * 16 + 4096 - (i & 3) * 1024);
  }
  sfor<0, 2>([&](auto I) { load_w<0, I>(q); });
  sfor<2, 5>([&](auto I) { load_w<0, I>(q); });
  {
    const char* ab = sel4(w, p.gw, p.gw + 1024, p.gw, (const char*)p.ws);
    const unsigned ao = (unsigned)((w == 1 ? r : lane) * 16);
    asm volatile("s_mov_b32 m0, %2\n\ts_nop 0\n\tglobal_load_lds_dwordx4 %0, %1 sc1" ::"v"(ao), "s"(ab), "s"((unsigned)(L_AUX + w * 1024)) : "memory");
  }
  sfor<0, S1PRE>([&](auto I) { load_w<1, I>(q); });
  q.idx = idx; q.xcd = xcd;
  u64 tag = 0;
  v4u wo[50], of0[4][5];
  OAddr oa;
  const int rj = (idx & 1) * 2;
  if (cg < 8) {
    phase1<false>(p, q, L, rs, b, rt, cg, w, lane, c2, jt, rj, tag, oa, of0, wo STPASS);
    phase2<P2CHK, false>(p, L, rs, b, w, lane, jt, c2, rj, tag, oa, of0, wo STPASS);
  } else {
    phase1<true>(p, q, L, rs, b, rt, cg - 8, w, lane, c2, jt, rj, tag, oa, of0, wo STPASS);
    phase2<P2CHKB, true>(p, L, rs, b, w, lane, jt, c2, rj, tag, oa, of0, wo STPASS);
  }
  asm volatile("fk_end:");
}

extern "C" void kernel_launch(void* const* d_in, const int* in_sizes, int n_in, void* d_out, int out_size, void* d_ws, size_t ws_size,
                              hipStream_t stream) {
  Params p;
  p.x = (const char*)d_in[0];
  p.wA = (const char*)d_in[1];
  p.woA = (const char*)d_in[2];
  p.wB = (const char*)d_in[4];
  p.woB = (const char*)d_in[5];
  p.gw = (const char*)d_in[6];
  p.out = (char*)d_out;
  p.ws = (char*)d_ws;
  (void)hipFuncSetAttribute((const void*)fused_kernel, hipFuncAttributeMaxDynamicSharedMemorySize, LDS_BYTES);
  fused_kernel<<<256, 256, LDS_BYTES, stream>>>(p);
}
```

```cpp
#include <hip/hip_runtime.h>
#include <stdint.h>
#include <type_traits>

#define LAS __attribute__((address_space(3)))
#define GAS __attribute__((address_space(1)))
typedef short s16x4 __attribute__((ext_vector_type(4)));
typedef unsigned v4u __attribute__((ext_vector_type(4)));
typedef unsigned v2u __attribute__((ext_vector_type(2)));
typedef float f32x4 __attribute__((ext_vector_type(4)));
typedef float f32x2 __attribute__((ext_vector_type(2)));
typedef __bf16 bf2 __attribute__((ext_vector_type(2)));
typedef unsigned long long u64;


constexpr int SLP = 2, BDLY = 4, GPOLL = 546, P2CHKB = 75, OPRE = 5, X1M = 11, S1PRE = 5, TQM = 184, PACKM = 105, P2CHK = 60;

constexpr int UNIT = 5120, SLOT = 4 * UNIT;
constexpr int L_XST = 4 * SLOT;
constexpr int L_SCR = L_XST;
constexpr int L_SLOT4 = L_SCR + 40960;
constexpr int L_PACK = L_XST + 65536;
constexpr int L_AUX = L_PACK + 2 * UNIT;
constexpr int LDS_BYTES = 163840;
static_assert(L_SLOT4 + SLOT <= L_PACK && L_AUX + 4096 <= LDS_BYTES, "lds map");
constexpr int slot_of(int s) { return s <= 10 ? (s & 3) : s == 11 ? 4 : (s - 9) & 3; }
constexpr int slot_base(int s) { return slot_of(s) == 4 ? L_SLOT4 : slot_of(s) * SLOT; }
constexpr int W_EPOCH = 0;
constexpr int W_FE = 131072, W_FO = 2 * 131072, W_F1 = 3 * 131072;
constexpr int W_PART = 1 << 20, PART_STR = 4096;
constexpr int W_HALO = 2 << 20, HALO_STR = 10240;
constexpr int W_WOP = 4 << 20;
constexpr int W_OB = 6 << 20;
constexpr int W_BYTES = W_OB + 128 * 40 * 1024;
constexpr int SC1 = 16;
constexpr unsigned OOB = 0x40000000u;
constexpr unsigned SPIN_CAP = 1u << 16;

struct Params {
  const char *x, *wA, *woA, *wB, *woB, *gw;
  char *out, *ws;
};

template <int I, int N, class F> __device__ __forceinline__ void sfor(F&& f) {
  if constexpr (I < N) { f(std::integral_constant<int, I>{}); sfor<I + 1, N>(f); }
}
template <int I, int N, class F> __device__ __forceinline__ void sfor2(F&& f) {
  if constexpr (N - I <= 20) sfor<I, N>(f);
  else { sfor<I, I + 20>(f); sfor2<I + 20, N>(f); }
}
#define IC(n) std::integral_constant<int, (n)>{}
__device__ __forceinline__ unsigned pk2(float a, float b) {
  f32x2 t = {a, b};
  return __builtin_bit_cast(unsigned, __builtin_convertvector(t, bf2));
}
#define MF "v_mfma_f32_16x16x32_bf16 "
__device__ __forceinline__ void mm_va(f32x4& d, v4u v, v4u a) { asm volatile(MF "%0, %1, %2, %0" : "+v"(d) : "v"(v), "a"(a)); }
__device__ __forceinline__ void mm_av(f32x4& d, v4u a, v4u v) { asm volatile(MF "%0, %1, %2, %0" : "+v"(d) : "a"(a), "v"(v)); }
__device__ __forceinline__ void mm_vv(f32x4& d, v4u a, v4u b) { asm volatile(MF "%0, %1, %2, %0" : "+v"(d) : "v"(a), "v"(b)); }
__device__ __forceinline__ void mm_va0(f32x4& d, v4u v, v4u a) { asm volatile(MF "%0, %1, %2, 0" : "=&v"(d) : "v"(v), "a"(a)); }
__device__ __forceinline__ void mm_av0(f32x4& d, v4u a, v4u v) { asm volatile(MF "%0, %1, %2, 0" : "=&v"(d) : "a"(a), "v"(v)); }
__device__ __forceinline__ void mm_vv0(f32x4& d, v4u a, v4u b) { asm volatile(MF "%0, %1, %2, 0" : "=&v"(d) : "v"(a), "v"(b)); }
__device__ __forceinline__ void mm_vac(f32x4& d, v4u v, v4u a, f32x4 c) { asm volatile(MF "%0, %1, %2, %3" : "=&v"(d) : "v"(v), "a"(a), "v"(c)); }
__device__ __forceinline__ void mm_avc(f32x4& d, v4u a, v4u v, f32x4 c) { asm volatile(MF "%0, %1, %2, %3" : "=&v"(d) : "a"(a), "v"(v), "v"(c)); }
__device__ __forceinline__ void mm_vvc(f32x4& d, v4u a, v4u b, f32x4 c) { asm volatile(MF "%0, %1, %2, %3" : "=&v"(d) : "v"(a), "v"(b), "v"(c)); }
#define NOPS(n) asm volatile("s_nop " #n)
#define PIN(x) asm volatile("" : "+v"(x))

template <int IMM> __device__ __forceinline__ void dma16(const char* sbase, unsigned voff, unsigned ldsbase) {
  asm volatile("s_add_u32 m0, %2, %3\n\ts_nop 0\n\tglobal_load_lds_dwordx4 %0, %1" ::"v"(voff), "s"(sbase), "s"(ldsbase), "i"(IMM) : "memory", "scc");
}
template <int IMM> __device__ __forceinline__ void dma_m0(unsigned ldsbase) { asm volatile("s_add_u32 m0, %0, %1" ::"s"(ldsbase), "i"(IMM) : "memory", "scc"); }
template <int OFF> __device__ __forceinline__ void dma_go(const char* sbase, unsigned voff) {
  asm volatile("global_load_lds_dwordx4 %0, %1 offset:%2" ::"v"(voff), "s"(sbase), "i"(OFF) : "memory");
}
template <int OFF> __device__ __forceinline__ void dma_sc1(const char* sbase, unsigned voff) {
  asm volatile("global_load_lds_dwordx4 %0, %1 offset:%2 sc1" ::"v"(voff), "s"(sbase), "i"(OFF) : "memory");
}
__device__ __forceinline__ void touch(const char* sbase, unsigned voff) {
  asm volatile("s_mov_b32 m0, %2\n\ts_nop 0\n\tglobal_load_lds_dword %0, %1" ::"v"(voff), "s"(sbase), "i"(L_AUX + 2048) : "memory");
}
template <int N> __device__ __forceinline__ void wait_vm() { asm volatile("s_waitcnt vmcnt(%0)" ::"i"(N) : "memory"); }
template <int N> __device__ __forceinline__ void wait_vm_p(u64& v) { asm volatile("s_waitcnt vmcnt(%1)" : "+v"(v) : "i"(N) : "memory"); }
__device__ __forceinline__ void wait_lgkm() { asm volatile("s_waitcnt lgkmcnt(0)" ::: "memory"); }
__device__ __forceinline__ v2u trread(LAS char* p) {
  return __builtin_bit_cast(v2u, __builtin_amdgcn_ds_read_tr16_b64_v4i16((LAS s16x4*)p));
}
#define SB0() __builtin_amdgcn_sched_barrier(0)
#define BAR() __builtin_amdgcn_s_barrier()
#define LD128S(voff, soff) __builtin_amdgcn_raw_buffer_load_b128(rs, (voff), (soff), SC1)
#define ST128(v, off) __builtin_amdgcn_raw_buffer_store_b128((v), rs, (off), 0, SC1)

__device__ __forceinline__ u64 ld_flag(char* ws, unsigned off) {
  return __hip_atomic_load((GAS u64*)(ws + off), __ATOMIC_RELAXED, __HIP_MEMORY_SCOPE_AGENT);
}
__device__ __forceinline__ void poll_async(u64& v, char* ws, unsigned off) {
  asm volatile("global_load_dwordx2 %0, %1, %2 sc1" : "+v"(v) : "v"(off), "s"(ws) : "memory");
}
__device__ __forceinline__ void settle_flags(char* ws, unsigned off, u64 tag, u64 v) {
  if (!__all(v == tag)) {
    unsigned spins = 0;
    bool ok;
#pragma unroll 1
    do { __builtin_amdgcn_s_sleep(SLP); ok = __all(ld_flag(ws, off) == tag); } while (!ok && ++spins < SPIN_CAP);
  }
  __builtin_amdgcn_fence(__ATOMIC_ACQUIRE, "wavefront");
}
template <class T> __device__ __forceinline__ T sel4(int i, T a, T b, T c, T d) {
  const bool lo = (i & 1) != 0, hi = (i & 2) != 0;
  const T x = lo ? b : a, y = lo ? d : c;
  return hi ? y : x;
}
__device__ __forceinline__ float asf(unsigned u) { return __builtin_bit_cast(float, u); }
__device__ __forceinline__ unsigned asu(float f) { return __builtin_bit_cast(unsigned, f); }
#define vmax(a, b) __builtin_amdgcn_fmed3f((a), (b), vinf)
__device__ __forceinline__ float vmax3(float a, float b, float c) { float r; asm("v_max3_f32 %0, %1, %2, %3" : "=v"(r) : "v"(a), "v"(b), "v"(c)); return r; }
constexpr float EPS = 1e-6f, LOG2E = 1.4426950408889634f;
__device__ __forceinline__ float phi_a(float t) { return __builtin_fmaf(t, LOG2E, -EPS * LOG2E); }
__device__ __forceinline__ float phi_b(float u) { return __builtin_amdgcn_fmed3f(__builtin_amdgcn_exp2f(u), 0.f, 1.f); }
#define phi_c(t) vmax((t), EPS)

constexpr int st_ch(int s) { return s >= 10 ? 2 : (s == 1 || s == 5 || s >= 7) ? 1 : 0; }
constexpr int st_kb(int s) { return s >= 10 ? s - 10 : s >= 7 ? s - 5 : s == 6 ? 4 : s == 5 ? 1 : s >= 2 ? s - 1 : 0; }
constexpr bool fr_tm(int f) { return f / 20 == 6 || f / 20 == 9; }
constexpr int fr_k(int f) { return f >= 240 ? 8 + (f - 240) % 12 : fr_tm(f) ? 16 + f % 4 : 4 * st_kb(f / 20) + (f % 20) / 5; }
constexpr int fr_j(int f) { return f >= 240 ? ((f - 240) / 12 + 4) % 5 : fr_tm(f) ? ((f % 20) / 4 + 4) % 5 : f % 5; }
constexpr int fr_s(int f) { return f >= 240 ? 10 + fr_k(f) / 4 : f / 20; }
constexpr int AHEAD = 8, RING = 12;

struct Lane {
  unsigned woff[5], xo[8];
  const char *Wb, *xb;
  unsigned lw, lx;
  int rot;
  int idx, xcd;
};
__device__ __forceinline__ int rot5(int rot, int pos) { const int k = pos + rot; return k >= 5 ? k - 5 : k; }
template <int S> __device__ __forceinline__ const char* w_src(const Lane& q) {
  return q.Wb + (st_ch(S) == 0 ? 1280 : st_ch(S) == 1 ? 2560 : 0) + (size_t)rot5(q.rot, st_kb(S)) * (128 * 3840);
}
template <int S, int I> __device__ __forceinline__ void w_m0(const Lane& q) { if constexpr ((I & 3) == 0) dma_m0<slot_base(S) + I * 1024>(q.lw); }
template <int S, int I> __device__ __forceinline__ void w_go(const Lane& q) { dma_go<(I & 3) * 1024>(w_src<S>(q), q.woff[I]); }
template <int S, int I> __device__ __forceinline__ void x_m0(const Lane& q) { if constexpr ((I & 3) == 0) dma_m0<L_XST + (S & 1) * 8192 + I * 1024>(q.lx); }
template <int S, int I> __device__ __forceinline__ void x_go(const Lane& q) { dma_go<(I & 3) * 1024>(q.xb + rot5(q.rot, S) * 256, q.xo[I]); }
template <int S, int I> __device__ __forceinline__ void load_w(const Lane& q) { w_m0<S, I>(q); if constexpr ((I & 3) == 0) NOPS(0); w_go<S, I>(q); }
template <int S, int I> __device__ __forceinline__ void load_x(const Lane& q) { x_m0<S, I>(q); if constexpr ((I & 3) == 0) NOPS(0); x_go<S, I>(q); }
__device__ __forceinline__ unsigned fo_of(int jt, int w, int lane, int br) {
  return W_FO + (((jt >> 1) * 16 + br * 8 + 2 * w + (lane & 1)) * 4 + 2 * (jt & 1) + ((lane >> 1) & 1)) * 128;
}
struct OAddr { unsigned vo[5]; int so[4]; };
__device__ __forceinline__ void o_addr(OAddr& o, int jt, int w, int rj, int lane) {
#pragma unroll
  for (int i = 0; i < 5; ++i) { o.vo[i] = lane * 16 + rot5(rj, i) * 1024; PIN(o.vo[i]); }
#pragma unroll
  for (int a = 0; a < 4; ++a) { o.so[a] = W_OB + ((4 * jt + ((a + w + 1) & 3)) * 40 + 20 + 5 * w) * 1024; asm volatile("" : "+s"(o.so[a])); }
}
constexpr int wo_hb(int z) { return z / 5 < 6 ? ((z / 5) & 1) : (z / 5 < 8 ? 0 : 1); }
constexpr int wo_i(int z) { return z / 5 < 6 ? (z / 10) : (z / 5 < 8 ? z / 5 - 3 : z / 5 - 5); }
constexpr int wo_idx(int z) { return wo_hb(z) * 25 + wo_i(z) * 5 + z % 5; }

template <bool ISB>
__device__ __forceinline__ void phase1(const Params& p, const Lane& q, LAS char* L, __amdgpu_buffer_rsrc_t rs, int b, int rt, int hg, int w, int lane, int c2,
                                       int jt2, int rj, u64& tag, OAddr& oa, v4u (&of0)[4][5], v4u (&wo)[50]) {
  const int r = lane & 15, g = lane >> 4;
  const int cg = (ISB ? 8 : 0) + hg;
  const unsigned lw = q.lw;
  auto lbase = [&](int off) { unsigned a = (unsigned)(uintptr_t)L + (unsigned)off; PIN(a); return (LAS char*)(uintptr_t)a; };
  const int o1 = (lane >> 2) * 160 + (lane & 3) * 40, o2 = (lane >> 2) * 160 + (lane & 3) * 8;
  LAS char* a1 = lbase(o1);
  LAS char* a2 = lbase(o2);
  LAS char* a1h = lbase(o1 + 3 * SLOT);
  LAS char* a2h = lbase(o2 + 3 * SLOT);
  LAS char* ax0 = lbase(L_XST + w * 16384 + r * 128 + ((g ^ (r >> 1)) * 16));
  LAS char* ax1 = lbase(L_XST + w * 16384 + r * 128 + (((4 + g) ^ (r >> 1)) * 16));
  LAS char* ag = L + L_AUX + g * 16;
  LAS char *a1s, *a2s;
  const int unit = b + 256 * w;
  unsigned lane16 = lane * 16, fl0 = lane < 8 ? lane * 16u : OOB;
  PIN(lane16); PIN(fl0);

  v4u xf[20][2], wf[RING], gf2[2][4];
  f32x4 acc[3][2][5], gacc[2];
  f32x4 eps4 = {EPS, EPS, EPS, EPS};
  PIN(eps4);
  float vinf = __builtin_inff();
  PIN(vinf);

  auto readW = [&](auto F, auto T) {
    constexpr int f = F, t = T, ks = fr_k(f), jt = fr_j(f), sl = slot_of(fr_s(f));
    constexpr int off = (ks % 4) * UNIT + t * 2560 + (jt == 4 ? 32 : 40 * jt);
    v2u v;
    if constexpr (sl == 4) v = trread((jt == 4 ? a1s : a2s) + off);
    else if constexpr (sl == 3) v = trread((jt == 4 ? a1h : a2h) + off);
    else v = trread((jt == 4 ? a1 : a2) + sl * SLOT + off);
    if constexpr (t == 0) { wf[f % RING].x = v.x; wf[f % RING].y = v.y; }
    else { wf[f % RING].z = v.x; wf[f % RING].w = v.y; }
  };
  auto readX = [&](auto S, auto U) {
    constexpr int s = S, u = U, j = u / 3, k = u % 3;
    if constexpr (k < 2) xf[4 * s + j][k] = *(LAS v4u*)(((j & 1) ? ax1 : ax0) + (s & 1) * 8192 + (j >> 1) * 4096 + k * 2048);
    else gf2[s & 1][j] = *(LAS v4u*)(ag + rot5(q.rot, s) * 256 + j * 64);
  };

  v2u ept;
  v4u tag4;
  u64 poll = 0;
  unsigned poll0_off, poll1_off, st_off, o_st, o_st1, o_st2, foB;
  LAS char *aW, *aR;
  const int rp = rt > 0 ? rt - 1 : 0;
  {
    const int li = lane < 10 ? lane : 0;
    const int pu = c2 * 40 + (li / 5) * 20 + 5 * w + li % 5;
    poll0_off = W_FE + ((pu & 255) * 4 + (pu >> 8)) * 128;
    poll1_off = ISB ? W_F1 + ((cg * 16 + rp) * 4 + 3) * 128 : W_F1 + ((cg * 16 + r) * 4 + w) * 128;
    PIN(poll0_off); PIN(poll1_off);
  }
  const char* halo_src = p.ws + W_HALO + (hg * 16 + rp) * HALO_STR;
  const int wo_b = W_WOP + (c2 * 40 + 5 * w) * 5 * 1024;
  auto wo_soff = [&](int z) { return wo_b + (wo_hb(z) ? 0 : 102400) + rot5(rj, wo_i(z)) * 5120 + ((z % 5) >> 2) * 4096; };
  auto ld_wo = [&](auto Z) { wo[wo_idx(Z)] = LD128S(lane16 + ((Z % 5) & 3) * 1024, wo_soff(Z)); };
  auto flag = [&](int off) { __builtin_amdgcn_raw_buffer_store_b128(tag4, rs, fl0, off, SC1); };
  constexpr int NWO1 = 40;
  float gate[2];
  v4u Kf[5], Vf[5], Ej[5], K4m[4], V4m, pst, gl[16];
  v2u Pq[2][5];
  f32x4 kvT, kvU, kvW, kvX, kvZ, rd[8];
  float ta[2], tb[2], tc[2];
  constexpr int KVS = L_SCR + 32768, KZERO = KVS + 4096;
  LAS v2u *kvs, *kvw, *kvz, *aU[4], *aWt[4], *aX[4], *aZ[4];
  unsigned sel0, sel1;
  v4u kx[5], kpv[5], vx5[5], Vb[2][5], Kc[2], Kp[2], Qf[2], Pb[2][2], V4b[2], ones4;
  v2u q4[2];
  f32x4 biasC, biasP, Sc[2], Sp[2];
  float sm[2][4], mx[2], mt;
  unsigned mC[2], mP[2], pp[2][2];
  f32x4 num[2], den[2], num4[2];
  float inv[2][4];
  v4u F[2][2];

  auto fin_op = [&](auto H, auto Z) {
    constexpr int h = H, z = Z;
    if constexpr (z < 2) { inv[z][h] = __builtin_amdgcn_rcpf(den[z][0]); PIN(inv[z][h]); PIN(den[z]); }
    else if constexpr (z < 4) { inv[z - 2][h] *= gate[z - 2]; PIN(inv[z - 2][h]); }
    else if constexpr (z < 12) { constexpr int t2 = (z - 4) >> 2, e = (z - 4) & 3; float u = num[t2][e] * inv[t2][h]; PIN(u); num[t2][e] = u; }
    else { constexpr int t2 = (z - 12) >> 1, e = (z - 12) & 1; unsigned o = pk2(num[t2][2 * e], num[t2][2 * e + 1]); PIN(o); F[t2][h >> 1][2 * (h & 1) + e] = o; }
  };
  constexpr float CS = 0.22360679774997896f * LOG2E;
  auto att_op = [&](auto H, auto Z) {
    constexpr int h = H, z = Z;
    if constexpr (z < 4) { constexpr int t2 = z >> 1, e = z & 1; unsigned o = pk2(acc[2][t2][h][2 * e], acc[2][t2][h][2 * e + 1]); PIN(o); Qf[t2][e] = o; }
    else if constexpr (z == 4) { PIN(Qf[0]); PIN(Qf[1]); NOPS(1); }
    else if constexpr (z < 9) { constexpr int t2 = (z - 5) >> 1; if constexpr ((z - 5) & 1) mm_vvc(Sp[t2], Kp[t2], Qf[t2], biasP); else mm_vvc(Sc[t2], Kc[t2], Qf[t2], biasC); }
    else if constexpr (z < 17) { constexpr int t2 = (z - 9) >> 2, e = (z - 9) & 3; unsigned o = (r >> 2) == h ? Vb[t2][4][e] : 0u; PIN(o); V4b[t2][e] = o; }
    else if constexpr (z < 25) { constexpr int t2 = (z - 17) >> 2, e = (z - 17) & 3; sm[t2][e] = vmax(Sc[t2][e], Sp[t2][e]); PIN(sm[t2][e]); }
    else if constexpr (z < 27) { mx[z - 25] = vmax3(sm[z - 25][0], sm[z - 25][1], sm[z - 25][2]); PIN(mx[z - 25]); }
    else if constexpr (z < 29) { mx[z - 27] = vmax(mx[z - 27], sm[z - 27][3]); PIN(mx[z - 27]); }
    else if constexpr (z == 29) {
      const auto s = __builtin_amdgcn_permlane16_swap(asu(mx[0]), asu(mx[1]), false, false);
      mt = vmax(asf(s[0]), asf(s[1])); PIN(mt);
    } else if constexpr (z == 30) {
      const auto s = __builtin_amdgcn_permlane32_swap(asu(mt), asu(mt), false, false);
      mt = vmax(asf(s[0]), asf(s[1])) * -CS; PIN(mt);
    } else if constexpr (z == 31) {
      const auto s = __builtin_amdgcn_permlane16_swap(asu(mt), asu(mt), false, false);
      mx[0] = asf(s[0]); mx[1] = asf(s[1]); PIN(mx[0]); PIN(mx[1]);
    }
    else if constexpr (z < 40) { constexpr int t2 = (z - 32) >> 2, e = (z - 32) & 3; sm[t2][e] = __builtin_fmaf(sm[t2][e], CS, mx[t2]); PIN(sm[t2][e]); }
    else if constexpr (z < 48) { constexpr int t2 = (z - 40) >> 2, e = (z - 40) & 3; sm[t2][e] = __builtin_amdgcn_exp2f(sm[t2][e]); PIN(sm[t2][e]); }
    else if constexpr (z < 52) { constexpr int t2 = (z - 48) >> 1, e = (z - 48) & 1; pp[t2][e] = pk2(sm[t2][2 * e], sm[t2][2 * e + 1]); PIN(pp[t2][e]); }
    else if constexpr (z < 60) { constexpr int t2 = (z - 52) >> 2, e = (z - 52) & 3; unsigned o = pp[t2][e & 1] & (e < 2 ? mP[e & 1] : mC[e & 1]); PIN(o); Pb[h & 1][t2][e] = o; }
    else if constexpr (z == 60) { PIN(Pb[h & 1][0]); PIN(Pb[h & 1][1]); PIN(V4b[0]); PIN(V4b[1]); PIN(Vb[0][h]); PIN(Vb[1][h]); NOPS(1); }
    else if constexpr (z < 63) mm_vv0(den[z - 61], ones4, Pb[h & 1][z - 61]);
    else if constexpr (z < 65) mm_vv0(num[z - 63], Vb[z - 63][h], Pb[h & 1][z - 63]);
    else if constexpr (z < 67) { if constexpr (h == 0) mm_vv0(num4[z - 65], V4b[z - 65], Pb[h & 1][z - 65]); else mm_vv(num4[z - 65], V4b[z - 65], Pb[h & 1][z - 65]); }
  };
  auto pv_op = [&](auto H, auto U) {
    constexpr int u = U;
    if constexpr (u >= 6 && u < 10) { att_op(H, IC(9 + 2 * (u - 6))); att_op(H, IC(10 + 2 * (u - 6))); }
    if constexpr (u == 10) { att_op(H, IC(60)); att_op(H, IC(61)); }
    if constexpr (u >= 11 && u < 16) att_op(H, IC(51 + u));
    if constexpr (u >= 16) { fin_op(H, IC(2 * (u - 16))); fin_op(H, IC(2 * (u - 16) + 1)); }
  };
  auto att_pre = [&](auto H, auto Z) {
    constexpr int h = H, z = Z;
    if constexpr (z < 2) {
      Kc[z].x = kx[h][2 * z]; Kc[z].y = kx[h][2 * z + 1];
      if constexpr (h == 0) { Kc[z].z = kx[4][2 * z]; Kc[z].w = kx[4][2 * z + 1]; }
      PIN(Kc[z]);
    } else if constexpr (z < 4) {
      constexpr int t2 = z - 2;
      Kp[t2].x = kpv[h][2 * t2]; Kp[t2].y = kpv[h][2 * t2 + 1];
      if constexpr (h == 0) { Kp[t2].z = kpv[4][2 * t2]; Kp[t2].w = kpv[4][2 * t2 + 1]; }
      PIN(Kp[t2]);
    }
    else { constexpr int t2 = z - 4; unsigned lo = g == h ? q4[t2].x : 0u, hi = g == h ? q4[t2].y : 0u; PIN(lo); PIN(hi); Qf[t2].z = lo; Qf[t2].w = hi; }
  };
  auto phi_op = [&](auto Z, const f32x4& t, auto H, unsigned& out) {
    constexpr int z = Z, h = H;
    if constexpr (z < 2) { ta[z] = phi_a(t[2 * h + z]); PIN(ta[z]); }
    else if constexpr (z < 4) { tb[z - 2] = phi_b(ta[z - 2]); PIN(tb[z - 2]); }
    else if constexpr (z < 6) { tc[z - 4] = phi_c(t[2 * h + z - 4]); PIN(tc[z - 4]); }
    else if constexpr (z < 8) { tc[z - 6] += tb[z - 6]; PIN(tc[z - 6]); }
    else { out = pk2(tc[0], tc[1]); PIN(out); }
  };

  a1s = lbase(o1 + L_SLOT4); a2s = lbase(o2 + L_SLOT4);
  aW = lbase(ISB ? L_SCR + ((w + 1) & 3) * 10240 + lane * 16 : L_SCR + w * 8192 + lane * 16);
  aR = lbase(ISB ? L_SCR + w * 10240 + lane * 16 : L_SCR + w * 2048 + lane * 16);
  st_off = ISB ? W_HALO + (hg * 16 + rt) * HALO_STR + lane * 16 : W_PART + (hg * 16 + rt) * PART_STR + w * 1024 + lane * 16;
  {
    const int fb = (ISB ? 20 : 0) + (hg >> 1) * 5, TT = rt * 8 + 2 * w;
    o_st = lane16 + W_OB + (TT * 40 + fb + ((hg & 1) ? 3 : 0)) * 1024;
    o_st1 = o_st + 40960;
    o_st2 = W_OB + ((TT + (g & 1)) * 40 + fb + 2) * 1024 + ((hg & 1) * 2 + (g >> 1)) * 256 + r * 16;
    foB = fo_of(jt2, w, lane, 1);
  }
  o_addr(oa, jt2, w, rj, lane);
  PIN(st_off); PIN(o_st); PIN(o_st1); PIN(o_st2); PIN(foB);
  LAS v2u* aZ0 = nullptr;
  if constexpr (!ISB) {
#pragma unroll
    for (int j = 0; j < 5; ++j) { const unsigned one = r == j ? 0x3F803F80u : 0u; Ej[j] = v4u{one, one, one, one}; PIN(Ej[j]); }
    kvs = (LAS v2u*)lbase(KVS + lane * 8); kvw = (LAS v2u*)lbase(KVS + w * 1024 + lane * 8); kvz = (LAS v2u*)lbase(KVS + (448 + g * 16) * 8);
    aZ0 = (LAS v2u*)lbase(KZERO);
    const unsigned sv = (g == (r >> 2)) ? ((r & 1) ? 0x3F800000u : 0x00003F80u) : 0u;
    sel0 = (r & 2) ? 0u : sv; sel1 = (r & 2) ? sv : 0u;
    PIN(sel0); PIN(sel1);
  } else {
#pragma unroll
    for (int e = 0; e < 4; ++e) {
      const int key = 4 * g + e;
      biasC[e] = (key <= r && key + 7 >= r) ? 0.f : -1e30f;
      biasP[e] = (key >= r + 9 && (rt > 0 || w > 0)) ? 0.f : -1e30f;
    }
#pragma unroll
    for (int e = 0; e < 2; ++e) {
      mC[e] = (4 * g + 2 * e <= r ? 0xFFFFu : 0u) | (4 * g + 2 * e + 1 <= r ? 0xFFFF0000u : 0u);
      mP[e] = ~mC[e];
      PIN(mC[e]); PIN(mP[e]);
    }
    ones4 = v4u{0x3F803F80u, 0x3F803F80u, 0x3F803F80u, 0x3F803F80u};
    PIN(ones4); PIN(biasC); PIN(biasP);
  }
  LAS char* apk = lbase(o1 + L_PACK + (w & 1) * UNIT);
  unsigned pk_dst = lane16 + W_WOP + unit * 5120;
  PIN(pk_dst);
  const char* Wg = (ISB ? p.wB : p.wA) + ((q.xcd >> 1) & 1) * 640;
  unsigned tw;
  { const int ln = 25 * (q.idx * 4 + w) + (lane < 25 ? lane : 0); tw = (unsigned)((ln / 5) * 3840 + (ln % 5) * 128); PIN(tw); }
  unsigned pk_off[5];
  const char* pk_src;
  {
    const int pf = unit % 40, pc = (unit / 40) & 7, pP = (pf % 20) / 5, pi = pf % 5;
    const int orow = 160 * pP + (pi == 2 ? 16 : (pi >= 3 ? 40 * pi - 40 : 40 * pi)), cgs = pi == 2 ? 40 : 4;
    pk_src = (pf >= 20 ? p.woB : p.woA) + pc * 160 + orow * 1280;
#pragma unroll
    for (int i = 0; i < 5; ++i) {
      const int c = i * 64 + lane, rho = c / 10, cc = c % 10;
      pk_off[i] = (unsigned)((cgs * ((rho >> 2) & 3) + 20 * (rho >> 4) + (rho & 3)) * 1280 + cc * 16);
      PIN(pk_off[i]);
    }
  }
  unsigned fl3 = lane < 8 ? W_FE + (b * 4 + w) * 128 : lane < 16 ? W_FO + ((rt * 16 + cg) * 4 + w) * 128 : W_F1 + (ISB ? 131072 : 0) + ((cg * 16 + rt) * 4 + w) * 128;
  fl3 = lane < 24 ? fl3 + (lane & 7) * 16 : OOB;
  PIN(fl3);
  sfor<S1PRE, 5>([&](auto I) { load_w<1, I>(q); });
  if (unit < 320) sfor<0, 5>([&](auto I) { dma16<L_PACK + I * 1024>(pk_src, pk_off[I], lw); });
  if (unit < 320) wait_vm<10>(); else wait_vm<5>();
  BAR();
  ept = *(LAS v2u*)(L + L_AUX + 3072);
  sfor<0, 3>([&](auto U) { readX(IC(0), U); });
  sfor<0, AHEAD>([&](auto F_) { readW(F_, IC(0)); readW(F_, IC(1)); });
  sfor<3, 12>([&](auto U) { readX(IC(0), U); });
  tag = (((u64)(unsigned)__builtin_amdgcn_readfirstlane(ept.y) << 32) | (unsigned)__builtin_amdgcn_readfirstlane(ept.x)) + 1ull;
  tag4 = v4u{(unsigned)tag, (unsigned)(tag >> 32), (unsigned)tag, (unsigned)(tag >> 32)};
  PIN(tag4);
  SB0();

  sfor2<0, 600>([&](auto M) {
    constexpr int m = M, f = m / 2, tt = m & 1, ks = fr_k(f), jt = fr_j(f), ch = st_ch(fr_s(f));
    constexpr int sb = (m + 16) / 40 - 1, mb = (m + 16) % 40;
    constexpr int tv = (m - 368) / 8, uv = (m - 368) % 8, jv = (tv + 4) % 5;
    constexpr int tq = (m - 504) / 24, uq = (m - 504) % 24, jq = (tq + 4) % 5;
    if constexpr (mb == 0 && sb >= 0 && sb <= 11) {
      constexpr int cntA[12] = {13, 13, 13, 9, 13, 7, 5, 6, 10, 14, 18, 6};
      constexpr int cntB[12] = {13, 13, 13, 9, 13, 7, 5, 6, 10, 14, 20, 5};
      constexpr int cnt = ISB ? cntB[sb] : cntA[sb];
      if constexpr (sb == 3) { if (unit < 320) wait_vm<cnt + 5>(); else wait_vm<cnt>(); }
      else if constexpr (ISB && (sb == 7 || sb == 8)) { if (w == 3) wait_vm<cnt + 5>(); else wait_vm<cnt>(); PIN(poll); }
      else wait_vm_p<cnt>(poll);
      BAR();
      SB0();
    }
    if constexpr (m % 8 == 0 && m > 0 && f + AHEAD <= 300) { __builtin_amdgcn_s_waitcnt(0xC07F | (8 << 8)); SB0(); }
    {
      f32x4& d = acc[ch][tt][jt];
      const v4u& wv = wf[f % RING];
      if constexpr (ch == 2 || (ch == 0 && ISB)) {
        if constexpr (ks != 0) mm_va(d, wv, xf[ks][tt]);
        else if constexpr (ISB) mm_va0(d, wv, xf[ks][tt]);
        else mm_vac(d, wv, xf[ks][tt], eps4);
      } else {
        if constexpr (ks != 0) mm_av(d, xf[ks][tt], wv);
        else if constexpr (ch == 0) mm_avc(d, xf[ks][tt], wv, eps4);
        else mm_av0(d, xf[ks][tt], wv);
      }
    }
    SB0();
    if constexpr (f + AHEAD < 300) readW(IC(f + AHEAD < 300 ? f + AHEAD : 0), IC(tt));
    if constexpr (((sb >= 1 && sb <= 3) || sb == 5) && mb < 12) readX(IC(sb == 5 ? 4 : sb), IC(mb));
    SB0();
    if constexpr (ch == 0 && m % 10 == 9) {
      constexpr int kg = 4 * st_kb(m / 40) + (m % 40) / 10;
      if constexpr (kg == 0) { mm_va0(gacc[0], gf2[0][0], xf[0][0]); mm_va0(gacc[1], gf2[0][0], xf[0][1]); }
      else { mm_va(gacc[0], gf2[(kg >> 2) & 1][kg & 3], xf[kg][0]); mm_va(gacc[1], gf2[(kg >> 2) & 1][kg & 3], xf[kg][1]); }
    }
    auto split_w = [&](auto S, auto P0, auto ST) {
      constexpr int p0 = P0, st = ST;
      if constexpr (mb >= p0 && (mb - p0) / st < 5) {
        if constexpr ((mb - p0) % st == 0) w_m0<S, (mb - p0) / st>(q);
        if constexpr ((mb - p0) % st == 1) w_go<S, (mb - p0) / st>(q);
      }
    };
    auto split_x = [&](auto XS, auto I0, auto N, auto P0, auto ST) {
      constexpr int p0 = P0, st = ST, xs = XS;
      if constexpr (mb >= p0 && (mb - p0) / st < N) {
        constexpr int i = I0 + (mb - p0) / st;
        if constexpr ((mb - p0) % st == 0) x_m0<xs, i>(q);
        if constexpr ((mb - p0) % st == 1) x_go<xs, i>(q);
      }
    };
    if constexpr (m >= X1M && m < X1M + 8) load_x<1, m - X1M>(q);
    if constexpr (m >= 19 && m < 24) load_w<2, m - 19>(q);
    if constexpr (sb == 0) { split_x(IC(2), IC(0), IC(8), IC(1), IC(3)); split_w(IC(3), IC(25), IC(3)); }
    if constexpr (sb == 1) { split_w(IC(4), IC(2), IC(4)); split_x(IC(3), IC(0), IC(7), IC(25), IC(2)); if constexpr (mb == 39) load_x<3, 7>(q); }
    if constexpr (sb == 2 || sb == 3) { split_w(IC(sb + 3), IC(2), IC(4)); split_x(IC(4), IC((sb - 2) * 4), IC(4), IC(25), IC(4)); }
    if constexpr (sb >= 4 && sb <= 6) split_w(IC(sb + 3), IC(2), IC(8));
    if constexpr (sb == 7) { split_w(IC(10), IC(2), IC(4)); split_w(IC(11), IC(21), IC(4)); }
    if constexpr (sb == 8 || sb == 9) split_w(IC(sb + 4), IC(2), IC(8));
    if constexpr (sb == 10) split_w(IC(14), IC(1), IC(3));
    if constexpr (m == TQM) touch(Wg, tw);
    if constexpr (m == PACKM) {
      if (unit < 320) {
        v4u pv[5];
        sfor<0, 5>([&](auto J) {
          const v2u lo = trread(apk + 8 * J), hi = trread(apk + 2560 + 8 * J);
          pv[J] = v4u{lo.x, lo.y, hi.x, hi.y};
        });
        sfor<0, 5>([&](auto J) { ST128(pv[J], pk_dst + J * 1024); });
      }
    }
    if constexpr (m == 185) {
      const unsigned x = lane < 8 ? 0u : ~0u;
      ST128((tag4 ^ v4u{x, x, x, x}), fl3);
    }
    if constexpr (m == 265) poll_async(poll, p.ws, poll0_off);
    if constexpr (m == 345) settle_flags(p.ws, poll0_off, tag, poll);
    if constexpr (m == 286) { PIN(gacc[0]); PIN(gacc[1]); }
    if constexpr (m == 483 || m == 487) {
      constexpr int t2 = (m - 483) / 4;
      gate[t2] = __builtin_amdgcn_rcpf(1.f + __builtin_amdgcn_exp2f(gacc[t2][0] * (ISB ? LOG2E : -LOG2E)));
      PIN(gate[t2]);
    }
    if constexpr (sb >= 8 && sb <= 9 && mb % 8 == 6 && mb < 32) ld_wo(IC((sb - 8) * 4 + mb / 8));
    if constexpr (m >= 445 && (m - 445) % 4 == 0 && 8 + (m - 445) / 4 < NWO1) ld_wo(IC(8 + (m - 445) / 4));

    if constexpr (!ISB) {
      if constexpr (m >= 252 && m < 387) {
        sfor<(m - 252) * 4 / 3, (m - 251) * 4 / 3>([&](auto Z) {
          constexpr int z = Z, kt = (z / 36 + 4) % 5, pr = z % 36 / 9, t2 = pr >> 1, h = pr & 1;
          unsigned o = 0;
          phi_op(IC(z % 9), acc[0][t2][kt], IC(h), o);
          if constexpr (z % 9 == 8) Kf[kt][2 * t2 + h] = o;
          if constexpr (z % 36 == 35) PIN(Kf[kt]);
        });
      }
      if constexpr (m >= 281 && m <= 389 && (m - 281) % 27 == 0) {
        constexpr int kt = ((m - 281) / 27 + 4) % 5;
        if constexpr (m == 281) mm_vv0(kvZ, Kf[kt], Ej[kt]); else mm_vv(kvZ, Kf[kt], Ej[kt]);
      }
      if constexpr (m >= 282 && m < 362 && (m - 282) % 5 == 0) {
        constexpr int z = (m - 282) / 5;
        unsigned o = (r >> 2) == (z >> 2) ? Kf[4][z & 3] : 0u;
        PIN(o);
        K4m[z >> 2][z & 3] = o;
      }
      LAS f32x4* red = (LAS f32x4*)aW;
      if constexpr (m >= 368 && tv < 5) {
        if constexpr (uv == 3 || uv == 4) {
          constexpr int t2 = uv - 3;
          unsigned lo = pk2(acc[1][t2][jv][0], acc[1][t2][jv][1]), hi = pk2(acc[1][t2][jv][2], acc[1][t2][jv][3]);
          PIN(lo); PIN(hi);
          Vf[jv][2 * t2] = lo; Vf[jv][2 * t2 + 1] = hi;
        }
        if constexpr (uv == 5) { PIN(Vf[jv]); if constexpr (tv > 0) PIN(K4m[jv & 3]); }
        if constexpr (tv == 0) { if constexpr (uv == 6) mm_vv0(kvX, Kf[4], Vf[4]); }
        else {
          if constexpr (uv < 2) {
            unsigned lo = (r >> 2) == jv ? Vf[4][2 * uv] : 0u, hi = (r >> 2) == jv ? Vf[4][2 * uv + 1] : 0u;
            PIN(lo); PIN(hi);
            V4m[2 * uv] = lo; V4m[2 * uv + 1] = hi;
          }
          if constexpr (uv == 2) { PIN(V4m); if constexpr (tv == 1) red[6 * 64] = kvX; else red[(jv - 1) * 64] = kvT; }
          if constexpr (uv == 4) { if constexpr (tv == 1) mm_vv0(kvW, Kf[jv], V4m); else mm_vv(kvW, Kf[jv], V4m); }
          if constexpr (uv == 6) mm_vv0(kvT, Kf[jv], Vf[jv]);
          if constexpr (uv == 7) { if constexpr (tv == 1) mm_vv0(kvU, K4m[jv & 3], Vf[jv]); else mm_vv(kvU, K4m[jv & 3], Vf[jv]); }
        }
      }
      if constexpr (m == 397) red[7 * 64] = kvZ;
      if constexpr (m == 409) red[5 * 64] = kvW;
      if constexpr (m == 412) red[3 * 64] = kvT;
      if constexpr (m == 415) red[4 * 64] = kvU;
      if constexpr (m == 423) asm volatile("s_waitcnt lgkmcnt(8)" ::: "memory");
      if constexpr (m >= 424 && m < 432) rd[m - 424] = *(LAS f32x4*)(aR + ((m - 424) >> 1) * 8192 + ((m - 424) & 1) * 1024);
      if constexpr (m >= 433 && m < 441) {
        sfor<0, 3>([&](auto E) {
          constexpr int z = 3 * (m - 433) + E, qq = z >> 2, e = z & 3;
          float s = rd[qq & 1][e] + rd[2 + qq][e]; PIN(s);
          rd[qq & 1][e] = s;
        });
      }
      if constexpr (m == 441 || m == 442) {
        unsigned lo = pk2(rd[m - 441][0], rd[m - 441][1]), hi = pk2(rd[m - 441][2], rd[m - 441][3]);
        PIN(lo); PIN(hi);
        pst[2 * (m - 441)] = lo; pst[2 * (m - 441) + 1] = hi;
      }
      if constexpr (m == 444) ST128(pst, st_off);
      if constexpr (m == 446) *aZ0 = v2u{0u, 0u};
      if constexpr (m >= 448 && m < 480 && m % 2 == 0) {
        constexpr int h = (m - 448) >> 3, k = ((m - 448) >> 1) & 3;
        const bool gh = g == h, rh = (r >> 2) == h;
        auto pick = [&](bool c, LAS v2u* a) { unsigned v = (unsigned)(uintptr_t)(c ? a : aZ0); PIN(v); return (LAS v2u*)(uintptr_t)v; };
        if constexpr (k == 0) aU[h] = pick(gh, kvs + 256);
        if constexpr (k == 1) aWt[h] = pick(rh, kvs + 320);
        if constexpr (k == 2) aX[h] = pick(rh && gh, kvs + 384);
        if constexpr (k == 3) aZ[h] = pick(gh, kvz + 4);
      }
      if constexpr (m == 494) { wait_vm<13>(); flag(W_F1 + ((cg * 16 + rt) * 4 + w) * 128); }
      if constexpr (m == GPOLL) poll = ld_flag(p.ws, poll1_off);
      if constexpr (m == GPOLL + 37) settle_flags(p.ws, poll1_off, tag, poll);
      if constexpr (m >= GPOLL + 38 && m < GPOLL + 54) gl[m - GPOLL - 38] = LD128S(lane16, W_PART + (hg * 16 + m - GPOLL - 38) * PART_STR + w * 1024);
      if constexpr (m >= 504 && uq >= 4 && uq < 22) {
        sfor<0, 2>([&](auto E) {
          constexpr int z = 2 * (uq - 4) + E, pr = z / 9, t2 = pr >> 1, h = pr & 1;
          unsigned o = 0;
          phi_op(IC(z % 9), acc[2][t2][jq], IC(h), o);
          if constexpr (z % 9 == 8) Pq[t2][jq][h] = o;
        });
      }
    } else {
      if constexpr (m >= 248 && m < 288 && (m - 248) % 8 >= 4) {
        constexpr int kt = ((m - 248) / 8 + 4) % 5, z = (m - 248) % 8 - 4, t2 = z >> 1, e = z & 1;
        unsigned o = pk2(acc[0][t2][kt][2 * e], acc[0][t2][kt][2 * e + 1]);
        PIN(o);
        kx[kt][2 * t2 + e] = o;
      }
      if constexpr (m == 302) { if (w == 3) sfor<0, 5>([&](auto J) { ST128(kx[J], st_off + J * 1024); }); }
      if constexpr (m >= 348 && m < 368 && (m - 348) % 4 == 0) *(LAS v4u*)(aW + (m - 348) / 4 * 1024) = kx[(m - 348) / 4];
      if constexpr (m >= 368 && tv < 5) {
        if constexpr (uv == 3 || uv == 4) {
          constexpr int t2 = uv - 3;
          unsigned lo = pk2(acc[1][t2][jv][0], acc[1][t2][jv][1]), hi = pk2(acc[1][t2][jv][2], acc[1][t2][jv][3]);
          PIN(lo); PIN(hi);
          vx5[jv][2 * t2] = lo; vx5[jv][2 * t2 + 1] = hi;
        }
        if constexpr (uv == 6) *(LAS v4u*)(aW + (5 + jv) * 1024) = vx5[jv];
      }
      if constexpr (m == 391) { if (w == 3) { ST128(vx5[4], st_off + 9 * 1024); ST128(vx5[0], st_off + 5 * 1024); ST128(vx5[1], st_off + 6 * 1024); } }
      if constexpr (m == 406) { if (w == 3) { ST128(vx5[2], st_off + 7 * 1024); ST128(vx5[3], st_off + 8 * 1024); } }
      if constexpr (m >= 410 && m < 420) { constexpr int j = (m - 410) >> 1; Vb[tt][j].z = vx5[j][2 * tt]; Vb[tt][j].w = vx5[j][2 * tt + 1]; }
      auto halo_in = [&](auto R) {
        if (w == 0) {
          if constexpr (R == 1) wait_vm_p<9>(poll);
          settle_flags(p.ws + R * 131072, poll1_off, tag, poll);
          dma_m0<L_SCR + R * 5120>(0u); NOPS(0);
          sfor<0, 4>([&](auto J) { dma_sc1<J * 1024>(halo_src + R * 5120, lane16); });
          dma_m0<L_SCR + R * 5120 + 4096>(0u); NOPS(0); dma_sc1<0>(halo_src + R * 5120 + 4096, lane16);
        }
      };
      if constexpr (m == 385) flag(W_F1 + ((cg * 16 + rt) * 4 + w) * 128);
      if constexpr (m == 421) poll_async(poll, p.ws, poll1_off);
      if constexpr (m == 466) { halo_in(IC(0)); flag(W_F1 + 131072 + ((cg * 16 + rt) * 4 + w) * 128); }
      if constexpr (m == 494) poll_async(poll, p.ws + 131072, poll1_off);
      if constexpr (m == 506) wait_vm<12>();
      if constexpr (m >= 506 && m < 511) kpv[m - 506] = *(LAS v4u*)(aR + (m - 506) * 1024);
      if constexpr (m == 530) halo_in(IC(1));
      if constexpr (m == 566) wait_vm_p<9>(poll);
      if constexpr (m >= 566 && m < 576) {
        constexpr int j = ((m - 566) / 2 + 4) % 5;
        const v2u v = *(LAS v2u*)(aR + (5 + j) * 1024 + tt * 8);
        Vb[tt][j].x = v.x; Vb[tt][j].y = v.y;
      }
      if constexpr (m == 511 || m == 513) {
        constexpr int t2 = (m - 511) / 2;
        q4[t2] = v2u{pk2(acc[2][t2][4][0], acc[2][t2][4][1]), pk2(acc[2][t2][4][2], acc[2][t2][4][3])};
        PIN(q4[t2]);
      }
      if constexpr (m >= 516 && m < 528 && m % 2 == 0) att_pre(IC(0), IC((m - 516) / 2));
      if constexpr (m >= 528) {
        constexpr int h = tq - 1;
        constexpr int z0[25] = {0, 0, 0, 0, 2, 5, 7, 9, 9, 9, 9, 13, 17, 21, 22, 23, 24, 28, 32, 35, 38, 41, 46, 52, 52};
        sfor<z0[uq], z0[uq + 1]>([&](auto Z) { att_op(IC(h), IC(Z < 9 ? Z : Z + 8)); });
        if constexpr (uq >= 7 && uq < 10) { att_pre(IC(h + 1), IC(2 * (uq - 7))); att_pre(IC(h + 1), IC(2 * (uq - 7) + 1)); }
        if constexpr (h == 2 && uq < 18) pv_op(IC(0), IC(uq + 6));
      }
    }
    SB0();
  });
  constexpr int WO_PRE = ISB ? 46 : 40;
  sfor<NWO1, WO_PRE>([&](auto Z) { ld_wo(Z); });
  NOPS(3);
  SB0();
  if constexpr (!ISB) {
    sfor<0, 36>([&](auto Z) {
      constexpr int z = Z, pr = z / 9, t2 = pr >> 1, h = pr & 1;
      unsigned o = 0;
      phi_op(IC(z % 9), acc[2][t2][3], IC(h), o);
      if constexpr (z % 9 == 8) Pq[t2][3][h] = o;
    });
    v4u Pf[2][4];
#pragma unroll
    for (int h = 0; h < 4; ++h)
#pragma unroll
      for (int tt = 0; tt < 2; ++tt) { Pf[tt][h] = v4u{Pq[tt][h].x, Pq[tt][h].y, Pq[tt][4].x, Pq[tt][4].y}; PIN(Pf[tt][h]); }
    v4u A1 = {sel0, sel1, 0u, 0u}, A2 = {0u, 0u, sel0, sel1};
    PIN(A1); PIN(A2); NOPS(1);
    SB0();
    f32x4 d1, d2;
#pragma unroll
    for (int i = 0; i < 16; ++i) {
      if (i == 0) { mm_vv0(d1, A1, gl[0]); mm_vv0(d2, A2, gl[0]); }
      else { mm_vv(d1, A1, gl[i]); mm_vv(d2, A2, gl[i]); }
    }
    NOPS(7);
    SB0();
    kvw[0] = v2u{pk2(d1[0], d1[1]), pk2(d1[2], d1[3])};
    kvw[64] = v2u{pk2(d2[0], d2[1]), pk2(d2[2], d2[3])};
    wait_lgkm();
    BAR();
    v4u Nh[4], N4[4], Zh[4];
#pragma unroll
    for (int h = 0; h < 4; ++h) { const v2u lo = kvz[h], hi = *aZ[h]; Zh[h] = v4u{lo.x, lo.y, hi.x, hi.y}; SB0(); }
#pragma unroll
    for (int h = 0; h < 4; ++h) { const v2u lo = kvs[h * 64], hi = *aU[h]; Nh[h] = v4u{lo.x, lo.y, hi.x, hi.y}; SB0(); }
#pragma unroll
    for (int h = 0; h < 4; ++h) { const v2u lo = *aWt[h], hi = *aX[h]; N4[h] = v4u{lo.x, lo.y, hi.x, hi.y}; SB0(); }
    poll = ld_flag(p.ws, foB);
    SB0();
    f32x4 nm[2][4], dn[2][4];
#pragma unroll
    for (int h = 0; h < 4; ++h)
#pragma unroll
      for (int tt = 0; tt < 2; ++tt) mm_vvc(dn[tt][h], Zh[h], Pf[tt][h], eps4);
    SB0();
#pragma unroll
    for (int h = 0; h < 4; ++h)
#pragma unroll
      for (int tt = 0; tt < 2; ++tt) {
        mm_vv0(nm[tt][h], Nh[h], Pf[tt][h]);
        inv[tt][h] = __builtin_amdgcn_rcpf(dn[tt][h][0]);
        PIN(inv[tt][h]); PIN(dn[tt][h]);
        SB0();
      }
#pragma unroll
    for (int h = 0; h < 4; ++h)
#pragma unroll
      for (int tt = 0; tt < 2; ++tt) {
        if (h == 0) mm_vv0(num4[tt], N4[h], Pf[tt][h]); else mm_vv(num4[tt], N4[h], Pf[tt][h]);
        inv[tt][h] *= gate[tt];
        PIN(inv[tt][h]);
        SB0();
      }
#pragma unroll
    for (int hp = 0; hp < 2; ++hp)
#pragma unroll
      for (int tt = 0; tt < 2; ++tt) {
        const f32x4 oa = nm[tt][2 * hp] * inv[tt][2 * hp], ob = nm[tt][2 * hp + 1] * inv[tt][2 * hp + 1];
        F[tt][hp] = v4u{pk2(oa[0], oa[1]), pk2(oa[2], oa[3]), pk2(ob[0], ob[1]), pk2(ob[2], ob[3])};
        ST128(F[tt][hp], (tt ? o_st1 : o_st) + hp * 1024);
        SB0();
      }
  } else {
    sfor<0, 9>([&](auto Z) { att_op(IC(3), Z); });
    SB0();
    sfor<9, 17>([&](auto Z) { att_op(IC(1), Z); });
    PIN(biasC); PIN(biasP);
    sfor<60, 67>([&](auto Z) { att_op(IC(1), Z); });
    SB0();
    sfor<17, 32>([&](auto Z) { att_op(IC(3), Z); });
    sfor<0, 16>([&](auto Z) { fin_op(IC(1), Z); });
    ST128(F[0][0], o_st); ST128(F[1][0], o_st1);
    sfor<9, 17>([&](auto Z) { att_op(IC(2), Z); });
    sfor<60, 67>([&](auto Z) { att_op(IC(2), Z); });
    SB0();
    sfor<32, 60>([&](auto Z) { att_op(IC(3), Z); });
    sfor<0, 16>([&](auto Z) { fin_op(IC(2), Z); });
    sfor<9, 17>([&](auto Z) { att_op(IC(3), Z); });
    sfor<60, 67>([&](auto Z) { att_op(IC(3), Z); });
    NOPS(3);
    SB0();
    sfor<0, 16>([&](auto Z) { fin_op(IC(3), Z); });
    ST128(F[0][1], o_st + 1024); ST128(F[1][1], o_st1 + 1024);
    SB0();
  }
  {
    v2u F2[2];
#pragma unroll
    for (int tt = 0; tt < 2; ++tt) {
      const f32x4 o4 = num4[tt] * sel4(g, inv[tt][0], inv[tt][1], inv[tt][2], inv[tt][3]);
      F2[tt] = v2u{pk2(o4[0], o4[1]), pk2(o4[2], o4[3])};
    }
    const auto sx = __builtin_amdgcn_permlane16_swap(F2[0].x, F2[1].x, false, false);
    const auto sy = __builtin_amdgcn_permlane16_swap(F2[0].y, F2[1].y, false, false);
    ST128((v4u{sx[0], sy[0], sx[1], sy[1]}), o_st2);
  }
  SB0();
  auto load_o0 = [&](auto Z0, auto Z1) { sfor<Z0, Z1>([&](auto Z) { of0[Z / 5][Z % 5] = LD128S(oa.vo[Z % 5], oa.so[Z / 5]); SB0(); }); };
  if constexpr (ISB) {
    sfor<WO_PRE, 50>([&](auto Z) { ld_wo(Z); });
    SB0();
    wait_vm<50 - WO_PRE>();
    flag(W_FO + ((rt * 16 + cg) * 4 + w) * 128);
    if (b == 4) {
      const unsigned fa = W_FE + (w * 64 + lane) * 512;
      settle_flags(p.ws, fa, tag, ld_flag(p.ws, fa));
    }
    BAR();
    if (b == 4 && w == 0) flag(W_EPOCH);
    __builtin_amdgcn_s_sleep(BDLY);
    settle_flags(p.ws, foB, tag, ld_flag(p.ws, foB));
    load_o0(IC(0), IC(OPRE));
  } else {
    if (__all(poll == tag)) {
      __builtin_amdgcn_fence(__ATOMIC_ACQUIRE, "wavefront");
      load_o0(IC(0), IC(OPRE));
      wait_vm<OPRE>();
      flag(W_FO + ((rt * 16 + cg) * 4 + w) * 128);
    } else {
      poll = ld_flag(p.ws, foB);
      SB0();
      wait_vm<1>();
      flag(W_FO + ((rt * 16 + cg) * 4 + w) * 128);
      settle_flags(p.ws, foB, tag, poll);
      load_o0(IC(0), IC(OPRE));
    }
  }
  load_o0(IC(OPRE), IC(10));
  asm volatile("" ::"v"(tag4));
}

template <int CHK, bool ISB>
__device__ __forceinline__ void phase2(const Params& p, LAS char* L, __amdgpu_buffer_rsrc_t rs, int b, int w, int lane, int jt, int c, int rj, u64 tag,
                                       OAddr& oa, v4u (&of0)[4][5], v4u (&wo)[50]) {
  const int r = lane & 15, g = lane >> 4;
  v4u of1[4][5];
  f32x4 acc[4][5];
  unsigned lane16 = lane * 16;
  PIN(lane16);
  const unsigned foA = fo_of(jt, w, lane, 0);
  u64 pollA = 0;
  unsigned ra = (unsigned)(uintptr_t)L + lane16;
  unsigned rw[3];
#pragma unroll
  for (int a = 0; a < 3; ++a) { rw[a] = ra + ((((a + w + 1) & 3) * 3 + a) * 5) * 1024; PIN(rw[a]); }
  sfor2<0, 200>([&](auto M) {
    constexpr int m = M, hb = m / 100, a = (m % 100) / 25, i = (m % 25) / 5, n = m % 5;
    if constexpr (m == CHK) { settle_flags(p.ws, foA, tag, pollA); }
    if constexpr (hb == 0 && i == 0) mm_av0(acc[a][n], wo[n], of0[a][0]);
    else mm_av(acc[a][n], wo[hb * 25 + i * 5 + n], hb ? of1[a][i] : of0[a][i]);
    SB0();
    if constexpr (m < 20 && m % 2 == 0) of0[2 + m / 10][m / 2 % 5] = LD128S(oa.vo[m / 2 % 5], oa.so[2 + m / 10]);
    if constexpr (!ISB && m >= 26 && m < 56 && (m - 26) % 3 == 0) {
      constexpr int z = 40 + (m - 26) / 3;
      wo[wo_idx(z)] = LD128S(lane16 + ((z % 5) & 3) * 1024, W_WOP + (c * 40 + 5 * w) * 5120 + rot5(rj, wo_i(z)) * 5120 + ((z % 5) >> 2) * 4096);
    }
    if constexpr (m == CHK - 35) pollA = ld_flag(p.ws, foA);
    if constexpr (m >= CHK && m < CHK + 40 && (m - CHK) % 2 == 0) of1[(m - CHK) / 10][(m - CHK) / 2 % 5] = LD128S(oa.vo[(m - CHK) / 2 % 5], oa.so[(m - CHK) / 10] - 20480);
    if constexpr (m >= 130 && (m - 130) % 25 < 20 && (m - 130) % 25 % 4 == 0) {
      constexpr int a2 = (m - 130) / 25, n2 = (m - 130) % 25 / 4;
      *(LAS f32x4*)(uintptr_t)(rw[a2] + n2 * 1024) = acc[a2][n2];
    }
    SB0();
  });
  wait_lgkm();
  BAR();
  LAS f32x4* red = (LAS f32x4*)(uintptr_t)(ra + w * 15 * 1024);
  unsigned o[10];
#pragma unroll
  for (int n = 0; n < 5; ++n) {
    const f32x4 s = (red[n * 64] + red[(5 + n) * 64]) + (red[(10 + n) * 64] + acc[3][n]);
    o[2 * n] = pk2(s[0], s[1]);
    o[2 * n + 1] = pk2(s[2], s[3]);
  }
  const int tok = 64 * jt + 32 * (w >> 1) + 2 * r + (w & 1);
  GAS char* op = (GAS char*)p.out + (size_t)tok * 1280 + c * 160 + g * 40;
  typedef unsigned v4u8 __attribute__((ext_vector_type(4), aligned(8)));
  *(GAS v4u8*)op = v4u8{o[0], o[1], o[2], o[3]};
  *(GAS v4u8*)(op + 16) = v4u8{o[4], o[5], o[6], o[7]};
  *(GAS v2u*)(op + 32) = v2u{o[8], o[9]};
}

__global__ __launch_bounds__(256, 1) void fused_kernel(Params p) {
  extern __shared__ __attribute__((aligned(16))) char smem[];
  LAS char* L = (LAS char*)smem;
  const int b = blockIdx.x, lane = threadIdx.x & 63;
  const int w = __builtin_amdgcn_readfirstlane(threadIdx.x >> 6);
  const int xcd = b & 7, idx = b >> 3;
  const int rt = (xcd & 1) * 8 + (idx & 7), cg = (xcd >> 1) * 4 + (idx >> 3);
  const int jt = xcd * 4 + (idx & 3), c2 = idx >> 2;
  const int r = lane & 15, g = lane >> 4;
  {
    const char* pc; unsigned len;
    asm volatile("s_getpc_b64 %0\nfk_here:\n\ts_mov_b32 %1, fk_end-fk_here" : "=s"(pc), "=s"(len));
    pc = (const char*)((uintptr_t)pc & ~(uintptr_t)63);
    unsigned co = (unsigned)((idx * 4 + w) * 1024 + lane * 16);
    co = co + 16 <= len ? co : 0u;
    asm volatile("s_mov_b32 m0, %2\n\ts_nop 0\n\tglobal_load_lds_dwordx4 %0, %1" ::"v"(co), "s"(pc), "s"((unsigned)(L_AUX + 2048)) : "memory");
  }
  const __amdgpu_buffer_rsrc_t rs = __builtin_amdgcn_make_buffer_rsrc((void*)p.ws, 0, W_BYTES, 0x00020000);
  {
    const char* ab = sel4(w, p.gw, p.gw + 1024, (xcd & 1) ? p.woB : p.woA, (const char*)p.ws);
    const unsigned ao = (unsigned)((w == 1 ? r : lane) * 16);
    asm volatile("s_mov_b32 m0, %2\n\ts_nop 0\n\tglobal_load_lds_dwordx4 %0, %1 sc1" ::"v"(ao), "s"(ab), "s"((unsigned)(L_AUX + w * 1024)) : "memory");
  }
  Lane q;
  q.rot = idx % 5;
  q.lw = (unsigned)(w * UNIT);
  q.lx = (unsigned)(w * 16384);
#pragma unroll
  for (int i = 0; i < 8; ++i) {
    const int e = i & 1, tt = (i >> 1) & 1, hh = i >> 2;
    q.xo[i] = (unsigned)((16 * e + 2 * (lane >> 3) + tt) * 1280 + hh * 128 + (((lane & 7) ^ (4 * e + g)) * 16) + 4096 - (i & 3) * 1024);
  }
  q.Wb = (cg < 8 ? p.wA : p.wB) + (cg & 7) * 160 + (size_t)w * 32 * 3840 - 4096;
  q.xb = p.x + (size_t)(rt * 128 + w * 32) * 1280 - 4096;
  sfor<0, 8>([&](auto I) { load_x<0, I>(q); });
#pragma unroll
  for (int i = 0; i < 5; ++i) {
    const int c = i * 64 + lane, rho = c / 10, cc = c % 10;
    q.woff[i] = (unsigned)((8 * ((rho >> 2) & 3) + 4 * (rho >> 4) + (rho & 3)) * 3840 + cc * 16 + 4096 - (i & 3) * 1024);
  }
  sfor<0, 2>([&](auto I) { load_w<0, I>(q); });
  sfor<2, 5>([&](auto I) { load_w<0, I>(q); });
  sfor<0, S1PRE>([&](auto I) { load_w<1, I>(q); });
  q.idx = idx; q.xcd = xcd;
  u64 tag = 0;
  v4u wo[50], of0[4][5];
  OAddr oa;
  const int rj = (idx & 1) * 2;
  if (cg < 8) {
    phase1<false>(p, q, L, rs, b, rt, cg, w, lane, c2, jt, rj, tag, oa, of0, wo);
    phase2<P2CHK, false>(p, L, rs, b, w, lane, jt, c2, rj, tag, oa, of0, wo);
  } else {
    phase1<true>(p, q, L, rs, b, rt, cg - 8, w, lane, c2, jt, rj, tag, oa, of0, wo);
    phase2<P2CHKB, true>(p, L, rs, b, w, lane, jt, c2, rj, tag, oa, of0, wo);
  }
  asm volatile("fk_end:");
}

extern "C" void kernel_launch(void* const* d_in, const int* in_sizes, int n_in, void* d_out, int out_size, void* d_ws, size_t ws_size,
                              hipStream_t stream) {
  Params p;
  p.x = (const char*)d_in[0];
  p.wA = (const char*)d_in[1];
  p.woA = (const char*)d_in[2];
  p.wB = (const char*)d_in[4];
  p.woB = (const char*)d_in[5];
  p.gw = (const char*)d_in[6];
  p.out = (char*)d_out;
  p.ws = (char*)d_ws;
  (void)hipFuncSetAttribute((const void*)fused_kernel, hipFuncAttributeMaxDynamicSharedMemorySize, LDS_BYTES);
  fused_kernel<<<256, 256, LDS_BYTES, stream>>>(p);
}
```

```cpp
#include <hip/hip_runtime.h>
#include <stdint.h>
#include <type_traits>

#define LAS __attribute__((address_space(3)))
#define GAS __attribute__((address_space(1)))
typedef short s16x4 __attribute__((ext_vector_type(4)));
typedef unsigned v4u __attribute__((ext_vector_type(4)));
typedef unsigned v2u __attribute__((ext_vector_type(2)));
typedef float f32x4 __attribute__((ext_vector_type(4)));
typedef float f32x2 __attribute__((ext_vector_type(2)));
typedef __bf16 bf2 __attribute__((ext_vector_type(2)));
typedef unsigned long long u64;


constexpr int SLP = 2, BDLY = 4, GPOLL = 546, P2CHKB = 75, OPRE = 5, X1M = 11, S1PRE = 5, ROTX = 1, PK2 = 64, TQM = 184, PACKM = 105, P2CHK = 60;

constexpr int UNIT = 5120, SLOT = 4 * UNIT;
constexpr int L_XST = 4 * SLOT;
constexpr int L_SCR = L_XST;
constexpr int L_SLOT4 = L_SCR + 40960;
constexpr int L_PACK = L_XST + 65536;
constexpr int L_AUX = L_PACK + 2 * UNIT;
constexpr int LDS_BYTES = 163840;
static_assert(L_SLOT4 + SLOT <= L_PACK && L_AUX + 4096 <= LDS_BYTES, "lds map");
constexpr int slot_of(int s) { return s <= 10 ? (s & 3) : s == 11 ? 4 : (s - 9) & 3; }
constexpr int slot_base(int s) { return slot_of(s) == 4 ? L_SLOT4 : slot_of(s) * SLOT; }
constexpr int W_EPOCH = 0;
constexpr int W_FE = 131072, W_FO = 2 * 131072, W_F1 = 3 * 131072;
constexpr int W_PART = 1 << 20, PART_STR = 4096;
constexpr int W_HALO = 2 << 20, HALO_STR = 10240;
constexpr int W_WOP = 4 << 20;
constexpr int W_OB = 6 << 20;
constexpr int W_BYTES = W_OB + 128 * 40 * 1024;
constexpr int SC1 = 16;
constexpr unsigned OOB = 0x40000000u;
constexpr unsigned SPIN_CAP = 1u << 16;

struct Params {
  const char *x, *wA, *woA, *wB, *woB, *gw;
  char *out, *ws;
};

template <int I, int N, class F> __device__ __forceinline__ void sfor(F&& f) {
  if constexpr (I < N) { f(std::integral_constant<int, I>{}); sfor<I + 1, N>(f); }
}
template <int I, int N, class F> __device__ __forceinline__ void sfor2(F&& f) {
  if constexpr (N - I <= 20) sfor<I, N>(f);
  else { sfor<I, I + 20>(f); sfor2<I + 20, N>(f); }
}
#define IC(n) std::integral_constant<int, (n)>{}
__device__ __forceinline__ unsigned pk2(float a, float b) {
  f32x2 t = {a, b};
  return __builtin_bit_cast(unsigned, __builtin_convertvector(t, bf2));
}
#define MF "v_mfma_f32_16x16x32_bf16 "
__device__ __forceinline__ void mm_va(f32x4& d, v4u v, v4u a) { asm volatile(MF "%0, %1, %2, %0" : "+v"(d) : "v"(v), "a"(a)); }
__device__ __forceinline__ void mm_av(f32x4& d, v4u a, v4u v) { asm volatile(MF "%0, %1, %2, %0" : "+v"(d) : "a"(a), "v"(v)); }
__device__ __forceinline__ void mm_vv(f32x4& d, v4u a, v4u b) { asm volatile(MF "%0, %1, %2, %0" : "+v"(d) : "v"(a), "v"(b)); }
__device__ __forceinline__ void mm_va0(f32x4& d, v4u v, v4u a) { asm volatile(MF "%0, %1, %2, 0" : "=&v"(d) : "v"(v), "a"(a)); }
__device__ __forceinline__ void mm_av0(f32x4& d, v4u a, v4u v) { asm volatile(MF "%0, %1, %2, 0" : "=&v"(d) : "a"(a), "v"(v)); }
__device__ __forceinline__ void mm_vv0(f32x4& d, v4u a, v4u b) { asm volatile(MF "%0, %1, %2, 0" : "=&v"(d) : "v"(a), "v"(b)); }
__device__ __forceinline__ void mm_vac(f32x4& d, v4u v, v4u a, f32x4 c) { asm volatile(MF "%0, %1, %2, %3" : "=&v"(d) : "v"(v), "a"(a), "v"(c)); }
__device__ __forceinline__ void mm_avc(f32x4& d, v4u a, v4u v, f32x4 c) { asm volatile(MF "%0, %1, %2, %3" : "=&v"(d) : "a"(a), "v"(v), "v"(c)); }
__device__ __forceinline__ void mm_vvc(f32x4& d, v4u a, v4u b, f32x4 c) { asm volatile(MF "%0, %1, %2, %3" : "=&v"(d) : "v"(a), "v"(b), "v"(c)); }
#define NOPS(n) asm volatile("s_nop " #n)
#define PIN(x) asm volatile("" : "+v"(x))

template <int IMM> __device__ __forceinline__ void dma16(const char* sbase, unsigned voff, unsigned ldsbase) {
  asm volatile("s_add_u32 m0, %2, %3\n\ts_nop 0\n\tglobal_load_lds_dwordx4 %0, %1 nt" ::"v"(voff), "s"(sbase), "s"(ldsbase), "i"(IMM) : "memory", "scc");
}
template <int IMM> __device__ __forceinline__ void dma_m0(unsigned ldsbase) { asm volatile("s_add_u32 m0, %0, %1" ::"s"(ldsbase), "i"(IMM) : "memory", "scc"); }
template <int OFF> __device__ __forceinline__ void dma_go(const char* sbase, unsigned voff) {
  asm volatile("global_load_lds_dwordx4 %0, %1 offset:%2" ::"v"(voff), "s"(sbase), "i"(OFF) : "memory");
}
template <int OFF> __device__ __forceinline__ void dma_sc1(const char* sbase, unsigned voff) {
  asm volatile("global_load_lds_dwordx4 %0, %1 offset:%2 sc1" ::"v"(voff), "s"(sbase), "i"(OFF) : "memory");
}
__device__ __forceinline__ void touch(const char* sbase, unsigned voff) {
  asm volatile("s_mov_b32 m0, %2\n\ts_nop 0\n\tglobal_load_lds_dword %0, %1" ::"v"(voff), "s"(sbase), "i"(L_AUX + 2048) : "memory");
}
template <int N> __device__ __forceinline__ void wait_vm() { asm volatile("s_waitcnt vmcnt(%0)" ::"i"(N) : "memory"); }
template <int N> __device__ __forceinline__ void wait_vm_p(u64& v) { asm volatile("s_waitcnt vmcnt(%1)" : "+v"(v) : "i"(N) : "memory"); }
__device__ __forceinline__ void wait_lgkm() { asm volatile("s_waitcnt lgkmcnt(0)" ::: "memory"); }
__device__ __forceinline__ v2u trread(LAS char* p) {
  return __builtin_bit_cast(v2u, __builtin_amdgcn_ds_read_tr16_b64_v4i16((LAS s16x4*)p));
}
#define SB0() __builtin_amdgcn_sched_barrier(0)
#define BAR() __builtin_amdgcn_s_barrier()
#define LD128S(voff, soff) __builtin_amdgcn_raw_buffer_load_b128(rs, (voff), (soff), SC1)
#define ST128(v, off) __builtin_amdgcn_raw_buffer_store_b128((v), rs, (off), 0, SC1)

__device__ __forceinline__ u64 ld_flag(char* ws, unsigned off) {
  return __hip_atomic_load((GAS u64*)(ws + off), __ATOMIC_RELAXED, __HIP_MEMORY_SCOPE_AGENT);
}
__device__ __forceinline__ void poll_async(u64& v, char* ws, unsigned off) {
  asm volatile("global_load_dwordx2 %0, %1, %2 sc1" : "+v"(v) : "v"(off), "s"(ws) : "memory");
}
__device__ __forceinline__ void settle_flags(char* ws, unsigned off, u64 tag, u64 v) {
  if (!__all(v == tag)) {
    unsigned spins = 0;
    bool ok;
#pragma unroll 1
    do { __builtin_amdgcn_s_sleep(SLP); ok = __all(ld_flag(ws, off) == tag); } while (!ok && ++spins < SPIN_CAP);
  }
  __builtin_amdgcn_fence(__ATOMIC_ACQUIRE, "wavefront");
}
template <class T> __device__ __forceinline__ T sel4(int i, T a, T b, T c, T d) {
  const bool lo = (i & 1) != 0, hi = (i & 2) != 0;
  const T x = lo ? b : a, y = lo ? d : c;
  return hi ? y : x;
}
__device__ __forceinline__ float asf(unsigned u) { return __builtin_bit_cast(float, u); }
__device__ __forceinline__ unsigned asu(float f) { return __builtin_bit_cast(unsigned, f); }
#define vmax(a, b) __builtin_amdgcn_fmed3f((a), (b), vinf)
__device__ __forceinline__ float vmax3(float a, float b, float c) { float r; asm("v_max3_f32 %0, %1, %2, %3" : "=v"(r) : "v"(a), "v"(b), "v"(c)); return r; }
constexpr float EPS = 1e-6f, LOG2E = 1.4426950408889634f;
__device__ __forceinline__ float phi_a(float t) { return __builtin_fmaf(t, LOG2E, -EPS * LOG2E); }
__device__ __forceinline__ float phi_b(float u) { return __builtin_amdgcn_fmed3f(__builtin_amdgcn_exp2f(u), 0.f, 1.f); }
#define phi_c(t) vmax((t), EPS)

constexpr int st_ch(int s) { return s >= 10 ? 2 : (s == 1 || s == 5 || s >= 7) ? 1 : 0; }
constexpr int st_kb(int s) { return s >= 10 ? s - 10 : s >= 7 ? s - 5 : s == 6 ? 4 : s == 5 ? 1 : s >= 2 ? s - 1 : 0; }
constexpr bool fr_tm(int f) { return f / 20 == 6 || f / 20 == 9; }
constexpr int fr_k(int f) { return f >= 240 ? 8 + (f - 240) % 12 : fr_tm(f) ? 16 + f % 4 : 4 * st_kb(f / 20) + (f % 20) / 5; }
constexpr int fr_j(int f) { return f >= 240 ? ((f - 240) / 12 + 4) % 5 : fr_tm(f) ? ((f % 20) / 4 + 4) % 5 : f % 5; }
constexpr int fr_s(int f) { return f >= 240 ? 10 + fr_k(f) / 4 : f / 20; }
constexpr int AHEAD = 8, RING = 12;

struct Lane {
  unsigned woff[5], xo[8];
  const char *Wb, *xb;
  unsigned lw, lx;
  int rot;
  int idx, xcd;
};
__device__ __forceinline__ int rot5(int rot, int pos) { const int k = pos + rot; return k >= 5 ? k - 5 : k; }
template <int S> __device__ __forceinline__ const char* w_src(const Lane& q) {
  return q.Wb + (st_ch(S) == 0 ? 1280 : st_ch(S) == 1 ? 2560 : 0) + (size_t)rot5(q.rot, st_kb(S)) * (128 * 3840);
}
template <int S, int I> __device__ __forceinline__ void w_m0(const Lane& q) { if constexpr ((I & 3) == 0) dma_m0<slot_base(S) + I * 1024>(q.lw); }
template <int S, int I> __device__ __forceinline__ void w_go(const Lane& q) { dma_go<(I & 3) * 1024>(w_src<S>(q), q.woff[I]); }
template <int S, int I> __device__ __forceinline__ void x_m0(const Lane& q) { if constexpr ((I & 3) == 0) dma_m0<L_XST + (S & 1) * 8192 + I * 1024>(q.lx); }
template <int S, int I> __device__ __forceinline__ void x_go(const Lane& q) { dma_go<(I & 3) * 1024>(q.xb + rot5(q.rot, S) * 256, q.xo[I]); }
template <int S, int I> __device__ __forceinline__ void load_w(const Lane& q) { w_m0<S, I>(q); if constexpr ((I & 3) == 0) NOPS(0); w_go<S, I>(q); }
template <int S, int I> __device__ __forceinline__ void load_x(const Lane& q) { x_m0<S, I>(q); if constexpr ((I & 3) == 0) NOPS(0); x_go<S, I>(q); }
__device__ __forceinline__ unsigned fo_of(int jt, int w, int lane, int br) {
  return W_FO + (((jt >> 1) * 16 + br * 8 + 2 * w + (lane & 1)) * 4 + 2 * (jt & 1) + ((lane >> 1) & 1)) * 128;
}
struct OAddr { unsigned vo[5]; int so[4]; };
__device__ __forceinline__ void o_addr(OAddr& o, int jt, int w, int rj, int lane) {
#pragma unroll
  for (int i = 0; i < 5; ++i) { o.vo[i] = lane * 16 + rot5(rj, i) * 1024; PIN(o.vo[i]); }
#pragma unroll
  for (int a = 0; a < 4; ++a) { o.so[a] = W_OB + ((4 * jt + ((a + w + 1) & 3)) * 40 + 20 + 5 * w) * 1024; asm volatile("" : "+s"(o.so[a])); }
}
constexpr int wo_hb(int z) { return z / 5 < 6 ? ((z / 5) & 1) : (z / 5 < 8 ? 0 : 1); }
constexpr int wo_i(int z) { return z / 5 < 6 ? (z / 10) : (z / 5 < 8 ? z / 5 - 3 : z / 5 - 5); }
constexpr int wo_idx(int z) { return wo_hb(z) * 25 + wo_i(z) * 5 + z % 5; }

template <bool ISB>
__device__ __forceinline__ void phase1(const Params& p, const Lane& q, LAS char* L, __amdgpu_buffer_rsrc_t rs, int b, int rt, int hg, int w, int lane, int c2,
                                       int jt2, int rj, u64& tag, OAddr& oa, v4u (&of0)[4][5], v4u (&wo)[50]) {
  const int r = lane & 15, g = lane >> 4;
  const int cg = (ISB ? 8 : 0) + hg;
  const unsigned lw = q.lw;
  auto lbase = [&](int off) { unsigned a = (unsigned)(uintptr_t)L + (unsigned)off; PIN(a); return (LAS char*)(uintptr_t)a; };
  const int o1 = (lane >> 2) * 160 + (lane & 3) * 40, o2 = (lane >> 2) * 160 + (lane & 3) * 8;
  LAS char* a1 = lbase(o1);
  LAS char* a2 = lbase(o2);
  LAS char* a1h = lbase(o1 + 3 * SLOT);
  LAS char* a2h = lbase(o2 + 3 * SLOT);
  LAS char* ax0 = lbase(L_XST + w * 16384 + r * 128 + ((g ^ (r >> 1)) * 16));
  LAS char* ax1 = lbase(L_XST + w * 16384 + r * 128 + (((4 + g) ^ (r >> 1)) * 16));
  LAS char* ag = L + L_AUX + g * 16;
  LAS char *a1s, *a2s;
  const int unit = w == 0 ? b : ((b - PK2) & 255) + 256 * w;
  unsigned lane16 = lane * 16, fl0 = lane < 8 ? lane * 16u : OOB;
  PIN(lane16); PIN(fl0);

  v4u xf[20][2], wf[RING], gf2[2][4];
  f32x4 acc[3][2][5], gacc[2];
  f32x4 eps4 = {EPS, EPS, EPS, EPS};
  PIN(eps4);
  float vinf = __builtin_inff();
  PIN(vinf);

  auto readW = [&](auto F, auto T) {
    constexpr int f = F, t = T, ks = fr_k(f), jt = fr_j(f), sl = slot_of(fr_s(f));
    constexpr int off = (ks % 4) * UNIT + t * 2560 + (jt == 4 ? 32 : 40 * jt);
    v2u v;
    if constexpr (sl == 4) v = trread((jt == 4 ? a1s : a2s) + off);
    else if constexpr (sl == 3) v = trread((jt == 4 ? a1h : a2h) + off);
    else v = trread((jt == 4 ? a1 : a2) + sl * SLOT + off);
    if constexpr (t == 0) { wf[f % RING].x = v.x; wf[f % RING].y = v.y; }
    else { wf[f % RING].z = v.x; wf[f % RING].w = v.y; }
  };
  auto readX = [&](auto S, auto U) {
    constexpr int s = S, u = U, j = u / 3, k = u % 3;
    if constexpr (k < 2) xf[4 * s + j][k] = *(LAS v4u*)(((j & 1) ? ax1 : ax0) + (s & 1) * 8192 + (j >> 1) * 4096 + k * 2048);
    else gf2[s & 1][j] = *(LAS v4u*)(ag + rot5(q.rot, s) * 256 + j * 64);
  };

  v2u ept;
  v4u tag4;
  u64 poll = 0;
  unsigned poll0_off, poll1_off, st_off, o_st, o_st1, o_st2, foB;
  LAS char *aW, *aR;
  const int rp = rt > 0 ? rt - 1 : 0;
  {
    const int li = lane < 10 ? lane : 0;
    const int pu = c2 * 40 + (li / 5) * 20 + 5 * w + li % 5;
    poll0_off = W_FE + (pu < 256 ? pu * 4 : (pu - 256 + PK2) * 4 + 1) * 128;
    poll1_off = ISB ? W_F1 + ((cg * 16 + rp) * 4 + 3) * 128 : W_F1 + ((cg * 16 + r) * 4 + w) * 128;
    PIN(poll0_off); PIN(poll1_off);
  }
  const char* halo_src = p.ws + W_HALO + (hg * 16 + rp) * HALO_STR;
  const int wo_b = W_WOP + (c2 * 40 + 5 * w) * 5 * 1024;
  auto wo_soff = [&](int z) { return wo_b + (wo_hb(z) ? 0 : 102400) + rot5(rj, wo_i(z)) * 5120 + ((z % 5) >> 2) * 4096; };
  auto ld_wo = [&](auto Z) { wo[wo_idx(Z)] = LD128S(lane16 + ((Z % 5) & 3) * 1024, wo_soff(Z)); };
  auto flag = [&](int off) { __builtin_amdgcn_raw_buffer_store_b128(tag4, rs, fl0, off, SC1); };
  constexpr int NWO1 = 40;
  float gate[2];
  v4u Kf[5], Vf[5], Ej[5], K4m[4], V4m, pst, gl[16];
  v2u Pq[2][5];
  f32x4 kvT, kvU, kvW, kvX, kvZ, rd[8];
  float ta[2], tb[2], tc[2];
  constexpr int KVS = L_SCR + 32768, KZERO = KVS + 4096;
  LAS v2u *kvs, *kvw, *kvz, *aU[4], *aWt[4], *aX[4], *aZ[4];
  unsigned sel0, sel1;
  v4u kx[5], kpv[5], vx5[5], Vb[2][5], Kc[2], Kp[2], Qf[2], Pb[2][2], V4b[2], ones4;
  v2u q4[2];
  f32x4 biasC, biasP, Sc[2], Sp[2];
  float sm[2][4], mx[2], mt;
  unsigned mC[2], mP[2], pp[2][2];
  f32x4 num[2], den[2], num4[2];
  float inv[2][4];
  v4u F[2][2];

  auto fin_op = [&](auto H, auto Z) {
    constexpr int h = H, z = Z;
    if constexpr (z < 2) { inv[z][h] = __builtin_amdgcn_rcpf(den[z][0]); PIN(inv[z][h]); PIN(den[z]); }
    else if constexpr (z < 4) { inv[z - 2][h] *= gate[z - 2]; PIN(inv[z - 2][h]); }
    else if constexpr (z < 12) { constexpr int t2 = (z - 4) >> 2, e = (z - 4) & 3; float u = num[t2][e] * inv[t2][h]; PIN(u); num[t2][e] = u; }
    else { constexpr int t2 = (z - 12) >> 1, e = (z - 12) & 1; unsigned o = pk2(num[t2][2 * e], num[t2][2 * e + 1]); PIN(o); F[t2][h >> 1][2 * (h & 1) + e] = o; }
  };
  constexpr float CS = 0.22360679774997896f * LOG2E;
  auto att_op = [&](auto H, auto Z) {
    constexpr int h = H, z = Z;
    if constexpr (z < 4) { constexpr int t2 = z >> 1, e = z & 1; unsigned o = pk2(acc[2][t2][h][2 * e], acc[2][t2][h][2 * e + 1]); PIN(o); Qf[t2][e] = o; }
    else if constexpr (z == 4) { PIN(Qf[0]); PIN(Qf[1]); NOPS(1); }
    else if constexpr (z < 9) { constexpr int t2 = (z - 5) >> 1; if constexpr ((z - 5) & 1) mm_vvc(Sp[t2], Kp[t2], Qf[t2], biasP); else mm_vvc(Sc[t2], Kc[t2], Qf[t2], biasC); }
    else if constexpr (z < 17) { constexpr int t2 = (z - 9) >> 2, e = (z - 9) & 3; unsigned o = (r >> 2) == h ? Vb[t2][4][e] : 0u; PIN(o); V4b[t2][e] = o; }
    else if constexpr (z < 25) { constexpr int t2 = (z - 17) >> 2, e = (z - 17) & 3; sm[t2][e] = vmax(Sc[t2][e], Sp[t2][e]); PIN(sm[t2][e]); }
    else if constexpr (z < 27) { mx[z - 25] = vmax3(sm[z - 25][0], sm[z - 25][1], sm[z - 25][2]); PIN(mx[z - 25]); }
    else if constexpr (z < 29) { mx[z - 27] = vmax(mx[z - 27], sm[z - 27][3]); PIN(mx[z - 27]); }
    else if constexpr (z == 29) {
      const auto s = __builtin_amdgcn_permlane16_swap(asu(mx[0]), asu(mx[1]), false, false);
      mt = vmax(asf(s[0]), asf(s[1])); PIN(mt);
    } else if constexpr (z == 30) {
      const auto s = __builtin_amdgcn_permlane32_swap(asu(mt), asu(mt), false, false);
      mt = vmax(asf(s[0]), asf(s[1])) * -CS; PIN(mt);
    } else if constexpr (z == 31) {
      const auto s = __builtin_amdgcn_permlane16_swap(asu(mt), asu(mt), false, false);
      mx[0] = asf(s[0]); mx[1] = asf(s[1]); PIN(mx[0]); PIN(mx[1]);
    }
    else if constexpr (z < 40) { constexpr int t2 = (z - 32) >> 2, e = (z - 32) & 3; sm[t2][e] = __builtin_fmaf(sm[t2][e], CS, mx[t2]); PIN(sm[t2][e]); }
    else if constexpr (z < 48) { constexpr int t2 = (z - 40) >> 2, e = (z - 40) & 3; sm[t2][e] = __builtin_amdgcn_exp2f(sm[t2][e]); PIN(sm[t2][e]); }
    else if constexpr (z < 52) { constexpr int t2 = (z - 48) >> 1, e = (z - 48) & 1; pp[t2][e] = pk2(sm[t2][2 * e], sm[t2][2 * e + 1]); PIN(pp[t2][e]); }
    else if constexpr (z < 60) { constexpr int t2 = (z - 52) >> 2, e = (z - 52) & 3; unsigned o = pp[t2][e & 1] & (e < 2 ? mP[e & 1] : mC[e & 1]); PIN(o); Pb[h & 1][t2][e] = o; }
    else if constexpr (z == 60) { PIN(Pb[h & 1][0]); PIN(Pb[h & 1][1]); PIN(V4b[0]); PIN(V4b[1]); PIN(Vb[0][h]); PIN(Vb[1][h]); NOPS(1); }
    else if constexpr (z < 63) mm_vv0(den[z - 61], ones4, Pb[h & 1][z - 61]);
    else if constexpr (z < 65) mm_vv0(num[z - 63], Vb[z - 63][h], Pb[h & 1][z - 63]);
    else if constexpr (z < 67) { if constexpr (h == 0) mm_vv0(num4[z - 65], V4b[z - 65], Pb[h & 1][z - 65]); else mm_vv(num4[z - 65], V4b[z - 65], Pb[h & 1][z - 65]); }
  };
  auto pv_op = [&](auto H, auto U) {
    constexpr int u = U;
    if constexpr (u >= 6 && u < 10) { att_op(H, IC(9 + 2 * (u - 6))); att_op(H, IC(10 + 2 * (u - 6))); }
    if constexpr (u == 10) { att_op(H, IC(60)); att_op(H, IC(61)); }
    if constexpr (u >= 11 && u < 16) att_op(H, IC(51 + u));
    if constexpr (u >= 16) { fin_op(H, IC(2 * (u - 16))); fin_op(H, IC(2 * (u - 16) + 1)); }
  };
  auto att_pre = [&](auto H, auto Z) {
    constexpr int h = H, z = Z;
    if constexpr (z < 2) {
      Kc[z].x = kx[h][2 * z]; Kc[z].y = kx[h][2 * z + 1];
      if constexpr (h == 0) { Kc[z].z = kx[4][2 * z]; Kc[z].w = kx[4][2 * z + 1]; }
      PIN(Kc[z]);
    } else if constexpr (z < 4) {
      constexpr int t2 = z - 2;
      Kp[t2].x = kpv[h][2 * t2]; Kp[t2].y = kpv[h][2 * t2 + 1];
      if constexpr (h == 0) { Kp[t2].z = kpv[4][2 * t2]; Kp[t2].w = kpv[4][2 * t2 + 1]; }
      PIN(Kp[t2]);
    }
    else { constexpr int t2 = z - 4; unsigned lo = g == h ? q4[t2].x : 0u, hi = g == h ? q4[t2].y : 0u; PIN(lo); PIN(hi); Qf[t2].z = lo; Qf[t2].w = hi; }
  };
  auto phi_op = [&](auto Z, const f32x4& t, auto H, unsigned& out) {
    constexpr int z = Z, h = H;
    if constexpr (z < 2) { ta[z] = phi_a(t[2 * h + z]); PIN(ta[z]); }
    else if constexpr (z < 4) { tb[z - 2] = phi_b(ta[z - 2]); PIN(tb[z - 2]); }
    else if constexpr (z < 6) { tc[z - 4] = phi_c(t[2 * h + z - 4]); PIN(tc[z - 4]); }
    else if constexpr (z < 8) { tc[z - 6] += tb[z - 6]; PIN(tc[z - 6]); }
    else { out = pk2(tc[0], tc[1]); PIN(out); }
  };

  a1s = lbase(o1 + L_SLOT4); a2s = lbase(o2 + L_SLOT4);
  aW = lbase(ISB ? L_SCR + ((w + 1) & 3) * 10240 + lane * 16 : L_SCR + w * 8192 + lane * 16);
  aR = lbase(ISB ? L_SCR + w * 10240 + lane * 16 : L_SCR + w * 2048 + lane * 16);
  st_off = ISB ? W_HALO + (hg * 16 + rt) * HALO_STR + lane * 16 : W_PART + (hg * 16 + rt) * PART_STR + w * 1024 + lane * 16;
  {
    const int fb = (ISB ? 20 : 0) + (hg >> 1) * 5, TT = rt * 8 + 2 * w;
    o_st = lane16 + W_OB + (TT * 40 + fb + ((hg & 1) ? 3 : 0)) * 1024;
    o_st1 = o_st + 40960;
    o_st2 = W_OB + ((TT + (g & 1)) * 40 + fb + 2) * 1024 + ((hg & 1) * 2 + (g >> 1)) * 256 + r * 16;
    foB = fo_of(jt2, w, lane, 1);
  }
  o_addr(oa, jt2, w, rj, lane);
  PIN(st_off); PIN(o_st); PIN(o_st1); PIN(o_st2); PIN(foB);
  LAS v2u* aZ0 = nullptr;
  if constexpr (!ISB) {
#pragma unroll
    for (int j = 0; j < 5; ++j) { const unsigned one = r == j ? 0x3F803F80u : 0u; Ej[j] = v4u{one, one, one, one}; PIN(Ej[j]); }
    kvs = (LAS v2u*)lbase(KVS + lane * 8); kvw = (LAS v2u*)lbase(KVS + w * 1024 + lane * 8); kvz = (LAS v2u*)lbase(KVS + (448 + g * 16) * 8);
    aZ0 = (LAS v2u*)lbase(KZERO);
    const unsigned sv = (g == (r >> 2)) ? ((r & 1) ? 0x3F800000u : 0x00003F80u) : 0u;
    sel0 = (r & 2) ? 0u : sv; sel1 = (r & 2) ? sv : 0u;
    PIN(sel0); PIN(sel1);
  } else {
#pragma unroll
    for (int e = 0; e < 4; ++e) {
      const int key = 4 * g + e;
      biasC[e] = (key <= r && key + 7 >= r) ? 0.f : -1e30f;
      biasP[e] = (key >= r + 9 && (rt > 0 || w > 0)) ? 0.f : -1e30f;
    }
#pragma unroll
    for (int e = 0; e < 2; ++e) {
      mC[e] = (4 * g + 2 * e <= r ? 0xFFFFu : 0u) | (4 * g + 2 * e + 1 <= r ? 0xFFFF0000u : 0u);
      mP[e] = ~mC[e];
      PIN(mC[e]); PIN(mP[e]);
    }
    ones4 = v4u{0x3F803F80u, 0x3F803F80u, 0x3F803F80u, 0x3F803F80u};
    PIN(ones4); PIN(biasC); PIN(biasP);
  }
  LAS char* apk = lbase(o1 + L_PACK + (w & 1) * UNIT);
  unsigned pk_dst = lane16 + W_WOP + unit * 5120;
  PIN(pk_dst);
  const char* Wg = (ISB ? p.wB : p.wA) + ((q.xcd >> 1) & 1) * 640;
  unsigned tw;
  { const int ln = 25 * (q.idx * 4 + w) + (lane < 25 ? lane : 0); tw = (unsigned)((ln / 5) * 3840 + (ln % 5) * 128); PIN(tw); }
  unsigned pk_off[5];
  const char* pk_src;
  {
    const int pf = unit % 40, pc = (unit / 40) & 7, pP = (pf % 20) / 5, pi = pf % 5;
    const int orow = 160 * pP + (pi == 2 ? 16 : (pi >= 3 ? 40 * pi - 40 : 40 * pi)), cgs = pi == 2 ? 40 : 4;
    pk_src = (pf >= 20 ? p.woB : p.woA) + pc * 160 + orow * 1280;
#pragma unroll
    for (int i = 0; i < 5; ++i) {
      const int c = i * 64 + lane, rho = c / 10, cc = c % 10;
      pk_off[i] = (unsigned)((cgs * ((rho >> 2) & 3) + 20 * (rho >> 4) + (rho & 3)) * 1280 + cc * 16);
      PIN(pk_off[i]);
    }
  }
  unsigned fl3 = lane < 8 ? W_FE + (b * 4 + w) * 128 : lane < 16 ? W_FO + ((rt * 16 + cg) * 4 + w) * 128 : W_F1 + (ISB ? 131072 : 0) + ((cg * 16 + rt) * 4 + w) * 128;
  fl3 = lane < 24 ? fl3 + (lane & 7) * 16 : OOB;
  PIN(fl3);
  sfor<S1PRE, 5>([&](auto I) { load_w<1, I>(q); });
  if (unit < 320) sfor<0, 5>([&](auto I) { dma16<L_PACK + I * 1024>(pk_src, pk_off[I], lw); });
  if (unit < 320) wait_vm<10>(); else wait_vm<5>();
  BAR();
  ept = *(LAS v2u*)(L + L_AUX + 3072);
  sfor<0, 3>([&](auto U) { readX(IC(0), U); });
  sfor<0, AHEAD>([&](auto F_) { readW(F_, IC(0)); readW(F_, IC(1)); });
  sfor<3, 12>([&](auto U) { readX(IC(0), U); });
  tag = (((u64)(unsigned)__builtin_amdgcn_readfirstlane(ept.y) << 32) | (unsigned)__builtin_amdgcn_readfirstlane(ept.x)) + 1ull;
  tag4 = v4u{(unsigned)tag, (unsigned)(tag >> 32), (unsigned)tag, (unsigned)(tag >> 32)};
  PIN(tag4);
  SB0();

  sfor2<0, 600>([&](auto M) {
    constexpr int m = M, f = m / 2, tt = m & 1, ks = fr_k(f), jt = fr_j(f), ch = st_ch(fr_s(f));
    constexpr int sb = (m + 16) / 40 - 1, mb = (m + 16) % 40;
    constexpr int tv = (m - 368) / 8, uv = (m - 368) % 8, jv = (tv + 4) % 5;
    constexpr int tq = (m - 504) / 24, uq = (m - 504) % 24, jq = (tq + 4) % 5;
    if constexpr (mb == 0 && sb >= 0 && sb <= 11) {
      constexpr int cntA[12] = {13, 13, 13, 9, 13, 7, 5, 6, 10, 14, 18, 6};
      constexpr int cntB[12] = {13, 13, 13, 9, 13, 7, 5, 6, 10, 14, 20, 5};
      constexpr int cnt = ISB ? cntB[sb] : cntA[sb];
      if constexpr (sb == 3) { if (unit < 320) wait_vm<cnt + 5>(); else wait_vm<cnt>(); }
      else if constexpr (ISB && (sb == 7 || sb == 8)) { if (w == 3) wait_vm<cnt + 5>(); else wait_vm<cnt>(); PIN(poll); }
      else wait_vm_p<cnt>(poll);
      BAR();
      SB0();
    }
    if constexpr (m % 8 == 0 && m > 0 && f + AHEAD <= 300) { __builtin_amdgcn_s_waitcnt(0xC07F | (8 << 8)); SB0(); }
    {
      f32x4& d = acc[ch][tt][jt];
      const v4u& wv = wf[f % RING];
      if constexpr (ch == 2 || (ch == 0 && ISB)) {
        if constexpr (ks != 0) mm_va(d, wv, xf[ks][tt]);
        else if constexpr (ISB) mm_va0(d, wv, xf[ks][tt]);
        else mm_vac(d, wv, xf[ks][tt], eps4);
      } else {
        if constexpr (ks != 0) mm_av(d, xf[ks][tt], wv);
        else if constexpr (ch == 0) mm_avc(d, xf[ks][tt], wv, eps4);
        else mm_av0(d, xf[ks][tt], wv);
      }
    }
    SB0();
    if constexpr (f + AHEAD < 300) readW(IC(f + AHEAD < 300 ? f + AHEAD : 0), IC(tt));
    if constexpr (((sb >= 1 && sb <= 3) || sb == 5) && mb < 12) readX(IC(sb == 5 ? 4 : sb), IC(mb));
    SB0();
    if constexpr (ch == 0 && m % 10 == 9) {
      constexpr int kg = 4 * st_kb(m / 40) + (m % 40) / 10;
      if constexpr (kg == 0) { mm_va0(gacc[0], gf2[0][0], xf[0][0]); mm_va0(gacc[1], gf2[0][0], xf[0][1]); }
      else { mm_va(gacc[0], gf2[(kg >> 2) & 1][kg & 3], xf[kg][0]); mm_va(gacc[1], gf2[(kg >> 2) & 1][kg & 3], xf[kg][1]); }
    }
    auto split_w = [&](auto S, auto P0, auto ST) {
      constexpr int p0 = P0, st = ST;
      if constexpr (mb >= p0 && (mb - p0) / st < 5) {
        if constexpr ((mb - p0) % st == 0) w_m0<S, (mb - p0) / st>(q);
        if constexpr ((mb - p0) % st == 1) w_go<S, (mb - p0) / st>(q);
      }
    };
    auto split_x = [&](auto XS, auto I0, auto N, auto P0, auto ST) {
      constexpr int p0 = P0, st = ST, xs = XS;
      if constexpr (mb >= p0 && (mb - p0) / st < N) {
        constexpr int i = I0 + (mb - p0) / st;
        if constexpr ((mb - p0) % st == 0) x_m0<xs, i>(q);
        if constexpr ((mb - p0) % st == 1) x_go<xs, i>(q);
      }
    };
    if constexpr (m >= X1M && m < X1M + 8) load_x<1, m - X1M>(q);
    if constexpr (m >= 19 && m < 24) load_w<2, m - 19>(q);
    if constexpr (sb == 0) { split_x(IC(2), IC(0), IC(8), IC(1), IC(3)); split_w(IC(3), IC(25), IC(3)); }
    if constexpr (sb == 1) { split_w(IC(4), IC(2), IC(4)); split_x(IC(3), IC(0), IC(7), IC(25), IC(2)); if constexpr (mb == 39) load_x<3, 7>(q); }
    if constexpr (sb == 2 || sb == 3) { split_w(IC(sb + 3), IC(2), IC(4)); split_x(IC(4), IC((sb - 2) * 4), IC(4), IC(25), IC(4)); }
    if constexpr (sb >= 4 && sb <= 6) split_w(IC(sb + 3), IC(2), IC(8));
    if constexpr (sb == 7) { split_w(IC(10), IC(2), IC(4)); split_w(IC(11), IC(21), IC(4)); }
    if constexpr (sb == 8 || sb == 9) split_w(IC(sb + 4), IC(2), IC(8));
    if constexpr (sb == 10) split_w(IC(14), IC(1), IC(3));
    if constexpr (m == TQM) touch(Wg, tw);
    if constexpr (m == PACKM) {
      if (unit < 320) {
        v4u pv[5];
        sfor<0, 5>([&](auto J) {
          const v2u lo = trread(apk + 8 * J), hi = trread(apk + 2560 + 8 * J);
          pv[J] = v4u{lo.x, lo.y, hi.x, hi.y};
        });
        sfor<0, 5>([&](auto J) { ST128(pv[J], pk_dst + J * 1024); });
      }
    }
    if constexpr (m == 185) {
      const unsigned x = lane < 8 ? 0u : ~0u;
      ST128((tag4 ^ v4u{x, x, x, x}), fl3);
    }
    if constexpr (m == 265) poll_async(poll, p.ws, poll0_off);
    if constexpr (m == 345) settle_flags(p.ws, poll0_off, tag, poll);
    if constexpr (m == 286) { PIN(gacc[0]); PIN(gacc[1]); }
    if constexpr (m == 483 || m == 487) {
      constexpr int t2 = (m - 483) / 4;
      gate[t2] = __builtin_amdgcn_rcpf(1.f + __builtin_amdgcn_exp2f(gacc[t2][0] * (ISB ? LOG2E : -LOG2E)));
      PIN(gate[t2]);
    }
    if constexpr (sb >= 8 && sb <= 9 && mb % 8 == 6 && mb < 32) ld_wo(IC((sb - 8) * 4 + mb / 8));
    if constexpr (m >= 445 && (m - 445) % 4 == 0 && 8 + (m - 445) / 4 < NWO1) ld_wo(IC(8 + (m - 445) / 4));

    if constexpr (!ISB) {
      if constexpr (m >= 252 && m < 387) {
        sfor<(m - 252) * 4 / 3, (m - 251) * 4 / 3>([&](auto Z) {
          constexpr int z = Z, kt = (z / 36 + 4) % 5, pr = z % 36 / 9, t2 = pr >> 1, h = pr & 1;
          unsigned o = 0;
          phi_op(IC(z % 9), acc[0][t2][kt], IC(h), o);
          if constexpr (z % 9 == 8) Kf[kt][2 * t2 + h] = o;
          if constexpr (z % 36 == 35) PIN(Kf[kt]);
        });
      }
      if constexpr (m >= 281 && m <= 389 && (m - 281) % 27 == 0) {
        constexpr int kt = ((m - 281) / 27 + 4) % 5;
        if constexpr (m == 281) mm_vv0(kvZ, Kf[kt], Ej[kt]); else mm_vv(kvZ, Kf[kt], Ej[kt]);
      }
      if constexpr (m >= 282 && m < 362 && (m - 282) % 5 == 0) {
        constexpr int z = (m - 282) / 5;
        unsigned o = (r >> 2) == (z >> 2) ? Kf[4][z & 3] : 0u;
        PIN(o);
        K4m[z >> 2][z & 3] = o;
      }
      LAS f32x4* red = (LAS f32x4*)aW;
      if constexpr (m >= 368 && tv < 5) {
        if constexpr (uv == 3 || uv == 4) {
          constexpr int t2 = uv - 3;
          unsigned lo = pk2(acc[1][t2][jv][0], acc[1][t2][jv][1]), hi = pk2(acc[1][t2][jv][2], acc[1][t2][jv][3]);
          PIN(lo); PIN(hi);
          Vf[jv][2 * t2] = lo; Vf[jv][2 * t2 + 1] = hi;
        }
        if constexpr (uv == 5) { PIN(Vf[jv]); if constexpr (tv > 0) PIN(K4m[jv & 3]); }
        if constexpr (tv == 0) { if constexpr (uv == 6) mm_vv0(kvX, Kf[4], Vf[4]); }
        else {
          if constexpr (uv < 2) {
            unsigned lo = (r >> 2) == jv ? Vf[4][2 * uv] : 0u, hi = (r >> 2) == jv ? Vf[4][2 * uv + 1] : 0u;
            PIN(lo); PIN(hi);
            V4m[2 * uv] = lo; V4m[2 * uv + 1] = hi;
          }
          if constexpr (uv == 2) { PIN(V4m); if constexpr (tv == 1) red[6 * 64] = kvX; else red[(jv - 1) * 64] = kvT; }
          if constexpr (uv == 4) { if constexpr (tv == 1) mm_vv0(kvW, Kf[jv], V4m); else mm_vv(kvW, Kf[jv], V4m); }
          if constexpr (uv == 6) mm_vv0(kvT, Kf[jv], Vf[jv]);
          if constexpr (uv == 7) { if constexpr (tv == 1) mm_vv0(kvU, K4m[jv & 3], Vf[jv]); else mm_vv(kvU, K4m[jv & 3], Vf[jv]); }
        }
      }
      if constexpr (m == 397) red[7 * 64] = kvZ;
      if constexpr (m == 409) red[5 * 64] = kvW;
      if constexpr (m == 412) red[3 * 64] = kvT;
      if constexpr (m == 415) red[4 * 64] = kvU;
      if constexpr (m == 423) asm volatile("s_waitcnt lgkmcnt(8)" ::: "memory");
      if constexpr (m >= 424 && m < 432) rd[m - 424] = *(LAS f32x4*)(aR + ((m - 424) >> 1) * 8192 + ((m - 424) & 1) * 1024);
      if constexpr (m >= 433 && m < 441) {
        sfor<0, 3>([&](auto E) {
          constexpr int z = 3 * (m - 433) + E, qq = z >> 2, e = z & 3;
          float s = rd[qq & 1][e] + rd[2 + qq][e]; PIN(s);
          rd[qq & 1][e] = s;
        });
      }
      if constexpr (m == 441 || m == 442) {
        unsigned lo = pk2(rd[m - 441][0], rd[m - 441][1]), hi = pk2(rd[m - 441][2], rd[m - 441][3]);
        PIN(lo); PIN(hi);
        pst[2 * (m - 441)] = lo; pst[2 * (m - 441) + 1] = hi;
      }
      if constexpr (m == 444) ST128(pst, st_off);
      if constexpr (m == 446) *aZ0 = v2u{0u, 0u};
      if constexpr (m >= 448 && m < 480 && m % 2 == 0) {
        constexpr int h = (m - 448) >> 3, k = ((m - 448) >> 1) & 3;
        const bool gh = g == h, rh = (r >> 2) == h;
        auto pick = [&](bool c, LAS v2u* a) { unsigned v = (unsigned)(uintptr_t)(c ? a : aZ0); PIN(v); return (LAS v2u*)(uintptr_t)v; };
        if constexpr (k == 0) aU[h] = pick(gh, kvs + 256);
        if constexpr (k == 1) aWt[h] = pick(rh, kvs + 320);
        if constexpr (k == 2) aX[h] = pick(rh && gh, kvs + 384);
        if constexpr (k == 3) aZ[h] = pick(gh, kvz + 4);
      }
      if constexpr (m == 494) { wait_vm<13>(); flag(W_F1 + ((cg * 16 + rt) * 4 + w) * 128); }
      if constexpr (m == GPOLL) poll = ld_flag(p.ws, poll1_off);
      if constexpr (m == GPOLL + 37) settle_flags(p.ws, poll1_off, tag, poll);
      if constexpr (m >= GPOLL + 38 && m < GPOLL + 54) gl[m - GPOLL - 38] = LD128S(lane16, W_PART + (hg * 16 + m - GPOLL - 38) * PART_STR + w * 1024);
      if constexpr (m >= 504 && uq >= 4 && uq < 22) {
        sfor<0, 2>([&](auto E) {
          constexpr int z = 2 * (uq - 4) + E, pr = z / 9, t2 = pr >> 1, h = pr & 1;
          unsigned o = 0;
          phi_op(IC(z % 9), acc[2][t2][jq], IC(h), o);
          if constexpr (z % 9 == 8) Pq[t2][jq][h] = o;
        });
      }
    } else {
      if constexpr (m >= 248 && m < 288 && (m - 248) % 8 >= 4) {
        constexpr int kt = ((m - 248) / 8 + 4) % 5, z = (m - 248) % 8 - 4, t2 = z >> 1, e = z & 1;
        unsigned o = pk2(acc[0][t2][kt][2 * e], acc[0][t2][kt][2 * e + 1]);
        PIN(o);
        kx[kt][2 * t2 + e] = o;
      }
      if constexpr (m == 302) { if (w == 3) sfor<0, 5>([&](auto J) { ST128(kx[J], st_off + J * 1024); }); }
      if constexpr (m >= 348 && m < 368 && (m - 348) % 4 == 0) *(LAS v4u*)(aW + (m - 348) / 4 * 1024) = kx[(m - 348) / 4];
      if constexpr (m >= 368 && tv < 5) {
        if constexpr (uv == 3 || uv == 4) {
          constexpr int t2 = uv - 3;
          unsigned lo = pk2(acc[1][t2][jv][0], acc[1][t2][jv][1]), hi = pk2(acc[1][t2][jv][2], acc[1][t2][jv][3]);
          PIN(lo); PIN(hi);
          vx5[jv][2 * t2] = lo; vx5[jv][2 * t2 + 1] = hi;
        }
        if constexpr (uv == 6) *(LAS v4u*)(aW + (5 + jv) * 1024) = vx5[jv];
      }
      if constexpr (m == 391) { if (w == 3) { ST128(vx5[4], st_off + 9 * 1024); ST128(vx5[0], st_off + 5 * 1024); ST128(vx5[1], st_off + 6 * 1024); } }
      if constexpr (m == 406) { if (w == 3) { ST128(vx5[2], st_off + 7 * 1024); ST128(vx5[3], st_off + 8 * 1024); } }
      if constexpr (m >= 410 && m < 420) { constexpr int j = (m - 410) >> 1; Vb[tt][j].z = vx5[j][2 * tt]; Vb[tt][j].w = vx5[j][2 * tt + 1]; }
      auto halo_in = [&](auto R) {
        if (w == 0) {
          if constexpr (R == 1) wait_vm_p<9>(poll);
          settle_flags(p.ws + R * 131072, poll1_off, tag, poll);
          dma_m0<L_SCR + R * 5120>(0u); NOPS(0);
          sfor<0, 4>([&](auto J) { dma_sc1<J * 1024>(halo_src + R * 5120, lane16); });
          dma_m0<L_SCR + R * 5120 + 4096>(0u); NOPS(0); dma_sc1<0>(halo_src + R * 5120 + 4096, lane16);
        }
      };
      if constexpr (m == 385) flag(W_F1 + ((cg * 16 + rt) * 4 + w) * 128);
      if constexpr (m == 421) poll_async(poll, p.ws, poll1_off);
      if constexpr (m == 466) { halo_in(IC(0)); flag(W_F1 + 131072 + ((cg * 16 + rt) * 4 + w) * 128); }
      if constexpr (m == 494) poll_async(poll, p.ws + 131072, poll1_off);
      if constexpr (m == 506) wait_vm<12>();
      if constexpr (m >= 506 && m < 511) kpv[m - 506] = *(LAS v4u*)(aR + (m - 506) * 1024);
      if constexpr (m == 530) halo_in(IC(1));
      if constexpr (m == 566) wait_vm_p<9>(poll);
      if constexpr (m >= 566 && m < 576) {
        constexpr int j = ((m - 566) / 2 + 4) % 5;
        const v2u v = *(LAS v2u*)(aR + (5 + j) * 1024 + tt * 8);
        Vb[tt][j].x = v.x; Vb[tt][j].y = v.y;
      }
      if constexpr (m == 511 || m == 513) {
        constexpr int t2 = (m - 511) / 2;
        q4[t2] = v2u{pk2(acc[2][t2][4][0], acc[2][t2][4][1]), pk2(acc[2][t2][4][2], acc[2][t2][4][3])};
        PIN(q4[t2]);
      }
      if constexpr (m >= 516 && m < 528 && m % 2 == 0) att_pre(IC(0), IC((m - 516) / 2));
      if constexpr (m >= 528) {
        constexpr int h = tq - 1;
        constexpr int z0[25] = {0, 0, 0, 0, 2, 5, 7, 9, 9, 9, 9, 13, 17, 21, 22, 23, 24, 28, 32, 35, 38, 41, 46, 52, 52};
        sfor<z0[uq], z0[uq + 1]>([&](auto Z) { att_op(IC(h), IC(Z < 9 ? Z : Z + 8)); });
        if constexpr (uq >= 7 && uq < 10) { att_pre(IC(h + 1), IC(2 * (uq - 7))); att_pre(IC(h + 1), IC(2 * (uq - 7) + 1)); }
        if constexpr (h == 2 && uq < 18) pv_op(IC(0), IC(uq + 6));
      }
    }
    SB0();
  });
  constexpr int WO_PRE = ISB ? 46 : 40;
  sfor<NWO1, WO_PRE>([&](auto Z) { ld_wo(Z); });
  NOPS(3);
  SB0();
  if constexpr (!ISB) {
    v4u A1 = {sel0, sel1, 0u, 0u}, A2 = {0u, 0u, sel0, sel1};
    PIN(A1); PIN(A2);
    SB0();
    f32x4 d1, d2;
    v4u Pf[2][4];
    sfor<0, 44>([&](auto Z) {
      constexpr int z = Z, pr = z / 9, t2 = pr >> 1, h = pr & 1;
      if constexpr (z < 36) {
        unsigned o = 0;
        phi_op(IC(z % 9), acc[2][t2][3], IC(h), o);
        if constexpr (z % 9 == 8) Pq[t2][3][h] = o;
      } else {
        constexpr int hh = (z - 36) >> 1, tt = (z - 36) & 1;
        Pf[tt][hh] = v4u{Pq[tt][hh].x, Pq[tt][hh].y, Pq[tt][4].x, Pq[tt][4].y}; PIN(Pf[tt][hh]);
      }
      if constexpr (z >= 12) {
        constexpr int i = (z - 12) >> 1;
        if constexpr (z == 12) mm_vv0(d1, A1, gl[0]); else if constexpr (z == 13) mm_vv0(d2, A2, gl[0]);
        else if constexpr (z & 1) mm_vv(d2, A2, gl[i]); else mm_vv(d1, A1, gl[i]);
      }
      SB0();
    });
    NOPS(7);
    SB0();
    kvw[0] = v2u{pk2(d1[0], d1[1]), pk2(d1[2], d1[3])};
    kvw[64] = v2u{pk2(d2[0], d2[1]), pk2(d2[2], d2[3])};
    wait_lgkm();
    BAR();
    v4u Nh[4], N4[4], Zh[4];
#pragma unroll
    for (int h = 0; h < 4; ++h) { const v2u lo = kvz[h], hi = *aZ[h]; Zh[h] = v4u{lo.x, lo.y, hi.x, hi.y}; SB0(); }
#pragma unroll
    for (int h = 0; h < 4; ++h) { const v2u lo = kvs[h * 64], hi = *aU[h]; Nh[h] = v4u{lo.x, lo.y, hi.x, hi.y}; SB0(); }
#pragma unroll
    for (int h = 0; h < 4; ++h) { const v2u lo = *aWt[h], hi = *aX[h]; N4[h] = v4u{lo.x, lo.y, hi.x, hi.y}; SB0(); }
    f32x4 nm[2][4], dn[2][4];
#pragma unroll
    for (int h = 0; h < 4; ++h)
#pragma unroll
      for (int tt = 0; tt < 2; ++tt) mm_vvc(dn[tt][h], Zh[h], Pf[tt][h], eps4);
    SB0();
#pragma unroll
    for (int h = 0; h < 4; ++h)
#pragma unroll
      for (int tt = 0; tt < 2; ++tt) {
        mm_vv0(nm[tt][h], Nh[h], Pf[tt][h]);
        inv[tt][h] = __builtin_amdgcn_rcpf(dn[tt][h][0]);
        PIN(inv[tt][h]); PIN(dn[tt][h]);
        SB0();
      }
#pragma unroll
    for (int h = 0; h < 4; ++h)
#pragma unroll
      for (int tt = 0; tt < 2; ++tt) {
        if (h == 0) mm_vv0(num4[tt], N4[h], Pf[tt][h]); else mm_vv(num4[tt], N4[h], Pf[tt][h]);
        inv[tt][h] *= gate[tt];
        PIN(inv[tt][h]);
        SB0();
      }
    poll = ld_flag(p.ws, foB);
    SB0();
#pragma unroll
    for (int hp = 0; hp < 2; ++hp)
#pragma unroll
      for (int tt = 0; tt < 2; ++tt) {
        const f32x4 oa = nm[tt][2 * hp] * inv[tt][2 * hp], ob = nm[tt][2 * hp + 1] * inv[tt][2 * hp + 1];
        F[tt][hp] = v4u{pk2(oa[0], oa[1]), pk2(oa[2], oa[3]), pk2(ob[0], ob[1]), pk2(ob[2], ob[3])};
        ST128(F[tt][hp], (tt ? o_st1 : o_st) + hp * 1024);
        SB0();
      }
  } else {
    sfor<0, 9>([&](auto Z) { att_op(IC(3), Z); });
    SB0();
    sfor<9, 17>([&](auto Z) { att_op(IC(1), Z); });
    PIN(biasC); PIN(biasP);
    sfor<60, 67>([&](auto Z) { att_op(IC(1), Z); });
    SB0();
    sfor<17, 32>([&](auto Z) { att_op(IC(3), Z); });
    sfor<0, 16>([&](auto Z) { fin_op(IC(1), Z); });
    ST128(F[0][0], o_st); ST128(F[1][0], o_st1);
    sfor<9, 17>([&](auto Z) { att_op(IC(2), Z); });
    sfor<60, 67>([&](auto Z) { att_op(IC(2), Z); });
    SB0();
    sfor<32, 60>([&](auto Z) { att_op(IC(3), Z); });
    sfor<0, 16>([&](auto Z) { fin_op(IC(2), Z); });
    sfor<9, 17>([&](auto Z) { att_op(IC(3), Z); });
    sfor<60, 67>([&](auto Z) { att_op(IC(3), Z); });
    NOPS(3);
    SB0();
    sfor<0, 16>([&](auto Z) { fin_op(IC(3), Z); });
    ST128(F[0][1], o_st + 1024); ST128(F[1][1], o_st1 + 1024);
    SB0();
  }
  {
    v2u F2[2];
#pragma unroll
    for (int tt = 0; tt < 2; ++tt) {
      const f32x4 o4 = num4[tt] * sel4(g, inv[tt][0], inv[tt][1], inv[tt][2], inv[tt][3]);
      F2[tt] = v2u{pk2(o4[0], o4[1]), pk2(o4[2], o4[3])};
    }
    const auto sx = __builtin_amdgcn_permlane16_swap(F2[0].x, F2[1].x, false, false);
    const auto sy = __builtin_amdgcn_permlane16_swap(F2[0].y, F2[1].y, false, false);
    ST128((v4u{sx[0], sy[0], sx[1], sy[1]}), o_st2);
  }
  SB0();
  auto load_o0 = [&](auto Z0, auto Z1) { sfor<Z0, Z1>([&](auto Z) { of0[Z / 5][Z % 5] = LD128S(oa.vo[Z % 5], oa.so[Z / 5]); SB0(); }); };
  if constexpr (ISB) {
    sfor<WO_PRE, 50>([&](auto Z) { ld_wo(Z); });
    SB0();
    wait_vm<50 - WO_PRE>();
    flag(W_FO + ((rt * 16 + cg) * 4 + w) * 128);
    if (b == 4) {
      const unsigned fa = W_FE + (w * 64 + lane) * 512;
      settle_flags(p.ws, fa, tag, ld_flag(p.ws, fa));
    }
    BAR();
    if (b == 4 && w == 0) flag(W_EPOCH);
    __builtin_amdgcn_s_sleep(BDLY);
    settle_flags(p.ws, foB, tag, ld_flag(p.ws, foB));
    load_o0(IC(0), IC(OPRE));
  } else {
    if (__all(poll == tag)) {
      __builtin_amdgcn_fence(__ATOMIC_ACQUIRE, "wavefront");
      load_o0(IC(0), IC(OPRE));
      wait_vm<OPRE>();
      flag(W_FO + ((rt * 16 + cg) * 4 + w) * 128);
    } else {
      poll = ld_flag(p.ws, foB);
      SB0();
      wait_vm<1>();
      flag(W_FO + ((rt * 16 + cg) * 4 + w) * 128);
      settle_flags(p.ws, foB, tag, poll);
      load_o0(IC(0), IC(OPRE));
    }
  }
  load_o0(IC(OPRE), IC(10));
  asm volatile("" ::"v"(tag4));
}

template <int CHK, bool ISB>
__device__ __forceinline__ void phase2(const Params& p, LAS char* L, __amdgpu_buffer_rsrc_t rs, int b, int w, int lane, int jt, int c, int rj, u64 tag,
                                       OAddr& oa, v4u (&of0)[4][5], v4u (&wo)[50]) {
  const int r = lane & 15, g = lane >> 4;
  v4u of1[4][5];
  f32x4 acc[4][5];
  unsigned lane16 = lane * 16;
  PIN(lane16);
  const unsigned foA = fo_of(jt, w, lane, 0);
  u64 pollA = 0;
  unsigned ra = (unsigned)(uintptr_t)L + lane16;
  unsigned rw[3];
#pragma unroll
  for (int a = 0; a < 3; ++a) { rw[a] = ra + ((((a + w + 1) & 3) * 3 + a) * 5) * 1024; PIN(rw[a]); }
  LAS f32x4* red = (LAS f32x4*)(uintptr_t)(ra + w * 15 * 1024);
  unsigned out_off = (64 * jt + 32 * (w >> 1) + 2 * r + (w & 1)) * 1280 + c * 160 + g * 40;
  PIN(out_off);
  f32x4 part[3][5];
  sfor2<0, 200>([&](auto M) {
    constexpr int m = M, hb = m / 100, a = (m % 100) / 25, i = (m % 25) / 5, n = m % 5;
    if constexpr (m == CHK) { settle_flags(p.ws, foA, tag, pollA); }
    if constexpr (hb == 0 && i == 0) mm_av0(acc[a][n], wo[n], of0[a][0]);
    else mm_av(acc[a][n], wo[hb * 25 + i * 5 + n], hb ? of1[a][i] : of0[a][i]);
    SB0();
    if constexpr (m < 20 && m % 2 == 0) of0[2 + m / 10][m / 2 % 5] = LD128S(oa.vo[m / 2 % 5], oa.so[2 + m / 10]);
    if constexpr (!ISB && m >= 26 && m < 56 && (m - 26) % 3 == 0) {
      constexpr int z = 40 + (m - 26) / 3;
      wo[wo_idx(z)] = LD128S(lane16 + ((z % 5) & 3) * 1024, W_WOP + (c * 40 + 5 * w) * 5120 + rot5(rj, wo_i(z)) * 5120 + ((z % 5) >> 2) * 4096);
    }
    if constexpr (m == CHK - 35) pollA = ld_flag(p.ws, foA);
    if constexpr (m >= CHK && m < CHK + 40 && (m - CHK) % 2 == 0) of1[(m - CHK) / 10][(m - CHK) / 2 % 5] = LD128S(oa.vo[(m - CHK) / 2 % 5], oa.so[(m - CHK) / 10] - 20480);
    if constexpr (m >= 130 && m < 175 && (m - 130) % 25 < 20 && (m - 130) % 25 % 4 == 0) {
      constexpr int a2 = (m - 130) / 25, n2 = (m - 130) % 25 / 4;
      *(LAS f32x4*)(uintptr_t)(rw[a2] + n2 * 1024) = acc[a2][n2];
    }
    if constexpr (m >= 178 && m < 183) *(LAS f32x4*)(uintptr_t)(rw[2] + (m - 178) * 1024) = acc[2][m - 178];
    if constexpr (m == 186) { wait_lgkm(); BAR(); }
    if constexpr (m >= 186 && m < 194) {
      sfor<2 * (m - 186), (2 * (m - 186) + 2 < 15 ? 2 * (m - 186) + 2 : 15)>([&](auto Z) { part[Z % 3][Z / 3] = red[((Z % 3) * 5 + Z / 3) * 64]; });
    }
    SB0();
  });
#pragma unroll
  for (int n = 0; n < 5; ++n) { part[0][n] = (part[0][n] + part[1][n]) + part[2][n]; PIN(part[0][n]); }
  SB0();
  unsigned o[10];
#pragma unroll
  for (int n = 0; n < 5; ++n) {
    const f32x4 s = part[0][n] + acc[3][n];
    o[2 * n] = pk2(s[0], s[1]);
    o[2 * n + 1] = pk2(s[2], s[3]);
  }
  GAS char* op = (GAS char*)p.out + out_off;
  typedef unsigned v4u8 __attribute__((ext_vector_type(4), aligned(8)));
  *(GAS v4u8*)op = v4u8{o[0], o[1], o[2], o[3]};
  *(GAS v4u8*)(op + 16) = v4u8{o[4], o[5], o[6], o[7]};
  *(GAS v2u*)(op + 32) = v2u{o[8], o[9]};
}

__global__ __launch_bounds__(256, 1) void fused_kernel(Params p) {
  extern __shared__ __attribute__((aligned(16))) char smem[];
  LAS char* L = (LAS char*)smem;
  const int b = blockIdx.x, lane = threadIdx.x & 63;
  const int w = __builtin_amdgcn_readfirstlane(threadIdx.x >> 6);
  const int xcd = b & 7, idx = b >> 3;
  const int rt = (xcd & 1) * 8 + (idx & 7), cg = (xcd >> 1) * 4 + (idx >> 3);
  const int jt = xcd * 4 + (idx & 3), c2 = idx >> 2;
  const int r = lane & 15, g = lane >> 4;
  {
    const char* pc; unsigned len;
    asm volatile("s_getpc_b64 %0\nfk_here:\n\ts_mov_b32 %1, fk_end-fk_here" : "=s"(pc), "=s"(len));
    pc = (const char*)((uintptr_t)pc & ~(uintptr_t)63);
    unsigned co = (unsigned)((idx * 4 + w) * 1024 + lane * 16);
    co = co + 16 <= len ? co : 0u;
    asm volatile("s_mov_b32 m0, %2\n\ts_nop 0\n\tglobal_load_lds_dwordx4 %0, %1" ::"v"(co), "s"(pc), "s"((unsigned)(L_AUX + 2048)) : "memory");
  }
  const __amdgpu_buffer_rsrc_t rs = __builtin_amdgcn_make_buffer_rsrc((void*)p.ws, 0, W_BYTES, 0x00020000);
  {
    const char* ab = sel4(w, p.gw, p.gw + 1024, (xcd & 1) ? p.woB : p.woA, (const char*)p.ws);
    const unsigned ao = (unsigned)((w == 1 ? r : lane) * 16);
    asm volatile("s_mov_b32 m0, %2\n\ts_nop 0\n\tglobal_load_lds_dwordx4 %0, %1 sc1" ::"v"(ao), "s"(ab), "s"((unsigned)(L_AUX + w * 1024)) : "memory");
  }
  Lane q;
  q.rot = (idx + (ROTX ? (xcd >> 1) : 0)) % 5;
  q.lw = (unsigned)(w * UNIT);
  q.lx = (unsigned)(w * 16384);
#pragma unroll
  for (int i = 0; i < 8; ++i) {
    const int e = i & 1, tt = (i >> 1) & 1, hh = i >> 2;
    q.xo[i] = (unsigned)((16 * e + 2 * (lane >> 3) + tt) * 1280 + hh * 128 + (((lane & 7) ^ (4 * e + g)) * 16) + 4096 - (i & 3) * 1024);
  }
  q.Wb = (cg < 8 ? p.wA : p.wB) + (cg & 7) * 160 + (size_t)w * 32 * 3840 - 4096;
  q.xb = p.x + (size_t)(rt * 128 + w * 32) * 1280 - 4096;
  sfor<0, 8>([&](auto I) { load_x<0, I>(q); });
#pragma unroll
  for (int i = 0; i < 5; ++i) {
    const int c = i * 64 + lane, rho = c / 10, cc = c % 10;
    q.woff[i] = (unsigned)((8 * ((rho >> 2) & 3) + 4 * (rho >> 4) + (rho & 3)) * 3840 + cc * 16 + 4096 - (i & 3) * 1024);
  }
  sfor<0, 2>([&](auto I) { load_w<0, I>(q); });
  sfor<2, 5>([&](auto I) { load_w<0, I>(q); });
  sfor<0, S1PRE>([&](auto I) { load_w<1, I>(q); });
  q.idx = idx; q.xcd = xcd;
  u64 tag = 0;
  v4u wo[50], of0[4][5];
  OAddr oa;
  const int rj = (idx & 1) * 2;
  if (cg < 8) {
    phase1<false>(p, q, L, rs, b, rt, cg, w, lane, c2, jt, rj, tag, oa, of0, wo);
    phase2<P2CHK, false>(p, L, rs, b, w, lane, jt, c2, rj, tag, oa, of0, wo);
  } else {
    phase1<true>(p, q, L, rs, b, rt, cg - 8, w, lane, c2, jt, rj, tag, oa, of0, wo);
    phase2<P2CHKB, true>(p, L, rs, b, w, lane, jt, c2, rj, tag, oa, of0, wo);
  }
  asm volatile("fk_end:");
}

extern "C" void kernel_launch(void* const* d_in, const int* in_sizes, int n_in, void* d_out, int out_size, void* d_ws, size_t ws_size,
                              hipStream_t stream) {
  Params p;
  p.x = (const char*)d_in[0];
  p.wA = (const char*)d_in[1];
  p.woA = (const char*)d_in[2];
  p.wB = (const char*)d_in[4];
  p.woB = (const char*)d_in[5];
  p.gw = (const char*)d_in[6];
  p.out = (char*)d_out;
  p.ws = (char*)d_ws;
  (void)hipFuncSetAttribute((const void*)fused_kernel, hipFuncAttributeMaxDynamicSharedMemorySize, LDS_BYTES);
  fused_kernel<<<256, 256, LDS_BYTES, stream>>>(p);
}
```
